```python
import torch

def _softmax(x, axis=-1):
    return torch.softmax(x, dim=axis)

def _gelu(x):
    return torch.nn.functional.gelu(x, approximate='tanh')

def _silu(x):
    return torch.nn.functional.silu(x)

def _sigmoid(x):
    return torch.sigmoid(x)

def _layernorm(x, axis=-1, eps=1e-05):
    m = x.mean(dim=axis, keepdim=True)
    v = x.var(dim=axis, keepdim=True, unbiased=False)
    return (x - m) / torch.sqrt(v + eps)

def _scatter_add(tgt, idx, src, axis=0):
    return tgt.clone().index_add_(axis, idx, src)

def _segment_sum(src, seg, n):
    out = torch.zeros((n,) + src.shape[1:], dtype=src.dtype)
    return out.index_add_(0, seg, src)

def _pad(x, pw):
    flat = tuple((v for pair in reversed(pw) for v in pair))
    return torch.nn.functional.pad(x, flat)
kernel_name = 'concat_heads__memory_aug_recurrent_mem_tr__sliding_local_dilated__prompt'

def setup_inputs():

    _w = lambda a: (lambda t: t.to(torch.bfloat16) if t.is_floating_point() else t)(torch.as_tensor(a))

    def _A_setup():
        B, S, Dm = (1, 2048, 1024)
        H, Dh = (32, 128)
        M, seg = (8, 32)
        seg = min(seg, S)
        S = S // seg * seg
        return {'x': _w((lambda *s: torch.randn(*s, dtype=torch.bfloat16) * 0.05)(B, S, Dm)), 'mem0': _w((lambda *s: torch.randn(*s, dtype=torch.bfloat16) * 1.6)(B, M, Dm)), 'Wq': _w((lambda *s: torch.randn(*s, dtype=torch.bfloat16) * 0.8)(Dm, H, Dh) * Dm ** (-0.5)), 'Wk': _w((lambda *s: torch.randn(*s, dtype=torch.bfloat16) * 0.8)(Dm, H, Dh) * Dm ** (-0.5)), 'Wv': _w((lambda *s: torch.randn(*s, dtype=torch.bfloat16) * 1.6)(Dm, H, Dh) * Dm ** (-0.5)), 'Wo': _w((lambda *s: torch.randn(*s, dtype=torch.bfloat16) * 1.6)(H, Dh, Dm) * (H * Dh) ** (-0.5)), 'seg_len': seg}
    _A_in = _A_setup()

    def _B_setup():
        B, S, D = (1, 2048, 1024)
        return {'x': _w((lambda *s: torch.randn(*s, dtype=torch.bfloat16) * 0.2)(B, S, D)), 'wqkv': _w((lambda *s: torch.randn(*s, dtype=torch.bfloat16) * 0.2)(D, 3 * D)), 'wo': _w((lambda *s: torch.randn(*s, dtype=torch.bfloat16) * 0.05)(D, D))}
    _B_in = _B_setup()
    return {'A_x': _A_in['x'], 'A_mem0': _A_in['mem0'], 'A_Wq': _A_in['Wq'], 'A_Wk': _A_in['Wk'], 'A_Wv': _A_in['Wv'], 'A_Wo': _A_in['Wo'], 'A_seg_len': _A_in['seg_len'], 'B_x': _B_in['x'], 'B_wqkv': _B_in['wqkv'], 'B_wo': _B_in['wo']}

def reference(A_x, A_mem0, A_Wq, A_Wk, A_Wv, A_Wo, A_seg_len, B_x, B_wqkv, B_wo):

    def _A_ref(x, mem0, Wq, Wk, Wv, Wo, seg_len):
        B, S, Dm = x.shape
        H, Dh = (Wq.shape[1], Wq.shape[2])
        M = mem0.shape[1]
        scale = Dh ** (-0.5)
        n_seg = S // seg_len
        L = M + seg_len
        idx = torch.arange(L)
        mask = (idx[:, None] < M) | (idx[:, None] >= idx[None, :])
        mem = mem0
        outs = []
        for s in range(n_seg):
            xs = x[:, s * seg_len:(s + 1) * seg_len, :].clone()
            h = torch.concatenate([mem, xs], axis=1)
            q = torch.einsum('btd,dhe->bhte', h, Wq)
            k = torch.einsum('btd,dhe->bhte', h, Wk)
            v = torch.einsum('btd,dhe->bhte', h, Wv)
            scores = torch.einsum('bhqe,bhke->bhqk', q, k) * scale
            scores = torch.where(mask[None, None], scores, -1000000000.0)
            p = _softmax(scores, axis=-1)
            o = torch.einsum('bhqk,bhke->bhqe', p, v)
            o = torch.einsum('bhte,hed->btd', o, Wo)
            mem = o[:, :M, :].clone()
            outs.append(o[:, M:, :])
        return torch.concatenate(outs, axis=1)

    def _B_ref(x, wqkv, wo):
        B, S, D = x.shape
        H = 32
        Dh = D // H
        W, dil = (8, 2)
        qkv = (lambda x, *d: x.permute(*d))((x @ wqkv).reshape(B, S, 3, H, Dh), 2, 0, 3, 1, 4)
        q, k, v = (qkv[0], qkv[1], qkv[2])
        scale = Dh ** (-0.5)
        pos = torch.arange(S)
        off = torch.arange(W) * dil
        idx = pos[:, None] - off[None, :]
        mask = idx >= 0
        idx_c = torch.where(mask, idx, 0).reshape(-1)
        kg = k[:, :, idx_c, :].reshape(B, H, S, W, Dh)
        vg = v[:, :, idx_c, :].reshape(B, H, S, W, Dh)
        logits = torch.einsum('bhsd,bhswd->bhsw', q, kg) * scale
        logits = torch.where(mask[None, None, :, :], logits, -1000000000.0)
        attn = _softmax(logits, axis=-1)
        o = torch.einsum('bhsw,bhswd->bhsd', attn, vg)
        return (lambda x, *d: x.permute(*d))(o, 0, 2, 1, 3).reshape(B, S, D) @ wo
    a = _A_ref(A_x, A_mem0, A_Wq, A_Wk, A_Wv, A_Wo, A_seg_len)
    b = _B_ref(B_x, B_wqkv, B_wo)
    return torch.concatenate([a, b], axis=-1)
```

```cpp
#include <hip/hip_runtime.h>
#include <hip/hip_bf16.h>
#include <stdint.h>
#include <stddef.h>

#define DQ 14
#define DQ2 12
#define DMV 126
#define DOV 78
#define GAIN 4
#define MINC 16
#define MDEC 4
#define RECOV 70
#define STAG 3
#define O2D 7
#define KVLATE 0
#define WD 2
#define NT 0
#define OUTAUX 0
#define ALQ 2
#define ALO 2
#define ALQ2 0
#define ALO2 5
#define DIRN 1
#define QTJ 2
#define PLIM 54
#define PSL2 4
#define PSL 6
#define STAG2 (8 + (16 << 8))
#define FLAT 110
#define FINE 3
#define OFFM 16.f
#define SH1 96
#define SH2 8
#define SH3 (10 + (16 << 8) + (8 << 16))
#define DVV 12
#define RANKM 20
#define TKS 5
#define DECIDER 1
#define VSLOT 17
typedef unsigned short u16;
typedef __attribute__((ext_vector_type(4))) float f32x4;
typedef unsigned v4u __attribute__((ext_vector_type(4)));

constexpr int S_ = 2048, DM = 1024, HA = 32, DHA = 128, HDA = 4096, MEM = 8, SEG = 32, NSEG = 64;
constexpr int NQKVA = 3 * HDA;
constexpr int LDO = 2 * HDA;
constexpr int DB = 1024, HB = 32, DHB = 32, NQKVB = 3 * DB;

#define PROBES 8
#define PNEAR 590u
#define SPROBES 6
constexpr int OREG = 262144, MREG = 65536, NPAGE = 84, NPROBER = 64;
struct Plc { unsigned long long e, x; };
__device__ __forceinline__ int nearo(const Plc pm, int off) { return off + (int)(((unsigned)(pm.e >> (off >> 22)) ^ ((unsigned)(pm.x >> (off >> 22)) & (off >> 21))) & 1u) * 8192; }
__device__ __forceinline__ int faro(const Plc pm, int off) { return nearo(pm, off) ^ 8192; }
__device__ __forceinline__ int sideo(const Plc pm, int off, int sd) { const int pg = off >> 21; return nearo(pm, off) + (int)(((unsigned)(pm.e >> (48 + (pg < 15 ? pg : 15))) ^ (off >> 19) ^ sd) & 1u) * 16384; }
__device__ __forceinline__ int hside(int h) { return (unsigned)(h - 12) >= 12u; }
constexpr int MB0 = 0, OB0 = NSEG * MREG, VB0 = OB0 + NSEG * OREG, RC0 = VB0 + NSEG * OREG  , CBASE = RC0 + 32768;
constexpr int QA0 = 20 << 21, RB0 = QA0 + (48 << 21), PT0 = RB0 + (16 << 21), ARENA = PT0;
typedef __attribute__((ext_vector_type(8))) __bf16 bfx8;
typedef __attribute__((ext_vector_type(2))) __bf16 bfx2;
typedef __attribute__((ext_vector_type(2))) float f32x2;
typedef unsigned v2u __attribute__((ext_vector_type(2)));
typedef unsigned v8u __attribute__((ext_vector_type(8)));
constexpr int NQK = 32, NVO = 64;
constexpr int NLW = 12, VXLD = 144;
constexpr int IMGW = 576, T0 = 16384, KIMG = 4 * IMGW, PIMG = 8 * IMGW, KB0 = 23104, KVB = 16512, SW0 = KB0 + 2 * KVB;
#define R8(c, n) #c #n "0", #c #n "1", #c #n "2", #c #n "3", #c #n "4", #c #n "5", #c #n "6", #c #n "7", #c #n "8", #c #n "9"
#define V162_225 "v162", "v163", "v164", "v165", "v166", "v167", "v168", "v169", R8(v, 17), R8(v, 18), R8(v, 19), R8(v, 20), R8(v, 21), "v220", "v221", "v222", "v223", "v224", "v225"
typedef unsigned v16u __attribute__((ext_vector_type(16)));
typedef unsigned v32u __attribute__((ext_vector_type(32)));

__device__ __forceinline__ void lds_barrier() { asm volatile("s_waitcnt lgkmcnt(0)" ::: "memory"); __builtin_amdgcn_s_barrier(); asm volatile("" ::: "memory"); }
template <int N> __device__ __forceinline__ float dpp_ror(float v) {
  return __int_as_float(__builtin_amdgcn_update_dpp(0, __float_as_int(v), 0x120 + N, 0xf, 0xf, false));
}
__device__ __forceinline__ unsigned pk2(float a, float b) { f32x2 t = {a, b}; return __builtin_bit_cast(unsigned, __builtin_convertvector(t, bfx2)); }
__device__ __forceinline__ unsigned hi2(float a, float b) { return __builtin_amdgcn_perm(__float_as_uint(b), __float_as_uint(a), 0x07060302u); }
__device__ __forceinline__ unsigned nosent(unsigned x) { return x < 0xfffffffeu ? x : 0xfffffffeu; }
__device__ __forceinline__ unsigned umax3(unsigned a, unsigned b, unsigned c) { unsigned m = a > b ? a : b; return m > c ? m : c; }
__device__ __forceinline__ unsigned chk4(unsigned m, v4u v) { return umax3(umax3(m, v.x, v.y), v.z, v.w); }
__device__ __forceinline__ unsigned long long now() { return __builtin_amdgcn_s_memtime(); }
__device__ __forceinline__ f32x4 mf(v4u a, v4u b, f32x4 c) {
  return __builtin_amdgcn_mfma_f32_16x16x32_bf16(__builtin_bit_cast(bfx8, a), __builtin_bit_cast(bfx8, b), c, 0, 0, 0);
}
__device__ __forceinline__ v2u trrd(const u16* p) {
  typedef __attribute__((ext_vector_type(4))) short v4i16_t;
  return __builtin_bit_cast(v2u, __builtin_amdgcn_ds_read_tr16_b64_v4i16((__attribute__((address_space(3))) v4i16_t*)p));
}
__device__ __forceinline__ v8u cat8(v4u a, v4u b) { return __builtin_shufflevector(a, b, 0, 1, 2, 3, 4, 5, 6, 7); }
__device__ __forceinline__ float xmax16(float x) { auto r = __builtin_amdgcn_permlane16_swap(__float_as_uint(x), __float_as_uint(x), false, false); return fmaxf(__uint_as_float(r[0]), __uint_as_float(r[1])); }
__device__ __forceinline__ float xmax32(float x) { auto r = __builtin_amdgcn_permlane32_swap(__float_as_uint(x), __float_as_uint(x), false, false); return fmaxf(__uint_as_float(r[0]), __uint_as_float(r[1])); }
__device__ __forceinline__ float xsum16(float x) { auto r = __builtin_amdgcn_permlane16_swap(__float_as_uint(x), __float_as_uint(x), false, false); return __uint_as_float(r[0]) + __uint_as_float(r[1]); }
__device__ __forceinline__ float xsum32(float x) { auto r = __builtin_amdgcn_permlane32_swap(__float_as_uint(x), __float_as_uint(x), false, false); return __uint_as_float(r[0]) + __uint_as_float(r[1]); }
__device__ __forceinline__ int dbl(int off) { return ((off >> 13) << 14) | (off & 8191); }
__device__ __forceinline__ int quad(int off) { return ((off >> 13) << 15) | (off & 8191); }
__device__ __forceinline__ int tr4(int p) { return (p & 3) * 4 + (p >> 2); }
__device__ __forceinline__ int mpos(int x, int j) { return (x == 0 ? 4 - j : 5 * x + j) & 15; }
__device__ __forceinline__ int mchunk(int x, int j) { return tr4(mpos(x, j)); }
__device__ __forceinline__ bool spin_fail(unsigned spins, unsigned* tmo, unsigned code) {
  if ((spins & 31u) != 31u) return false;
  if (spins > (1u << 15)) { __hip_atomic_store(tmo, code, __ATOMIC_RELAXED, __HIP_MEMORY_SCOPE_AGENT); return true; }
  return __builtin_amdgcn_readfirstlane(__hip_atomic_load(tmo, __ATOMIC_RELAXED, __HIP_MEMORY_SCOPE_AGENT)) != 0u;
}
#define SB0 __builtin_amdgcn_sched_barrier(0)
__device__ __forceinline__ int opaque_tid() { int t = threadIdx.x; asm volatile("" : "+v"(t)); return t; }

#define PACEQ asm volatile("s_nop 7\n\ts_nop 3")
#define PACE asm volatile("s_nop 7\n\ts_nop 3")

template <class T> __device__ __forceinline__ T* uni(T* p) {
  const unsigned long long v = (unsigned long long)p;
  return (T*)(((unsigned long long)(unsigned)__builtin_amdgcn_readfirstlane((unsigned)(v >> 32)) << 32) | (unsigned)__builtin_amdgcn_readfirstlane((unsigned)v));
}
namespace wg {
constexpr int A_B = 128 * 128, STAGE_B = 384 * 128;
#define LATE_SIGNAL do { if (tid < pn) __hip_atomic_fetch_add(psig + 64 * tid, 1u, __ATOMIC_RELAXED, __HIP_MEMORY_SCOPE_AGENT); } while (0)
#define EARLY_PICK unsigned tn = 0; if (qh && tid == 0) tn = __hip_atomic_fetch_add(qh, 1u, __ATOMIC_RELAXED, __HIP_MEMORY_SCOPE_AGENT); SB0
#define END_TILE do { SB0; if (tid == 0) *(unsigned*)(lds + 3 * STAGE_B) = tn; lds_barrier(); } while (0)
constexpr int A2_B = 256 * 128, B2_B = 32 * 512, B2_0 = 3 * A2_B;
__device__ __forceinline__ void gemm_big(const u16* A, int lda, const u16* B, int ldb, void* C, int ldc, int cbs, int adel, int cdel  , bool frag, char* lds, unsigned* psig, int pn, unsigned* qh) {
  const int tid = opaque_tid(), lane = tid & 63, w = __builtin_amdgcn_readfirstlane(tid >> 6), wm = w >> 1, wn = w & 1;
  const int l15 = lane & 15, lq = lane >> 4;
  constexpr int nk = 16;
  A = uni(A); B = uni(B); C = uni(C);
  lda = __builtin_amdgcn_readfirstlane(lda); ldb = __builtin_amdgcn_readfirstlane(ldb); ldc = __builtin_amdgcn_readfirstlane(ldc); cbs = __builtin_amdgcn_readfirstlane(cbs);
  const __amdgpu_buffer_rsrc_t rsa = __builtin_amdgcn_make_buffer_rsrc((void*)A, 0, (255 * lda + nk * 64) * 2 + __builtin_amdgcn_readfirstlane(adel), 0x00020000);
  const __amdgpu_buffer_rsrc_t rsb = __builtin_amdgcn_make_buffer_rsrc((void*)B, 0, ((nk * 64 - 1) * ldb + 256) * 2, 0x00020000);
  int va[2], vb[2];
  { const int c0 = (lane & 7) ^ (lane >> 4), cb0 = (lane & 31) ^ ((lane >> 5) << 1) ^ ((w & 1) << 3);
#pragma unroll
    for (int i = 0; i < 2; ++i) { va[i] = ((64 * w + (lane >> 3)) * lda + (c0 ^ (4 * i)) * 8) * 2 + wm * adel; vb[i] = ((8 * w + (lane >> 5)) * ldb + (cb0 ^ (4 * i)) * 8) * 2; } }
  const int sa = 16 * lda, sb = 4 * ldb, skb = 64 * ldb;
  typedef __attribute__((address_space(3))) void* ldsp_t;
#define PA(q, ka, la) __builtin_amdgcn_raw_ptr_buffer_load_lds(rsa, (ldsp_t)(lds + (la) + (64 * w + 8 * (q)) * 128), 16, va[(q) & 1], (ka) + (q) * sa, 0, 0)
#define PB(q, kb, lb) __builtin_amdgcn_raw_ptr_buffer_load_lds(rsb, (ldsp_t)(lds + B2_0 + (lb) + (8 * w + 2 * (q)) * 512), 16, vb[(q) & 1], (kb) + (q) * sb, 0, 0)
#pragma unroll
  for (int q = 0; q < 8; ++q) PA(q, 0, 0);
#pragma unroll
  for (int g = 0; g < 4; ++g) {
#pragma unroll
    for (int q = 0; q < 4; ++q) PB(q, g * skb, g * B2_B);
    if (g > 0)
#pragma unroll
      for (int q = 0; q < 4; ++q) PA(4 * ((g - 1) & 1) + q, (1 + (g - 1) / 2) * 128, (1 + (g - 1) / 2) * A2_B);
  }
  f32x4 acc[8][8];
#pragma unroll
  for (int j = 0; j < 8; ++j) {
#pragma unroll
    for (int i = 0; i < 8; ++i) acc[i][j] = (f32x4){0.f, 0.f, 0.f, 0.f};
    asm volatile("" : "+a"(acc[0][j]), "+a"(acc[1][j]), "+a"(acc[2][j]), "+a"(acc[3][j]), "+a"(acc[4][j]), "+a"(acc[5][j]), "+a"(acc[6][j]), "+a"(acc[7][j]));
  }
  v4u fa[2][8]; v2u bl[2][8], bh[2][8]; unsigned ra[2], rb[8];
  { const unsigned lds0 = (unsigned)(size_t)lds; const int sw = (l15 >> 1) & 7, hh = (l15 >> 2) | ((lq & 1) << 2);
    ra[0] = lds0 + (128 * wm + l15) * 128 + ((lq ^ sw) << 4); ra[1] = lds0 + (128 * wm + l15) * 128 + (((4 + lq) ^ sw) << 4);
#pragma unroll
    for (int i = 0; i < 8; ++i) rb[i] = lds0 + B2_0 + (8 * lq + (l15 >> 2)) * 512 + ((((16 * wn + 2 * i) ^ (2 * hh)) + ((l15 >> 1) & 1)) << 4) + (l15 & 1) * 8; }
#define RDB(ks, i) asm volatile("ds_read_b64_tr_b16 %0, %2\n\tds_read_b64_tr_b16 %1, %2 offset:2048" : "=&v"(bl[ks][i]), "=&v"(bh[ks][i]) : "v"(rb[i]) : "memory")
#define RDA(ks, j) asm volatile("ds_read_b128 %0, %1 offset:%2" : "=&v"(fa[ks][j]) : "v"(ra[ks]), "i"((j) * 16 * 128) : "memory")
#define RDF(ks, r) do { if ((r) == 0) RDA(ks, 0); else if ((r) < 9) RDB(ks, (r) - 1); else RDA(ks, (r) - 8); } while (0)
#define WAITF(ks, txt) asm volatile(txt : "+v"(bl[ks][0]), "+v"(bh[ks][0]), "+v"(bl[ks][1]), "+v"(bh[ks][1]), "+v"(bl[ks][2]), "+v"(bh[ks][2]), "+v"(bl[ks][3]), "+v"(bh[ks][3]), \
    "+v"(bl[ks][4]), "+v"(bh[ks][4]), "+v"(bl[ks][5]), "+v"(bh[ks][5]), "+v"(bl[ks][6]), "+v"(bh[ks][6]), "+v"(bl[ks][7]), "+v"(bh[ks][7]), \
    "+v"(fa[ks][0]), "+v"(fa[ks][1]), "+v"(fa[ks][2]), "+v"(fa[ks][3]), "+v"(fa[ks][4]), "+v"(fa[ks][5]), "+v"(fa[ks][6]), "+v"(fa[ks][7]) :: "memory")
  asm volatile("s_waitcnt vmcnt(24)\n\ts_barrier" ::: "memory");
  LATE_SIGNAL;
#pragma unroll
  for (int r = 0; r < 16; ++r) RDF(0, r);
#pragma unroll
  for (int i = 0; i < 8; ++i) rb[i] += B2_B;
  int la = 2 * A2_B, ka = 2 * 128, lb = 0, kb = 4 * skb, lra = 0, da = 0, db = 0;
  WAITF(0, "s_waitcnt lgkmcnt(0) vmcnt(16)\n\ts_barrier");
  int kt = nk;
  do {
#pragma unroll
    for (int ks = 0; ks < 2; ++ks) {
      SB0;
#pragma unroll
      for (int u = 0; u < 64; ++u) {
        const int j = u >> 3, i = u & 7;
        asm volatile("v_mfma_f32_16x16x32_bf16 %0, %1, %2, %0" : "+a"(acc[i][j]) : "v"((v4u){bl[ks][i].x, bl[ks][i].y, bh[ks][i].x, bh[ks][i].y}), "v"(fa[ks][j]));
        SB0;
        if (u % 3 == 0 && u < 48) { RDF(ks ^ 1, u / 3); SB0; }
        if (u % 6 == 1 && u < 48) {
          asm volatile("" : "+s"(la), "+s"(ka), "+s"(lb), "+s"(kb));
          if ((u / 6) & 1) PA((ks ^ 1) * 4 + (u / 12), ka, la); else PB(u / 12, kb, lb);
          SB0;
        }
        if (u == 48) {
          asm volatile("" : "+s"(lb), "+s"(kb)); lb = (lb + B2_B) & (4 * B2_B - 1); db = lb == 3 * B2_B ? -3 * B2_B : B2_B; kb += skb; asm volatile("" : "+s"(lb), "+s"(kb), "+s"(db)); SB0; }
        if (u > 48 && u <= 56) { rb[u - 49] += db; asm volatile("" : "+v"(rb[u - 49])); SB0; }
        if (ks == 0 && u == 57) { asm volatile("" : "+s"(lra), "+s"(ka)); la = lra; da = lra == 2 * A2_B ? -2 * A2_B : A2_B; lra += da; ka += 128; asm volatile("" : "+s"(lra), "+s"(la), "+s"(da), "+s"(ka)); SB0; }
        if (ks == 0 && (u == 58 || u == 59)) { ra[u - 58] += da; asm volatile("" : "+v"(ra[u - 58])); SB0; }
        if (u == 60) { db = kt == 3 ? 0x7f000000 : 0; asm volatile("" : "+s"(db)); SB0; }
        if (ks == 0 && (u == 61 || u == 62)) { va[u - 61] |= db; asm volatile("" : "+v"(va[u - 61])); SB0; }
        if (ks == 1 && (u == 61 || u == 62)) { vb[u - 61] |= db; asm volatile("" : "+v"(vb[u - 61])); SB0; }
      }
      if (ks == 0) WAITF(1, "s_waitcnt lgkmcnt(0) vmcnt(16)\n\ts_barrier"); else WAITF(0, "s_waitcnt lgkmcnt(0) vmcnt(16)\n\ts_barrier");
    }
  } while (--kt > 0);
  asm volatile("s_nop 7" : "+a"(acc[4][7]), "+a"(acc[5][7]), "+a"(acc[6][7]), "+a"(acc[7][7]));
  SB0;
#undef PA
#undef PB
#undef RDB
#undef RDA
#undef RDF
#undef WAITF
  asm volatile("s_waitcnt vmcnt(0)" ::: "memory");
  __builtin_amdgcn_s_barrier();
  asm volatile("" ::: "memory");
  EARLY_PICK;
  char* st = lds + w * 32768;
  {
    const __amdgpu_buffer_rsrc_t rs = __builtin_amdgcn_make_buffer_rsrc(C, 0, ((ldc == DHA ? 479 : 255) * ldc + cbs + 128) * 2 + __builtin_amdgcn_readfirstlane(cdel), 0x00020000);
    const int rs2 = ldc == DHA ? 16384 : 64 * ldc;
    const int rd = frag ? l15 * 256 + ((lq ^ l15) << 4) : lq * 256 + ((l15 ^ lq) << 4);
    const int go = wm * (4 * rs2 + cdel) + wn * cbs * 2 + (frag ? lane * 16 : (lq * ldc + l15 * 8) * 2);
    const int rstep = frag ? 0 : 1024, gstep = frag ? 1024 : 8 * ldc;
    v4u v[4];
#pragma unroll
    for (int j = 0; j < 10; ++j) {
      if (j >= 2)
#pragma unroll
        for (int q = 0; q < 4; ++q) __builtin_amdgcn_raw_buffer_store_b128(v[q], rs, go, ((j - 2) >> 1) * rs2 + ((j - 2) & 1) * 32 * ldc + q * gstep, 16);
      SB0;
      if (j >= 1 && j < 9)
#pragma unroll
        for (int q = 0; q < 4; ++q) v[q] = *(const v4u*)(st + (j - 1) * 4096 + ((rd + q * rstep) ^ (q * 64)));
      SB0;
      if (j < 8) {
        asm volatile("" : "+a"(acc[0][j]), "+a"(acc[1][j]), "+a"(acc[2][j]), "+a"(acc[3][j]), "+a"(acc[4][j]), "+a"(acc[5][j]), "+a"(acc[6][j]), "+a"(acc[7][j]));
#pragma unroll
        for (int i = 0; i < 8; ++i)
          *(v2u*)(st + (j * 16 + l15) * 256 + (((2 * i + (lq >> 1)) ^ l15) << 4) + (lq & 1) * 8) = (v2u){pk2(acc[i][j][0], acc[i][j][1]), pk2(acc[i][j][2], acc[i][j][3])};
      }
      SB0;
    }
  }
  END_TILE;
}
}

struct Hdr {
  unsigned long long epoch[16], token[16];
  unsigned long long flag0[64];
  unsigned long long vote[NPROBER];
  unsigned long long fill[NPROBER];
};
struct Ctl {
  unsigned qhead[8][32];
  unsigned prog[32][32];
  unsigned cntB[16][32];
  unsigned cntAB[16][32];
  unsigned tticket[64][4][16];
  unsigned flagP[8][64][4];
  unsigned pseg[32][16];
  unsigned tmo[32];
  unsigned done[32];
};
constexpr int HBASE = CBASE + (((int)sizeof(Ctl) + 255) & ~255);
struct FParams {
  const u16 *Ax, *Bx, *Wq, *Wk, *Wv, *Wo, *wqkvB, *woB;
  u16 *obuf, *out;
  Ctl* ctl; unsigned long long* prof; Hdr* hdr; const u16* mem0;
  int dq, dq2, dmv, dov, gain, inc, dec, rec, stag, sh1, sh2, sh3, dv, pad, stag2, pad2;
};
#define RLX_AGENT __ATOMIC_RELAXED, __HIP_MEMORY_SCOPE_AGENT
__device__ __forceinline__ unsigned ld_agent(const unsigned* p) { return __hip_atomic_load(p, RLX_AGENT); }
__device__ __forceinline__ unsigned long long ld_agent(const unsigned long long* p) { return __hip_atomic_load(p, RLX_AGENT); }
__device__ __forceinline__ unsigned long long uni64(unsigned long long v) { return (unsigned long long)uni((char*)v); }
__device__ __forceinline__ void wait_token(const FParams& F, unsigned long long tok) {
  for (unsigned spins = 0; uni64(ld_agent(&F.hdr->token[0])) != tok && spins < (1u << 16); ++spins) __builtin_amdgcn_s_sleep(8);
  asm volatile("" ::: "memory");
}
__device__ __forceinline__ void leave_epoch(const FParams& F) {
  __syncthreads();
  if (threadIdx.x == 0 && __hip_atomic_fetch_add(&F.ctl->done[0], 1u, RLX_AGENT) == 255u) __hip_atomic_fetch_add(&F.hdr->epoch[0], 1ull, RLX_AGENT);
}
__device__ __forceinline__ Plc count_votes(unsigned long long v) {
  const unsigned long long mk = (1ull << (NPAGE / 2)) - 1, e = __ballot(v & 1) & mk, o = __ballot(v & 2) & mk;
  const int n = __builtin_popcountll(e) + __builtin_popcountll(~o & mk);
  const unsigned m = __ballot(v & 1) >> 48, l = (m << 1) | (m & 1), r = (m >> 1) | (m & 0x8000), c = __builtin_popcount(m);
  const unsigned long long sd = (unsigned long long)(c >= 13 ? 0xffffu : (c <= 3 ? 0u : (m & l) | (m & r) | (l & r))) << 48;
  return n >= NPAGE - 12 ? Plc{mk | sd, ~0ull} : (n <= 12 ? Plc{sd, ~0ull} : Plc{e | sd, e ^ o});
}
__device__ __forceinline__ Plc wait_votes(const FParams& F, unsigned long long tok, int lane, bool all) {
  unsigned long long v;
  for (unsigned spins = 0; spins < (1u << 15); ++spins) {
    v = ld_agent(&F.hdr->vote[lane]);
    const unsigned long long f = all ? ld_agent(&F.hdr->fill[lane]) : tok;
    if (__all((v | 3) == 4 * tok + 3 && f == tok)) break;
    __builtin_amdgcn_s_sleep(32);
  }
  asm volatile("" ::: "memory");
  return count_votes(v);
}
__device__ __forceinline__ u16* wsp(const FParams& F, int off) { return (u16*)((char*)F.obuf + off); }
__device__ __forceinline__ void wg_wait(const unsigned* p, unsigned need, Ctl* ctl, bool acq) {
  if (threadIdx.x < 64) {
    for (unsigned spins = 0;; ++spins) {
      const bool ok = (p == nullptr) || ld_agent(p) >= need;
      if (__all(ok)) break;
      if (spin_fail(spins, &ctl->tmo[0], 2u)) break;
      __builtin_amdgcn_s_sleep(16);
    }
    if (acq) __builtin_amdgcn_fence(__ATOMIC_ACQUIRE, "agent");
    asm volatile("s_waitcnt vmcnt(0)" ::: "memory");
  }
  __syncthreads();
}
__device__ __forceinline__ void wg_signal(unsigned* p) { if (threadIdx.x == 0) __hip_atomic_fetch_add(p, 1u, RLX_AGENT); }

struct AttnX { v4u q[2][4], kx[2][4], vr[8]; };
template <int PART = 3> __device__ __forceinline__ void attnA_load_x(const FParams& F, const Plc pm, int seg, int h, int lane, AttnX& X) {
  const int o0 = QA0 + (3 * h << 20);
  const __amdgpu_buffer_rsrc_t rs = __builtin_amdgcn_make_buffer_rsrc(wsp(F, o0 + seg * 16384), 0, (2 << 20) + 16384, 0x00020000);
  const int vo = lane * 16, oq = faro(pm, o0) - o0, ok = faro(pm, o0 + (1 << 20)) - o0, ov = faro(pm, o0 + (2 << 20)) - o0;
  if (PART & 1)
#pragma unroll
  for (int i = 0; i < 8; ++i) X.vr[i] = __builtin_amdgcn_raw_buffer_load_b128(rs, vo + (i & 3) * 1024, ov + (i >> 2) * 4096, 16);
  if (PART & 2)
#pragma unroll
  for (int t = 0; t < 2; ++t)
#pragma unroll
    for (int j = 0; j < 4; ++j) {
      X.q[t][j] = __builtin_amdgcn_raw_buffer_load_b128(rs, vo + 1024 * j, oq + 4096 * t, 16);
      X.kx[t][j] = __builtin_amdgcn_raw_buffer_load_b128(rs, vo + 1024 * j, ok + 4096 * t, 16);
    }
}
__device__ __forceinline__ void attnA_stage_v(const AttnX& X, u16* vs  , int lane) {
#pragma unroll
  for (int i = 0; i < 8; ++i) *(v4u*)&vs[(16 * (i >> 2) + (lane & 15)) * VXLD + 32 * (i & 3) + 8 * (lane >> 4)] = X.vr[i];
}
__device__ __forceinline__ unsigned chk_m(const v4u (&km)[4], const unsigned (&vm)[8]) {
  unsigned m = 0;
#pragma unroll
  for (int j = 0; j < 4; ++j) m = chk4(m, km[j]);
#pragma unroll
  for (int t = 0; t < 8; t += 2) m = umax3(m, vm[t], vm[t + 1]);
  return m;
}
__device__ __forceinline__ void attnA_load_m(const FParams& F, const Plc pm, int seg, int h, int lane, v4u (&km)[4], unsigned (&vm)[8]) {
  const int l15 = lane & 15, lq = lane >> 4;
  const __amdgpu_buffer_rsrc_t rk = __builtin_amdgcn_make_buffer_rsrc((char*)F.obuf + sideo(pm, VB0 + seg * OREG, hside(h) ^ 1) + quad(h * 2048), 0, 2048, 0x00020000);
  const __amdgpu_buffer_rsrc_t rv = __builtin_amdgcn_make_buffer_rsrc((char*)F.obuf + sideo(pm, VB0 + seg * OREG, hside(h)) + quad(h * 2048), 0, 2048, 0x00020000);
#pragma unroll
  for (int j = 0; j < 4; ++j) km[j] = __builtin_amdgcn_raw_buffer_load_b128(rk, (lq & 1) * 256 + ((l15 & 13) + 2 * (lq >> 1)) * 16 + 512 * j, 0, 16);
#pragma unroll
  for (int t = 0; t < 8; ++t) vm[t] = __builtin_amdgcn_raw_buffer_load_b32(rv, (l15 >> 3) * 256 + lq * 64 + ((l15 & 4) * 2 + (l15 & 3)) * 4 + (t >> 1) * 512 + (t & 1) * 16, 0, 16);
}
__device__ __forceinline__ void attnA_compute(const AttnX& X, const v4u (&km)[4], const unsigned (&vm)[8], const u16* vs, int lane, v4u (&ov)[2][4]) {
  const int l15 = lane & 15, lq = lane >> 4;
  const float scale = 0.08838834764831845f, L2E = 1.4426950408889634f;
  v4u px[2], pm[2];
#pragma unroll
  for (int t = 0; t < 2; ++t) {
    f32x4 s0 = {0.f,0.f,0.f,0.f}, s1 = s0, s2 = s0;
#pragma unroll
    for (int j = 0; j < 4; ++j) { s0 = mf(km[j], X.q[t][j], s0); s1 = mf(X.kx[0][j], X.q[t][j], s1); if (t == 1) s2 = mf(X.kx[1][j], X.q[t][j], s2); }
    float sv[12];
#pragma unroll
    for (int e = 0; e < 4; ++e) { sv[e] = s0[e]; sv[4 + e] = s1[e]; sv[8 + e] = s2[e]; }
#pragma unroll
    for (int e = 0; e < 12; e += 2) {
      unsigned p = pk2(sv[e], sv[e + 1]);
      p = pk2(__uint_as_float(p << 16) * scale, __uint_as_float(p & 0xffff0000u) * scale);
      sv[e] = __uint_as_float(p << 16); sv[e + 1] = __uint_as_float(p & 0xffff0000u);
    }
    bool ok[12];
#pragma unroll
    for (int e = 0; e < 4; ++e) { ok[e] = e < 2; ok[4 + e] = (t == 1) || (4 * lq + e <= l15); ok[8 + e] = (t == 1) && (4 * lq + e <= l15); }
    float mx = -INFINITY;
#pragma unroll
    for (int e = 0; e < 12; ++e) mx = ok[e] ? fmaxf(mx, sv[e]) : mx;
    mx = xmax32(xmax16(mx));
    float sum = 0.f;
#pragma unroll
    for (int e = 0; e < 12; ++e) { sv[e] = ok[e] ? __builtin_amdgcn_exp2f((sv[e] - mx) * L2E) : 0.f; sum += sv[e]; }
    sum = xsum32(xsum16(sum));
    const float inv = __builtin_amdgcn_rcpf(sum);
#pragma unroll
    for (int e = 0; e < 12; ++e) sv[e] = ok[e] ? sv[e] * inv : 0.f;
    px[t] = (v4u){pk2(sv[4], sv[5]), pk2(sv[6], sv[7]), pk2(sv[8], sv[9]), pk2(sv[10], sv[11])};
    pm[t] = (v4u){pk2(sv[0], sv[1]), 0u, 0u, 0u};
  }
  const u16* vp = &vs[(4 * lq + (l15 >> 2)) * VXLD + 8 * (l15 & 3)];
#pragma unroll
  for (int u = 0; u < 4; ++u) {
    f32x4 o[2][2];
#pragma unroll
    for (int ab = 0; ab < 2; ++ab) {
      v2u lo = trrd(vp + 32 * u + 4 * ab), hi = trrd(vp + 16 * VXLD + 32 * u + 4 * ab);
      const v4u va = {lo.x, lo.y, hi.x, hi.y}, vb = {vm[2 * u + ab], 0u, 0u, 0u};
#pragma unroll
      for (int t = 0; t < 2; ++t) { o[t][ab] = mf(va, px[t], (f32x4){0.f,0.f,0.f,0.f}); o[t][ab] = mf(vb, pm[t], o[t][ab]); }
    }
#pragma unroll
    for (int t = 0; t < 2; ++t)
      ov[t][u] = (v4u){pk2(o[t][0][0], o[t][0][1]), pk2(o[t][0][2], o[t][0][3]), pk2(o[t][1][0], o[t][1][1]), pk2(o[t][1][2], o[t][1][3])};
  }
}
__device__ __forceinline__ unsigned attnA_block(const FParams& F, const Plc pm, int s0, int s1, int st, int h, u16* vs, int lane, unsigned* qh) {
  AttnX X, Xn; v4u km[4], kn[4]; unsigned vm[8], vn[8]; unsigned tn = 0;
  auto kpoll = [&](int sg) {
    return __builtin_amdgcn_raw_buffer_load_b32(__builtin_amdgcn_make_buffer_rsrc((char*)F.obuf + sideo(pm, VB0 + sg * OREG, hside(h) ^ 1) + quad(h * 2048), 0, 2048, 0x00020000), 512 * (lane >> 4) + 508, 0, 16);
  };
  const unsigned z0 = kpoll(s0);
  unsigned zn = ~0u;
#define LOADM(sg) do { attnA_load_m(F, pm, sg, h, lane, kn, vn); zn = kpoll((sg) + st < s1 ? (sg) + st : (sg)); } while (0)
  SB0;
  attnA_load_x(F, pm, s0, h, lane, Xn);
  SB0;
#pragma unroll
  for (int j = 0; j < 4; ++j) { kn[j] = (v4u){~0u, ~0u, ~0u, ~0u}; vn[j] = 0u; vn[4 + j] = vn[j]; }
  if (__all(z0 != 0xffffffffu)) LOADM(s0);
  int sig = -1;
#define SIGP do { asm volatile("s_waitcnt vmcnt(0)" ::: "memory"); if (lane == 0) __hip_atomic_store(&F.ctl->flagP[h >> 2][sig][h & 3], 1u, RLX_AGENT); sig = -1; } while (0)
#pragma unroll 1
  for (int seg = s0; seg < s1; seg += st) {
    v4u ov[2][4];
    attnA_stage_v(Xn, vs, lane);
    for (unsigned spins = 0; !__all(chk_m(kn, vn) != 0xffffffffu); ++spins) {
      const unsigned z = kpoll(seg);
      if (sig >= 0) SIGP;
      if (spin_fail(spins, &F.ctl->tmo[0], 6u)) break;
      if (__all(z != 0xffffffffu)) LOADM(seg); else __builtin_amdgcn_s_sleep(PSL);
    }
    if (sig >= 0) SIGP;
#pragma unroll
    for (int j = 0; j < 4; ++j) { X.q[0][j] = Xn.q[0][j]; X.q[1][j] = Xn.q[1][j]; X.kx[0][j] = Xn.kx[0][j]; X.kx[1][j] = Xn.kx[1][j]; km[j] = kn[j]; vm[j] = vn[j]; vm[4 + j] = vn[4 + j]; }
    SB0;
    kn[0].x = ~0u;
    if (seg + st < s1) {
      attnA_load_x(F, pm, seg + st, h, lane, Xn);
      if (__all(zn != 0xffffffffu)) LOADM(seg + st);
    } else if (qh && lane == 0) tn = __hip_atomic_fetch_add(qh, 1u, RLX_AGENT);
    SB0;
    attnA_compute(X, km, vm, vs, lane, ov);
    const __amdgpu_buffer_rsrc_t rs = __builtin_amdgcn_make_buffer_rsrc(wsp(F, faro(pm, QA0 + (3 * h << 20)) + seg * 16384), 0, 8192, 0x00020000);
#pragma unroll
    for (int i = 0; i < 8; ++i) __builtin_amdgcn_raw_buffer_store_b128(ov[i & 1][i >> 1], rs, lane * 16 + (i & 3) * 1024, (i >> 2) * 4096, 16);
    sig = seg;
  }
  if (sig >= 0) SIGP;
  return tn;
}

__device__ __forceinline__ void proj_task(const FParams& F, const Plc pm, bool lastset  , int hg, int nb, char* lds) {
  const int tid = opaque_tid(), lane = tid & 63, w = __builtin_amdgcn_readfirstlane(tid >> 6);
  const int l15 = lane & 15, lq = lane >> 4, h = 4 * hg + w;
  Ctl* ctl = F.ctl;
  v4u wf[4][16];
#pragma unroll
  for (int ps = 0; ps < 2; ++ps) {
    char* stg = lds + w * 32768;
#pragma unroll 4
    for (int q = 0; q < 32; ++q)
      __builtin_amdgcn_global_load_lds((const unsigned*)(F.Wo + (size_t)(hg * 512 + ps * 256 + 8 * q + (lane >> 3)) * DM + nb * 256 + 64 * w + (lane & 7) * 8), (unsigned*)(stg + q * 1024), 16, 0, 0);
    asm volatile("s_waitcnt vmcnt(0)" ::: "memory");
    const u16* p = (const u16*)(stg + (8 * lq + (l15 >> 2)) * 128 + (l15 & 3) * 16);
#pragma unroll
    for (int i = 0; i < 4; ++i)
#pragma unroll
      for (int u = 0; u < 8; ++u) { v2u lo = trrd(p + (32 * u) * 64 + 32 * (i >> 1) + 4 * (i & 1)), hi = trrd(p + (32 * u + 4) * 64 + 32 * (i >> 1) + 4 * (i & 1)); wf[i][8 * ps + u] = (v4u){lo.x, lo.y, hi.x, hi.y}; asm volatile("" : "+a"(wf[i][8 * ps + u])); }
    asm volatile("s_waitcnt lgkmcnt(0)" ::: "memory");
  }
  lds_barrier();
  const __amdgpu_buffer_rsrc_t rp = __builtin_amdgcn_make_buffer_rsrc(wsp(F, QA0), 0, PT0 - QA0 + (2 << 20), 0x00020000);
  const __amdgpu_buffer_rsrc_t ro = __builtin_amdgcn_make_buffer_rsrc(wsp(F, faro(pm, QA0 + (3 * h << 20))), 0, (1 << 20) - 8192, 0x00020000);
  constexpr int segl = NSEG - 1, SL0 = 60;
  auto pofs = [&](int hd) { return faro(pm, QA0 + ((3 * hd + 1 + (nb >> 1)) << 20)) - QA0 + (nb & 1) * 4096; };
  const int pw = pofs(h) + lane * 16, pl = PT0 - QA0 - (SL0 << 19) + nb * 131072, pr = ((w & 2) + (hg & 1)) * 1024 + (w & 1) * 512 + lane * 8;
  int po[8];
#pragma unroll
  for (int k = 0; k < 8; ++k) po[k] = pofs(4 * k + (hg >> 1));
  auto fin_load = [&](int sg, v2u (&v)[8]) {
#pragma unroll
    for (int k = 0; k < 8; ++k) v[k] = __builtin_amdgcn_raw_buffer_load_b64(rp, pr, sg >= SL0 ? pl + (sg << 19) + (4 * k + (hg >> 1)) * 4096 : po[k] + sg * 16384, 16);
  };
  const unsigned s1 = (l15 >> 2) == lq ? 0x3f80u << (16 * (l15 & 1)) : 0u;
  const v4u sel = {(l15 & 2) ? 0u : s1, (l15 & 2) ? s1 : 0u, (l15 & 2) ? 0u : s1, (l15 & 2) ? s1 : 0u};
  auto fin_store = [&](int sg, const v2u (&v)[8]) {
    f32x4 f = {0.f, 0.f, 0.f, 0.f};
#pragma unroll
    for (int j = 0; j < 4; ++j) f = mf(sel, (v4u){v[2 * j].x, v[2 * j].y, v[2 * j + 1].x, v[2 * j + 1].y}, f);
    __builtin_amdgcn_raw_buffer_store_b64((v2u){pk2(f[0], f[1]), pk2(f[2], f[3])}, __builtin_amdgcn_make_buffer_rsrc(F.out + (size_t)(sg * SEG + 16 * (hg & 1)) * 2 * DM + nb * 256 + 64 * (hg >> 1) + 16 * w, 0, 15 * 4096 + 32, 0x00020000),
                                          ((lane >> 1) & 15) * 4096 + (lane >> 5) * 16 + (lane & 1) * 8, 0, OUTAUX);
  };
  auto ticket = [&](int sg) { if (lane == 0) __hip_atomic_fetch_add(&ctl->tticket[sg][nb][0], 1u, RLX_AGENT); };
  auto low = [&](unsigned long long m) { return (int)__builtin_ctzll(m | 1ull << segl); };
  unsigned long long fm = 0, tm = 0;
  unsigned lm = 0;
  auto fins = [&]() {
    if (lm) {
      v2u v[8]; unsigned m = 0; const int sg = SL0 + __builtin_ctz(lm);
      fin_load(sg, v);
#pragma unroll
      for (int k = 0; k < 8; ++k) m = umax3(m, v[k].x, v[k].y);
      if (__all(m != 0xffffffffu)) { fin_store(sg, v); lm &= lm - 1; }
      asm volatile("" ::: "memory");
    }
    if (!fm) return false;
    unsigned long long c = __ballot(ld_agent(&ctl->tticket[((fm >> lane) & 1) ? lane : segl][nb][0]) >= 32u);
    if (!c) return false;
    asm volatile("" ::: "memory");
    v2u v[3][8]; int sg[3];
#pragma unroll
    for (int j = 0; j < 3; ++j) { sg[j] = c ? low(c) : sg[0]; c &= c - 1; fin_load(sg[j], v[j]); fm &= ~(1ull << sg[j]); }
#pragma unroll
    for (int j = 0; j < 3; ++j) if (j == 0 || sg[j] != sg[0]) fin_store(sg[j], v[j]);
    return true;
  };
  unsigned* sh = (unsigned*)(lds + 65536);
  if (tid < 2) sh[tid] = __hip_atomic_fetch_add(&ctl->pseg[hg * 4 + nb][0], 1u, RLX_AGENT);
  lds_barrier();
  int seg = __builtin_amdgcn_readfirstlane(sh[0] < sh[1] ? sh[0] : sh[1]), sn = __builtin_amdgcn_readfirstlane(sh[0] < sh[1] ? sh[1] : sh[0]), sprev = 0, par = 0;
  const int send = lastset ? segl : NSEG;
  if (seg >= segl) seg = send;
  sn = seg >= segl ? NSEG : (sn >= segl ? send : sn);
  v32u WA[8]; v16u C; v8u PO; v4u Q;
#pragma unroll
  for (int i = 0; i < 256; ++i) WA[i >> 5][i & 31] = wf[i >> 6][(i >> 2) & 15][i & 3];
  C[0] = 0; C[1] = lane * 16; C[2] = pw; C[3] = pr; C[4] = ((lane >> 1) & 15) * 4096 + (lane >> 5) * 16 + (lane & 1) * 8; C[5] = tid == 0 ? (int)offsetof(Ctl, pseg) + (hg * 4 + nb) * 64 : 0x7ffff000;
  C[6] = lane == 0 ? 0 : 0x7ffff000; C[7] = 65536 + 8 + w * 4; C[8] = (int)offsetof(Ctl, flagP) + hg * 1024 + lane * 16; C[9] = 1; C[10] = 65536 + 8; C[11] = 0; C[12] = sel.x; C[13] = sel.y; C[14] = sel.z; C[15] = sel.w;
#pragma unroll
  for (int k = 0; k < 8; ++k) PO[k] = __builtin_amdgcn_readfirstlane(po[k]);
  Q = (v4u){(unsigned)offsetof(Ctl, tticket) + nb * 64, w * 8192u, (unsigned)(pl + h * 4096), (unsigned)((lastset ? PLIM : 255) | send << 8)};
#pragma unroll
  for (int k = 0; k < 4; ++k) Q[k] = __builtin_amdgcn_readfirstlane(Q[k]);
  const __amdgpu_buffer_rsrc_t rc = __builtin_amdgcn_make_buffer_rsrc(ctl, 0, sizeof(Ctl), 0x00020000);
  const __amdgpu_buffer_rsrc_t rout = __builtin_amdgcn_make_buffer_rsrc(uni(F.out + (size_t)(16 * (hg & 1)) * 2 * DM + nb * 256 + 64 * (hg >> 1) + 16 * w), 0, (S_ - 17) * 4096 + 32, 0x00020000);
#pragma unroll 1
  while (seg < NSEG) {
    if (seg == segl) {
      wg_wait(lane < 12 ? &ctl->prog[4 * hg + (lane & 3)][(lane >> 2) * 8] : nullptr, NSEG, ctl, false);
      asm volatile("s_waitcnt vmcnt(0)" ::: "memory");
      for (; tm; tm &= tm - 1) ticket(low(tm));
      AttnX X; v4u km[4], ov[2][4]; unsigned vm[8];
      u16* vs = (u16*)(lds + 69632 + w * 16384);
      attnA_load_x(F, pm, segl, h, lane, X);
      attnA_stage_v(X, vs, lane);
      for (unsigned spins = 0;; ++spins) {
        const unsigned z = __builtin_amdgcn_raw_buffer_load_b32(__builtin_amdgcn_make_buffer_rsrc((char*)F.obuf + sideo(pm, VB0 + (segl - 1) * OREG, hside(h) ^ 1) + quad(h * 2048), 0, 2048, 0x00020000), 512 * lq + 508, 0, 16);
        if (!fins() && !__all(z != 0xffffffffu)) __builtin_amdgcn_s_sleep(PSL2);
        if (__all(z != 0xffffffffu) || spin_fail(spins, &ctl->tmo[0], 4u)) break;
      }
      for (unsigned spins = 0;; ++spins) {
        attnA_load_m(F, pm, segl, h, lane, km, vm);
        if (__all(chk_m(km, vm) != 0xffffffffu) || spin_fail(spins, &ctl->tmo[0], 4u)) break;
        asm volatile("" ::: "memory");
      }
      attnA_compute(X, km, vm, vs, lane, ov);
#pragma unroll
      for (int i = 0; i < 8; ++i) *(v4u*)(lds + par + w * 8192 + i * 1024 + lane * 16) = ov[i & 1][i >> 1];
    } else {
      for (unsigned spins = 0;; ++spins) {
        const unsigned z = ld_agent(&ctl->flagP[hg][seg][w]);
        asm volatile("s_waitcnt vmcnt(0)" ::: "memory");
        for (; tm; tm &= tm - 1) ticket(low(tm));
        if (__builtin_amdgcn_readfirstlane(z) != 0u || spin_fail(spins, &ctl->tmo[0], 4u)) break;
        if (!fins()) __builtin_amdgcn_s_sleep(PSL2);
      }
#pragma unroll
      for (int k = 0; k < 8; ++k) __builtin_amdgcn_raw_ptr_buffer_load_lds(ro, (__attribute__((address_space(3))) void*)(lds + par + w * 8192 + k * 1024), 16, lane * 16, seg * 16384 + k * 1024, 0, 16);
    }
    v8u T = {(unsigned)seg, (unsigned)sn, 0u, (unsigned)par, (unsigned)fm, (unsigned)(fm >> 32), 0u, 0u};
#pragma unroll
    for (int k = 0; k < 8; ++k) T[k] = __builtin_amdgcn_readfirstlane(T[k]);
    asm volatile(
      "v_mov_b32 v146, 0\n\t"
      "v_mov_b32 v150, 0\n\t"
      "s_cmp_eq_u32 s64, 63\n\t"
      "s_cbranch_scc1 Lpj_e2\n\t"
      "buffer_load_dwordx4 v[146:149], v168, s[44:47], 0 offen sc1\n\t"
      "s_ff1_i32_b64 s83, s[68:69]\n\t"
      "s_and_b32 s83, s83, 63\n\t"
      "s_lshl_b32 s83, s83, 8\n\t"
      "s_add_u32 s83, s83, s60\n\t"
      "buffer_load_dword v150, off, s[44:47], s83 sc1\n\t"
      "s_waitcnt vmcnt(0)\n\t"
      "Lpj_e2:\n\t"
      "v_add_u32 v160, s67, v161\n\t"
      "s_mov_b32 s73, 0\n\t"
      "s_mov_b32 s78, 1032192\n\t"
      "v_and_b32 v146, v146, v147\n\t"
      "v_and_b32 v148, v148, v149\n\t"
      "v_and_b32 v146, v146, v148\n\t"
      "v_cmp_ne_u32 vcc, 0, v146\n\t"
      "s_min_u32 s79, s65, 63\n\t"
      "s_cmp_lt_u32 s64, 60\n\t"
      "s_cselect_b32 s79, s79, 63\n\t"
      "s_bitcmp1_b64 vcc, s79\n\t"
      "s_cselect_b32 s79, 256, 0\n\t"
      "s_or_b32 s79, s79, s65\n\t"
      "v_mov_b32 v152, s79\n\t"
      "ds_write_b32 v167, v152\n\t"
      "s_waitcnt lgkmcnt(0)\n\t"
      "s_barrier\n\t"
      "ds_read_b32 v152, v170\n\t"
      "ds_read_b128 v[90:93], v160 offset:0\n\t"
      "ds_read_b128 v[94:97], v160 offset:1024\n\t"
      "ds_read_b128 v[98:101], v160 offset:2048\n\t"
      "ds_read_b128 v[102:105], v160 offset:3072\n\t"
      "ds_read_b128 v[106:109], v160 offset:4096\n\t"
      "ds_read_b128 v[110:113], v160 offset:5120\n\t"
      "s_waitcnt lgkmcnt(6)\n\t"
      "v_readfirstlane_b32 s72, v152\n\t"
      "s_lshr_b32 s72, s72, 8\n\t"
      "v_xor_b32 v170, 32, v170\n\t"
      "v_xor_b32 v167, 32, v167\n\t"
      "s_lshl_b32 s85, s78, 3\n\t"
      "s_cmp_lg_u32 s78, 1032192\n\t"
      "s_cselect_b32 s79, 0, 0x7ffff000\n\t"
      "v_or_b32 v158, s79, v164\n\t"
      "v_readfirstlane_b32 s79, v150\n\t"
      "s_ff1_i32_b64 s75, s[68:69]\n\t"
      "s_cmp_ge_u32 s79, 32\n\t"
      "s_cselect_b32 s79, 0, 0x7ffff000\n\t"
      "s_cselect_b32 s80, s75, 63\n\t"
      "s_bitset0_b64 s[68:69], s80\n\t"
      "s_lshl_b32 s78, s80, 14\n\t"
      "s_or_b32 s79, s79, s78\n\t"
      "v_or_b32 v155, s79, v163\n\t"
      "s_cmp_lg_u32 s72, 0\n\t"
      "s_cselect_b32 s79, 0, 0x7ffff000\n\t"
      "v_or_b32 v154, s79, v161\n\t"
      "s_lshl_b32 s77, s65, 14\n\t"
      "s_cmp_lg_u32 s73, 0\n\t"
      "s_cselect_b32 s79, 0, 0x7ffff000\n\t"
      "v_or_b32 v153, s79, v162\n\t"
      "s_lshl_b32 s76, s66, 14\n\t"
      "s_xor_b32 s84, s67, 0x8000\n\t"
      "s_add_u32 s84, s84, s61\n\t"
      "s_ff1_i32_b64 s83, s[68:69]\n\t"
      "s_and_b32 s83, s83, 63\n\t"
      "s_lshl_b32 s83, s83, 8\n\t"
      "s_add_u32 s83, s83, s60\n\t"
      "s_and_b32 s79, s63, 255\n\t"
      "s_cmp_lt_u32 s65, s79\n\t"
      "s_cselect_b32 s74, 0, 0x7ffff000\n\t"
      "v_or_b32 v156, s74, v165\n\t"
      "v_mov_b32 v151, 1\n\t"
      "s_mov_b32 m0, s84\n\t"
      ".p2align 6\n\t"
      "Lpj_loop:\n\t"
      "s_waitcnt lgkmcnt(4)\n\t"
      "v_mfma_f32_16x16x32_bf16 v[10:13], a[0:3], v[90:93], 0\n\t"
      "buffer_atomic_add v151, v156, s[44:47], 0 offen sc0\n\t"
      "v_mfma_f32_16x16x32_bf16 v[14:17], a[0:3], v[94:97], 0\n\t"
      "buffer_load_dwordx4 v[146:149], v168, s[44:47], 0 offen sc1\n\t"
      "v_mfma_f32_16x16x32_bf16 v[18:21], a[64:67], v[90:93], 0\n\t"
      "buffer_load_dword v150, off, s[44:47], s83 sc1\n\t"
      "v_mfma_f32_16x16x32_bf16 v[22:25], a[64:67], v[94:97], 0\n\t"
      "buffer_load_dwordx4 v154, s[40:43], s77 offen offset:0 sc1 lds\n\t"
      "v_mfma_f32_16x16x32_bf16 v[26:29], a[128:131], v[90:93], 0\n\t"
      "v_cvt_pk_bf16_f32 v74, v42, v43\n\t"
      "v_mfma_f32_16x16x32_bf16 v[30:33], a[128:131], v[94:97], 0\n\t"
      "ds_read_b128 v[114:117], v160 offset:6144\n\t"
      "v_mfma_f32_16x16x32_bf16 v[34:37], a[192:195], v[90:93], 0\n\t"
      "ds_read_b128 v[118:121], v160 offset:7168\n\t"
      "v_mfma_f32_16x16x32_bf16 v[38:41], a[192:195], v[94:97], 0\n\t"
      "buffer_load_dwordx4 v154, s[40:43], s77 offen offset:1024 sc1 lds\n\t"
      "s_waitcnt lgkmcnt(4)\n\t"
      "v_mfma_f32_16x16x32_bf16 v[10:13], a[4:7], v[98:101], v[10:13]\n\t"
      "v_cvt_pk_bf16_f32 v75, v44, v45\n\t"
      "v_mfma_f32_16x16x32_bf16 v[14:17], a[4:7], v[102:105], v[14:17]\n\t"
      "v_cvt_pk_bf16_f32 v76, v50, v51\n\t"
      "v_mfma_f32_16x16x32_bf16 v[18:21], a[68:71], v[98:101], v[18:21]\n\t"
      "v_cvt_pk_bf16_f32 v77, v52, v53\n\t"
      "v_mfma_f32_16x16x32_bf16 v[22:25], a[68:71], v[102:105], v[22:25]\n\t"
      "buffer_load_dwordx4 v154, s[40:43], s77 offen offset:2048 sc1 lds\n\t"
      "v_mfma_f32_16x16x32_bf16 v[26:29], a[132:135], v[98:101], v[26:29]\n\t"
      "v_cvt_pk_bf16_f32 v78, v46, v47\n\t"
      "v_mfma_f32_16x16x32_bf16 v[30:33], a[132:135], v[102:105], v[30:33]\n\t"
      "ds_read_b128 v[90:93], v160 offset:8192\n\t"
      "v_mfma_f32_16x16x32_bf16 v[34:37], a[196:199], v[98:101], v[34:37]\n\t"
      "ds_read_b128 v[94:97], v160 offset:9216\n\t"
      "v_mfma_f32_16x16x32_bf16 v[38:41], a[196:199], v[102:105], v[38:41]\n\t"
      "buffer_load_dwordx4 v154, s[40:43], s77 offen offset:3072 sc1 lds\n\t"
      "s_waitcnt lgkmcnt(4)\n\t"
      "v_mfma_f32_16x16x32_bf16 v[10:13], a[8:11], v[106:109], v[10:13]\n\t"
      "s_add_u32 m0, s84, 4096\n\t"
      "v_mfma_f32_16x16x32_bf16 v[14:17], a[8:11], v[110:113], v[14:17]\n\t"
      "s_add_u32 s77, s77, 4096\n\t"
      "v_mfma_f32_16x16x32_bf16 v[18:21], a[72:75], v[106:109], v[18:21]\n\t"
      "v_cvt_pk_bf16_f32 v79, v48, v49\n\t"
      "v_mfma_f32_16x16x32_bf16 v[22:25], a[72:75], v[110:113], v[22:25]\n\t"
      "buffer_load_dwordx4 v154, s[40:43], s77 offen offset:0 sc1 lds\n\t"
      "v_mfma_f32_16x16x32_bf16 v[26:29], a[136:139], v[106:109], v[26:29]\n\t"
      "v_cvt_pk_bf16_f32 v80, v54, v55\n\t"
      "v_mfma_f32_16x16x32_bf16 v[30:33], a[136:139], v[110:113], v[30:33]\n\t"
      "ds_read_b128 v[98:101], v160 offset:10240\n\t"
      "v_mfma_f32_16x16x32_bf16 v[34:37], a[200:203], v[106:109], v[34:37]\n\t"
      "ds_read_b128 v[102:105], v160 offset:11264\n\t"
      "v_mfma_f32_16x16x32_bf16 v[38:41], a[200:203], v[110:113], v[38:41]\n\t"
      "buffer_load_dwordx4 v154, s[40:43], s77 offen offset:1024 sc1 lds\n\t"
      "s_waitcnt lgkmcnt(4)\n\t"
      "v_mfma_f32_16x16x32_bf16 v[10:13], a[12:15], v[114:117], v[10:13]\n\t"
      "v_cvt_pk_bf16_f32 v81, v56, v57\n\t"
      "v_mfma_f32_16x16x32_bf16 v[14:17], a[12:15], v[118:121], v[14:17]\n\t"
      "v_cvt_pk_bf16_f32 v82, v58, v59\n\t"
      "v_mfma_f32_16x16x32_bf16 v[18:21], a[76:79], v[114:117], v[18:21]\n\t"
      "v_cvt_pk_bf16_f32 v83, v60, v61\n\t"
      "v_mfma_f32_16x16x32_bf16 v[22:25], a[76:79], v[118:121], v[22:25]\n\t"
      "buffer_load_dwordx4 v154, s[40:43], s77 offen offset:2048 sc1 lds\n\t"
      "v_mfma_f32_16x16x32_bf16 v[26:29], a[140:143], v[114:117], v[26:29]\n\t"
      "v_cvt_pk_bf16_f32 v84, v66, v67\n\t"
      "v_mfma_f32_16x16x32_bf16 v[30:33], a[140:143], v[118:121], v[30:33]\n\t"
      "ds_read_b128 v[106:109], v160 offset:12288\n\t"
      "v_mfma_f32_16x16x32_bf16 v[34:37], a[204:207], v[114:117], v[34:37]\n\t"
      "ds_read_b128 v[110:113], v160 offset:13312\n\t"
      "v_mfma_f32_16x16x32_bf16 v[38:41], a[204:207], v[118:121], v[38:41]\n\t"
      "buffer_load_dwordx4 v154, s[40:43], s77 offen offset:3072 sc1 lds\n\t"
      "s_waitcnt lgkmcnt(4)\n\t"
      "v_mfma_f32_16x16x32_bf16 v[10:13], a[16:19], v[90:93], v[10:13]\n\t"
      "v_cvt_pk_bf16_f32 v85, v68, v69\n\t"
      "v_mfma_f32_16x16x32_bf16 v[14:17], a[16:19], v[94:97], v[14:17]\n\t"
      "s_waitcnt vmcnt(22)\n\t"
      "v_mfma_f32_16x16x32_bf16 v[138:141], v[172:175], v[122:125], 0\n\t"
      "v_mfma_f32_16x16x32_bf16 v[18:21], a[80:83], v[90:93], v[18:21]\n\t"
      "v_cvt_pk_bf16_f32 v86, v62, v63\n\t"
      "v_mfma_f32_16x16x32_bf16 v[22:25], a[80:83], v[94:97], v[22:25]\n\t"
      "s_waitcnt vmcnt(20)\n\t"
      "v_mfma_f32_16x16x32_bf16 v[138:141], v[172:175], v[126:129], v[138:141]\n\t"
      "v_mfma_f32_16x16x32_bf16 v[26:29], a[144:147], v[90:93], v[26:29]\n\t"
      "v_cvt_pk_bf16_f32 v87, v64, v65\n\t"
      "v_mfma_f32_16x16x32_bf16 v[30:33], a[144:147], v[94:97], v[30:33]\n\t"
      "ds_read_b128 v[114:117], v160 offset:14336\n\t"
      "s_waitcnt vmcnt(18)\n\t"
      "v_mfma_f32_16x16x32_bf16 v[138:141], v[172:175], v[130:133], v[138:141]\n\t"
      "v_mfma_f32_16x16x32_bf16 v[34:37], a[208:211], v[90:93], v[34:37]\n\t"
      "ds_read_b128 v[118:121], v160 offset:15360\n\t"
      "v_mfma_f32_16x16x32_bf16 v[38:41], a[208:211], v[94:97], v[38:41]\n\t"
      "s_waitcnt vmcnt(16)\n\t"
      "v_mfma_f32_16x16x32_bf16 v[138:141], v[172:175], v[134:137], v[138:141]\n\t"
      "s_waitcnt lgkmcnt(4)\n\t"
      "v_mfma_f32_16x16x32_bf16 v[10:13], a[20:23], v[98:101], v[10:13]\n\t"
      "v_cvt_pk_bf16_f32 v88, v70, v71\n\t"
      "v_mfma_f32_16x16x32_bf16 v[14:17], a[20:23], v[102:105], v[14:17]\n\t"
      "v_cvt_pk_bf16_f32 v89, v72, v73\n\t"
      "v_mfma_f32_16x16x32_bf16 v[18:21], a[84:87], v[98:101], v[18:21]\n\t"
      "v_cvt_pk_bf16_f32 v138, v138, v139\n\t"
      "v_mfma_f32_16x16x32_bf16 v[22:25], a[84:87], v[102:105], v[22:25]\n\t"
      "v_cvt_pk_bf16_f32 v139, v140, v141\n\t"
      "v_mfma_f32_16x16x32_bf16 v[26:29], a[148:151], v[98:101], v[26:29]\n\t"
      "buffer_store_dwordx2 v[138:139], v158, s[48:51], s85 offen\n\t"
      "v_mfma_f32_16x16x32_bf16 v[30:33], a[148:151], v[102:105], v[30:33]\n\t"
      "ds_read_b128 v[90:93], v160 offset:16384\n\t"
      "v_mfma_f32_16x16x32_bf16 v[34:37], a[212:215], v[98:101], v[34:37]\n\t"
      "ds_read_b128 v[94:97], v160 offset:17408\n\t"
      "v_mfma_f32_16x16x32_bf16 v[38:41], a[212:215], v[102:105], v[38:41]\n\t"
      "buffer_load_dwordx2 v[122:123], v155, s[36:39], s52 offen sc1\n\t"
      "s_waitcnt lgkmcnt(4)\n\t"
      "v_mfma_f32_16x16x32_bf16 v[10:13], a[24:27], v[106:109], v[10:13]\n\t"
      "v_mfma_f32_16x16x32_bf16 v[14:17], a[24:27], v[110:113], v[14:17]\n\t"
      "buffer_load_dwordx2 v[124:125], v155, s[36:39], s53 offen sc1\n\t"
      "v_mfma_f32_16x16x32_bf16 v[18:21], a[88:91], v[106:109], v[18:21]\n\t"
      "v_mfma_f32_16x16x32_bf16 v[22:25], a[88:91], v[110:113], v[22:25]\n\t"
      "buffer_load_dwordx2 v[126:127], v155, s[36:39], s54 offen sc1\n\t"
      "v_mfma_f32_16x16x32_bf16 v[26:29], a[152:155], v[106:109], v[26:29]\n\t"
      "v_mfma_f32_16x16x32_bf16 v[30:33], a[152:155], v[110:113], v[30:33]\n\t"
      "ds_read_b128 v[98:101], v160 offset:18432\n\t"
      "v_mfma_f32_16x16x32_bf16 v[34:37], a[216:219], v[106:109], v[34:37]\n\t"
      "ds_read_b128 v[102:105], v160 offset:19456\n\t"
      "v_mfma_f32_16x16x32_bf16 v[38:41], a[216:219], v[110:113], v[38:41]\n\t"
      "buffer_load_dwordx2 v[128:129], v155, s[36:39], s55 offen sc1\n\t"
      "s_waitcnt lgkmcnt(4)\n\t"
      "v_mfma_f32_16x16x32_bf16 v[10:13], a[28:31], v[114:117], v[10:13]\n\t"
      "v_mfma_f32_16x16x32_bf16 v[14:17], a[28:31], v[118:121], v[14:17]\n\t"
      "buffer_load_dwordx2 v[130:131], v155, s[36:39], s56 offen sc1\n\t"
      "v_mfma_f32_16x16x32_bf16 v[18:21], a[92:95], v[114:117], v[18:21]\n\t"
      "v_mfma_f32_16x16x32_bf16 v[22:25], a[92:95], v[118:121], v[22:25]\n\t"
      "buffer_load_dwordx2 v[132:133], v155, s[36:39], s57 offen sc1\n\t"
      "v_mfma_f32_16x16x32_bf16 v[26:29], a[156:159], v[114:117], v[26:29]\n\t"
      "v_mfma_f32_16x16x32_bf16 v[30:33], a[156:159], v[118:121], v[30:33]\n\t"
      "ds_read_b128 v[106:109], v160 offset:20480\n\t"
      "v_mfma_f32_16x16x32_bf16 v[34:37], a[220:223], v[114:117], v[34:37]\n\t"
      "ds_read_b128 v[110:113], v160 offset:21504\n\t"
      "v_mfma_f32_16x16x32_bf16 v[38:41], a[220:223], v[118:121], v[38:41]\n\t"
      "buffer_load_dwordx2 v[134:135], v155, s[36:39], s58 offen sc1\n\t"
      "s_waitcnt lgkmcnt(4)\n\t"
      "v_mfma_f32_16x16x32_bf16 v[10:13], a[32:35], v[90:93], v[10:13]\n\t"
      "v_mfma_f32_16x16x32_bf16 v[14:17], a[32:35], v[94:97], v[14:17]\n\t"
      "buffer_load_dwordx2 v[136:137], v155, s[36:39], s59 offen sc1\n\t"
      "v_mfma_f32_16x16x32_bf16 v[18:21], a[96:99], v[90:93], v[18:21]\n\t"
      "s_sub_u32 s79, s70, 1\n\t"
      "v_mfma_f32_16x16x32_bf16 v[22:25], a[96:99], v[94:97], v[22:25]\n\t"
      "s_subb_u32 s80, s71, 0\n\t"
      "v_mfma_f32_16x16x32_bf16 v[26:29], a[160:163], v[90:93], v[26:29]\n\t"
      "s_ff1_i32_b64 s75, s[70:71]\n\t"
      "v_mfma_f32_16x16x32_bf16 v[30:33], a[160:163], v[94:97], v[30:33]\n\t"
      "ds_read_b128 v[114:117], v160 offset:22528\n\t"
      "v_mfma_f32_16x16x32_bf16 v[34:37], a[224:227], v[90:93], v[34:37]\n\t"
      "ds_read_b128 v[118:121], v160 offset:23552\n\t"
      "v_mfma_f32_16x16x32_bf16 v[38:41], a[224:227], v[94:97], v[38:41]\n\t"
      "s_and_b32 s79, s79, s70\n\t"
      "s_waitcnt lgkmcnt(4)\n\t"
      "v_mfma_f32_16x16x32_bf16 v[10:13], a[36:39], v[98:101], v[10:13]\n\t"
      "s_and_b32 s80, s80, s71\n\t"
      "v_mfma_f32_16x16x32_bf16 v[14:17], a[36:39], v[102:105], v[14:17]\n\t"
      "s_or_b32 s79, s79, s80\n\t"
      "v_mfma_f32_16x16x32_bf16 v[18:21], a[100:103], v[98:101], v[18:21]\n\t"
      "s_cselect_b32 s79, 0, 0x7ffff000\n\t"
      "v_mfma_f32_16x16x32_bf16 v[22:25], a[100:103], v[102:105], v[22:25]\n\t"
      "buffer_store_dwordx4 v[74:77], v153, s[36:39], s76 offen offset:0 sc1\n\t"
      "v_mfma_f32_16x16x32_bf16 v[26:29], a[164:167], v[98:101], v[26:29]\n\t"
      "s_cselect_b32 s75, s75, 63\n\t"
      "v_mfma_f32_16x16x32_bf16 v[30:33], a[164:167], v[102:105], v[30:33]\n\t"
      "ds_read_b128 v[90:93], v160 offset:24576\n\t"
      "v_mfma_f32_16x16x32_bf16 v[34:37], a[228:231], v[98:101], v[34:37]\n\t"
      "ds_read_b128 v[94:97], v160 offset:25600\n\t"
      "v_mfma_f32_16x16x32_bf16 v[38:41], a[228:231], v[102:105], v[38:41]\n\t"
      "buffer_store_dwordx4 v[78:81], v153, s[36:39], s76 offen offset:1024 sc1\n\t"
      "s_waitcnt lgkmcnt(4)\n\t"
      "v_mfma_f32_16x16x32_bf16 v[10:13], a[40:43], v[106:109], v[10:13]\n\t"
      "v_or_b32 v157, s79, v166\n\t"
      "v_mfma_f32_16x16x32_bf16 v[14:17], a[40:43], v[110:113], v[14:17]\n\t"
      "s_bitset0_b64 s[70:71], s75\n\t"
      "v_mfma_f32_16x16x32_bf16 v[18:21], a[104:107], v[106:109], v[18:21]\n\t"
      "s_lshl_b32 s75, s75, 8\n\t"
      "v_mfma_f32_16x16x32_bf16 v[22:25], a[104:107], v[110:113], v[22:25]\n\t"
      "buffer_store_dwordx4 v[82:85], v153, s[36:39], s76 offen offset:2048 sc1\n\t"
      "v_mfma_f32_16x16x32_bf16 v[26:29], a[168:171], v[106:109], v[26:29]\n\t"
      "s_add_u32 s75, s75, s60\n\t"
      "v_mfma_f32_16x16x32_bf16 v[30:33], a[168:171], v[110:113], v[30:33]\n\t"
      "ds_read_b128 v[98:101], v160 offset:26624\n\t"
      "v_mfma_f32_16x16x32_bf16 v[34:37], a[232:235], v[106:109], v[34:37]\n\t"
      "ds_read_b128 v[102:105], v160 offset:27648\n\t"
      "v_mfma_f32_16x16x32_bf16 v[38:41], a[232:235], v[110:113], v[38:41]\n\t"
      "buffer_store_dwordx4 v[86:89], v153, s[36:39], s76 offen offset:3072 sc1\n\t"
      "s_waitcnt lgkmcnt(4)\n\t"
      "v_mfma_f32_16x16x32_bf16 v[10:13], a[44:47], v[114:117], v[10:13]\n\t"
      "s_waitcnt vmcnt(22)\n\t"
      "v_and_b32 v146, v146, v147\n\t"
      "v_mfma_f32_16x16x32_bf16 v[14:17], a[44:47], v[118:121], v[14:17]\n\t"
      "v_readfirstlane_b32 s81, v151\n\t"
      "s_or_b32 s81, s81, s74\n\t"
      "v_mfma_f32_16x16x32_bf16 v[18:21], a[108:111], v[114:117], v[18:21]\n\t"
      "s_lshr_b32 s79, s63, 8\n\t"
      "s_cmp_ge_u32 s81, 63\n\t"
      "v_mfma_f32_16x16x32_bf16 v[22:25], a[108:111], v[118:121], v[22:25]\n\t"
      "s_cselect_b32 s81, s79, s81\n\t"
      "s_cmp_ge_u32 s65, 63\n\t"
      "v_mfma_f32_16x16x32_bf16 v[26:29], a[172:175], v[114:117], v[26:29]\n\t"
      "s_cselect_b32 s81, 64, s81\n\t"
      "v_and_b32 v148, v148, v149\n\t"
      "v_mfma_f32_16x16x32_bf16 v[30:33], a[172:175], v[118:121], v[30:33]\n\t"
      "ds_read_b128 v[106:109], v160 offset:28672\n\t"
      "v_and_b32 v146, v146, v148\n\t"
      "v_mfma_f32_16x16x32_bf16 v[34:37], a[236:239], v[114:117], v[34:37]\n\t"
      "ds_read_b128 v[110:113], v160 offset:29696\n\t"
      "v_cmp_ne_u32 vcc, 0, v146\n\t"
      "v_mfma_f32_16x16x32_bf16 v[38:41], a[236:239], v[118:121], v[38:41]\n\t"
      "s_min_u32 s79, s81, 63\n\t"
      "s_cmp_lt_u32 s65, 60\n\t"
      "s_waitcnt lgkmcnt(4)\n\t"
      "v_mfma_f32_16x16x32_bf16 v[10:13], a[48:51], v[90:93], v[10:13]\n\t"
      "s_cselect_b32 s79, s79, 63\n\t"
      "s_bitcmp1_b64 vcc, s79\n\t"
      "v_mfma_f32_16x16x32_bf16 v[14:17], a[48:51], v[94:97], v[14:17]\n\t"
      "s_cselect_b32 s79, 256, 0\n\t"
      "v_mfma_f32_16x16x32_bf16 v[18:21], a[112:115], v[90:93], v[18:21]\n\t"
      "s_or_b32 s79, s79, s81\n\t"
      "v_mfma_f32_16x16x32_bf16 v[22:25], a[112:115], v[94:97], v[22:25]\n\t"
      "v_mov_b32 v152, s79\n\t"
      "v_mfma_f32_16x16x32_bf16 v[26:29], a[176:179], v[90:93], v[26:29]\n\t"
      "ds_write_b32 v167, v152\n\t"
      "v_mfma_f32_16x16x32_bf16 v[30:33], a[176:179], v[94:97], v[30:33]\n\t"
      "ds_read_b128 v[114:117], v160 offset:30720\n\t"
      "v_mfma_f32_16x16x32_bf16 v[34:37], a[240:243], v[90:93], v[34:37]\n\t"
      "ds_read_b128 v[118:121], v160 offset:31744\n\t"
      "v_mfma_f32_16x16x32_bf16 v[38:41], a[240:243], v[94:97], v[38:41]\n\t"
      "s_waitcnt vmcnt(13) lgkmcnt(0)\n\t"
      "s_barrier\n\t"
      "v_mfma_f32_16x16x32_bf16 v[10:13], a[52:55], v[98:101], v[10:13]\n\t"
      "v_xor_b32 v160, 0x8000, v160\n\t"
      "ds_read_b32 v152, v170\n\t"
      "v_mfma_f32_16x16x32_bf16 v[14:17], a[52:55], v[102:105], v[14:17]\n\t"
      "buffer_atomic_add v169, v157, s[44:47], s75 offen\n\t"
      "v_xor_b32 v170, 32, v170\n\t"
      "v_mfma_f32_16x16x32_bf16 v[18:21], a[116:119], v[98:101], v[18:21]\n\t"
      "v_xor_b32 v167, 32, v167\n\t"
      "s_bitset1_b64 s[68:69], s64\n\t"
      "s_bitset1_b64 s[70:71], s64\n\t"
      "v_mfma_f32_16x16x32_bf16 v[22:25], a[116:119], v[102:105], v[22:25]\n\t"
      "s_and_b32 s69, s69, 0xfffffff\n\t"
      "s_and_b32 s71, s71, 0xfffffff\n\t"
      "s_mov_b32 s66, s64\n\t"
      "v_mfma_f32_16x16x32_bf16 v[26:29], a[180:183], v[98:101], v[26:29]\n\t"
      "s_mov_b32 s64, s65\n\t"
      "s_mov_b32 s73, s72\n\t"
      "v_mfma_f32_16x16x32_bf16 v[30:33], a[180:183], v[102:105], v[30:33]\n\t"
      "ds_read_b128 v[90:93], v160 offset:0\n\t"
      "s_xor_b32 s67, s67, 0x8000\n\t"
      "v_mfma_f32_16x16x32_bf16 v[34:37], a[244:247], v[98:101], v[34:37]\n\t"
      "ds_read_b128 v[94:97], v160 offset:1024\n\t"
      "s_waitcnt lgkmcnt(2)\n\t"
      "v_readfirstlane_b32 s65, v152\n\t"
      "v_mfma_f32_16x16x32_bf16 v[38:41], a[244:247], v[102:105], v[38:41]\n\t"
      "s_lshr_b32 s72, s65, 8\n\t"
      "s_and_b32 s65, s65, 255\n\t"
      "v_mfma_f32_16x16x32_bf16 v[10:13], a[56:59], v[106:109], v[10:13]\n\t"
      "s_lshl_b32 s85, s78, 3\n\t"
      "s_cmp_lg_u32 s78, 1032192\n\t"
      "s_cselect_b32 s79, 0, 0x7ffff000\n\t"
      "v_mfma_f32_16x16x32_bf16 v[14:17], a[56:59], v[110:113], v[14:17]\n\t"
      "v_or_b32 v158, s79, v164\n\t"
      "v_readfirstlane_b32 s79, v150\n\t"
      "s_ff1_i32_b64 s75, s[68:69]\n\t"
      "v_mfma_f32_16x16x32_bf16 v[18:21], a[120:123], v[106:109], v[18:21]\n\t"
      "s_cmp_ge_u32 s79, 32\n\t"
      "s_cselect_b32 s79, 0, 0x7ffff000\n\t"
      "s_cselect_b32 s80, s75, 63\n\t"
      "v_mfma_f32_16x16x32_bf16 v[22:25], a[120:123], v[110:113], v[22:25]\n\t"
      "s_bitset0_b64 s[68:69], s80\n\t"
      "s_lshl_b32 s78, s80, 14\n\t"
      "s_or_b32 s79, s79, s78\n\t"
      "v_mfma_f32_16x16x32_bf16 v[26:29], a[184:187], v[106:109], v[26:29]\n\t"
      "v_or_b32 v155, s79, v163\n\t"
      "s_cmp_lg_u32 s72, 0\n\t"
      "v_mfma_f32_16x16x32_bf16 v[30:33], a[184:187], v[110:113], v[30:33]\n\t"
      "ds_read_b128 v[98:101], v160 offset:2048\n\t"
      "s_cselect_b32 s79, 0, 0x7ffff000\n\t"
      "v_mfma_f32_16x16x32_bf16 v[34:37], a[248:251], v[106:109], v[34:37]\n\t"
      "ds_read_b128 v[102:105], v160 offset:3072\n\t"
      "v_or_b32 v154, s79, v161\n\t"
      "v_mfma_f32_16x16x32_bf16 v[38:41], a[248:251], v[110:113], v[38:41]\n\t"
      "s_lshl_b32 s77, s65, 14\n\t"
      "s_cmp_lg_u32 s73, 0\n\t"
      "v_mfma_f32_16x16x32_bf16 v[42:45], a[60:63], v[114:117], v[10:13]\n\t"
      "s_cselect_b32 s79, 0, 0x7ffff000\n\t"
      "v_or_b32 v153, s79, v162\n\t"
      "s_lshl_b32 s76, s66, 14\n\t"
      "v_mfma_f32_16x16x32_bf16 v[46:49], a[60:63], v[118:121], v[14:17]\n\t"
      "s_xor_b32 s84, s67, 0x8000\n\t"
      "s_add_u32 s84, s84, s61\n\t"
      "s_ff1_i32_b64 s83, s[68:69]\n\t"
      "v_mfma_f32_16x16x32_bf16 v[50:53], a[124:127], v[114:117], v[18:21]\n\t"
      "s_and_b32 s83, s83, 63\n\t"
      "s_lshl_b32 s83, s83, 8\n\t"
      "s_add_u32 s83, s83, s60\n\t"
      "v_mfma_f32_16x16x32_bf16 v[54:57], a[124:127], v[118:121], v[22:25]\n\t"
      "s_and_b32 s79, s63, 255\n\t"
      "s_cmp_lt_u32 s65, s79\n\t"
      "s_cselect_b32 s74, 0, 0x7ffff000\n\t"
      "v_mfma_f32_16x16x32_bf16 v[58:61], a[188:191], v[114:117], v[26:29]\n\t"
      "v_or_b32 v156, s74, v165\n\t"
      "v_mov_b32 v151, 1\n\t"
      "v_mfma_f32_16x16x32_bf16 v[62:65], a[188:191], v[118:121], v[30:33]\n\t"
      "ds_read_b128 v[106:109], v160 offset:4096\n\t"
      "s_mov_b32 m0, s84\n\t"
      "v_mfma_f32_16x16x32_bf16 v[66:69], a[252:255], v[114:117], v[34:37]\n\t"
      "ds_read_b128 v[110:113], v160 offset:5120\n\t"
      "v_mfma_f32_16x16x32_bf16 v[70:73], a[252:255], v[118:121], v[38:41]\n\t"
      "s_cmp_lg_u32 s73, 0\n\t"
      "s_cbranch_scc1 Lpj_loop\n\t"
      "s_lshl_b32 s76, s66, 14\n\t"
      "s_lshl_b32 s79, s66, 19\n\t"
      "s_add_u32 s79, s79, s62\n\t"
      "s_cmp_ge_u32 s66, 60\n\t"
      "s_cselect_b32 s76, s79, s76\n\t"
      "s_cselect_b64 vcc, -1, 0\n\t"
      "v_cndmask_b32 v153, v162, v161, vcc\n\t"
      "s_lshr_b32 s79, s78, 14\n\t"
      "s_bitset1_b64 s[68:69], s79\n\t"
      "s_bitset0_b64 s[68:69], 63\n\t"
      "v_cvt_pk_bf16_f32 v74, v42, v43\n\t"
      "v_cvt_pk_bf16_f32 v75, v44, v45\n\t"
      "v_cvt_pk_bf16_f32 v76, v50, v51\n\t"
      "v_cvt_pk_bf16_f32 v77, v52, v53\n\t"
      "buffer_store_dwordx4 v[74:77], v153, s[36:39], s76 offen offset:0 sc1\n\t"
      "v_cvt_pk_bf16_f32 v78, v46, v47\n\t"
      "v_cvt_pk_bf16_f32 v79, v48, v49\n\t"
      "v_cvt_pk_bf16_f32 v80, v54, v55\n\t"
      "v_cvt_pk_bf16_f32 v81, v56, v57\n\t"
      "buffer_store_dwordx4 v[78:81], v153, s[36:39], s76 offen offset:1024 sc1\n\t"
      "v_cvt_pk_bf16_f32 v82, v58, v59\n\t"
      "v_cvt_pk_bf16_f32 v83, v60, v61\n\t"
      "v_cvt_pk_bf16_f32 v84, v66, v67\n\t"
      "v_cvt_pk_bf16_f32 v85, v68, v69\n\t"
      "buffer_store_dwordx4 v[82:85], v153, s[36:39], s76 offen offset:2048 sc1\n\t"
      "v_cvt_pk_bf16_f32 v86, v62, v63\n\t"
      "v_cvt_pk_bf16_f32 v87, v64, v65\n\t"
      "v_cvt_pk_bf16_f32 v88, v70, v71\n\t"
      "v_cvt_pk_bf16_f32 v89, v72, v73\n\t"
      "buffer_store_dwordx4 v[86:89], v153, s[36:39], s76 offen offset:3072 sc1\n\t"
      "s_waitcnt vmcnt(9) lgkmcnt(0)\n\t"
      "v_mfma_f32_16x16x32_bf16 v[138:141], v[172:175], v[122:125], 0\n\t"
      "v_mfma_f32_16x16x32_bf16 v[138:141], v[172:175], v[126:129], v[138:141]\n\t"
      "v_mfma_f32_16x16x32_bf16 v[138:141], v[172:175], v[130:133], v[138:141]\n\t"
      "v_mfma_f32_16x16x32_bf16 v[138:141], v[172:175], v[134:137], v[138:141]\n\t"
      "s_nop 7\n\t"
      "v_cvt_pk_bf16_f32 v138, v138, v139\n\t"
      "v_cvt_pk_bf16_f32 v139, v140, v141\n\t"
      "buffer_store_dwordx2 v[138:139], v158, s[48:51], s85 offen\n\t"
      : "+{s[64:71]}"(T), "+{v[160:175]}"(C)
      : "{a[0:31]}"(WA[0]), "{a[32:63]}"(WA[1]), "{a[64:95]}"(WA[2]), "{a[96:127]}"(WA[3]), "{a[128:159]}"(WA[4]), "{a[160:191]}"(WA[5]), "{a[192:223]}"(WA[6]), "{a[224:255]}"(WA[7]),
        "{s[36:39]}"(rp), "{s[40:43]}"(ro), "{s[44:47]}"(rc), "{s[48:51]}"(rout), "{s[52:59]}"(PO), "{s[60:63]}"(Q)
      : R8(v, 1), R8(v, 2), R8(v, 3), R8(v, 4), R8(v, 5), R8(v, 6), R8(v, 7), R8(v, 8), R8(v, 9), R8(v, 10), R8(v, 11), R8(v, 12), R8(v, 13), R8(v, 14), R8(v, 15),
        "s72", "s73", "s74", "s75", "s76", "s77", "s78", "s79", "s80", "s81", "s82", "s83", "s84", "s85", "s86", "s87", "vcc", "scc", "m0", "memory");
#pragma unroll
    for (int k = 0; k < 8; ++k) T[k] = __builtin_amdgcn_readfirstlane(T[k]);
    seg = T[0]; sn = T[1]; sprev = T[2]; par = T[3]; fm = T[4] | (unsigned long long)T[5] << 32; tm = T[6] | (unsigned long long)T[7] << 32;
    if (sprev >= SL0) lm |= 1u << (sprev - SL0);
  }
  if (tm) asm volatile("s_waitcnt vmcnt(0)" ::: "memory");
  for (; tm; tm &= tm - 1) ticket(low(tm));
#pragma unroll 1
  for (unsigned spins = 0; (fm || lm) && !spin_fail(spins, &ctl->tmo[0], 5u); ++spins) {
    fins();
  }
}

__device__ __forceinline__ Plc qkv_task(const FParams& F, unsigned long long ep, int mat  , int h, char* lds) {
  const int tid = opaque_tid(), lane = tid & 63, w = __builtin_amdgcn_readfirstlane(tid >> 6);
  const int l15 = lane & 15, lq = lane >> 4;
  const u16* W = mat == 0 ? F.Wk : (mat == 1 ? F.Wq : F.Wv);
  v32u WA[8];
#pragma unroll
  for (int ps = 0; ps < 2; ++ps) {
    char* stg = lds + w * 32768;
#pragma unroll 4
    for (int q = 0; q < 32; ++q)
      __builtin_amdgcn_global_load_lds((const unsigned*)(W + (size_t)(ps * 512 + 16 * q + (lane >> 2)) * HDA + h * DHA + 32 * w + (lane & 3) * 8), (unsigned*)(stg + q * 1024), 16, 0, 0);
    asm volatile("s_waitcnt vmcnt(0)" ::: "memory");
    const u16* p = (const u16*)(stg + (128 * lq + (l15 >> 2)) * 64 + (l15 & 3) * 16);
#pragma unroll
    for (int i = 0; i < 2; ++i)
#pragma unroll
      for (int u = 0; u < 16; ++u) {
        const v2u lo = trrd(p + 8 * u * 32 + 4 * i), hi = trrd(p + (8 * u + 4) * 32 + 4 * i);
        v4u z = {lo.x, lo.y, hi.x, hi.y};
        asm volatile("" : "+a"(z));
#pragma unroll
        for (int e = 0; e < 4; ++e) WA[4 * i + 2 * ps + (u >> 3)][(u & 7) * 4 + e] = z[e];
      }
    asm volatile("s_waitcnt lgkmcnt(0)" ::: "memory");
  }
  const unsigned long long tok = uni64(ep) + 1;
  wait_token(F, tok);
  const Plc pm = wait_votes(F, tok, lane, false);
  lds_barrier();
  v16u C; v8u S;
  C[0] = (unsigned)(size_t)lds + l15 * 1040 + lq * 256;
#pragma unroll
  for (int k = 0; k < 8; ++k) C[1 + k] = (8 * w + k) * 2048 + lane * 16;
  C[9] = w * 1024 + lane * 16; C[10] = mat == 1 ? 0x7ffff000 : quad(h * 2048) + (w & 1) * 1024 + lane * 16; C[11] = mat == 1 || tid >= 32 ? 0x7ffff000 : quad(h * 512) + lane * 16;
  C[12] = tid == 0 ? CBASE + (int)offsetof(Ctl, prog) + h * 128 + mat * 32 : 0x7ffff000; C[13] = tid == 0 && mat != 1 ? HBASE + 256 + (mat * 16 + h) * 8 : 0x7ffff000; C[14] = C[15] = 0;
  { const unsigned long long pl2 = __ballot((sideo(pm, (lane >> 1) << 21, hside(h) ^ (mat == 0)) >> (13 + (lane & 1))) & 1);
    S = (v8u){(unsigned)pl2, (unsigned)(pl2 >> 32), (unsigned)(w < 2 ? VB0 : OB0), (unsigned)(size_t)lds + w * 8320, (unsigned)tok, (unsigned)(tok >> 32), 0u, 0u}; }
#pragma unroll
  for (int k = 0; k < 8; ++k) S[k] = __builtin_amdgcn_readfirstlane(S[k]);
  asm volatile(
      "s_mov_b32 s56, 0\n\t"
      "s_mov_b32 s58, 0\n\t"
      "s_mov_b32 s59, s51\n\t"
      "s_mov_b32 s60, 66560\n\t"
      "v_mov_b32 v116, -1\n\t"
      "v_mov_b32 v117, -1\n\t"
      "v_mov_b32 v118, -1\n\t"
      "v_mov_b32 v119, -1\n\t"
      "s_add_u32 m0, s59, 0\n\t"
      "s_nop 0\n\t"
      "buffer_load_dwordx4 v101, s[36:39], s58 offen lds\n\t"
      "s_add_u32 m0, s59, 1040\n\t"
      "s_nop 0\n\t"
      "buffer_load_dwordx4 v102, s[36:39], s58 offen lds\n\t"
      "s_add_u32 m0, s59, 2080\n\t"
      "s_nop 0\n\t"
      "buffer_load_dwordx4 v103, s[36:39], s58 offen lds\n\t"
      "s_add_u32 m0, s59, 3120\n\t"
      "s_nop 0\n\t"
      "buffer_load_dwordx4 v104, s[36:39], s58 offen lds\n\t"
      "s_add_u32 m0, s59, 4160\n\t"
      "s_nop 0\n\t"
      "buffer_load_dwordx4 v105, s[36:39], s58 offen lds\n\t"
      "s_add_u32 m0, s59, 5200\n\t"
      "s_nop 0\n\t"
      "buffer_load_dwordx4 v106, s[36:39], s58 offen lds\n\t"
      "s_add_u32 m0, s59, 6240\n\t"
      "s_nop 0\n\t"
      "buffer_load_dwordx4 v107, s[36:39], s58 offen lds\n\t"
      "s_add_u32 m0, s59, 7280\n\t"
      "s_nop 0\n\t"
      "buffer_load_dwordx4 v108, s[36:39], s58 offen lds\n\t"
      "s_add_u32 s58, s58, 1024\n\t"
      "s_add_u32 m0, s59, 33280\n\t"
      "s_nop 0\n\t"
      "buffer_load_dwordx4 v101, s[36:39], s58 offen lds\n\t"
      "s_add_u32 m0, s59, 34320\n\t"
      "s_nop 0\n\t"
      "buffer_load_dwordx4 v102, s[36:39], s58 offen lds\n\t"
      "s_add_u32 m0, s59, 35360\n\t"
      "s_nop 0\n\t"
      "buffer_load_dwordx4 v103, s[36:39], s58 offen lds\n\t"
      "s_add_u32 m0, s59, 36400\n\t"
      "s_nop 0\n\t"
      "buffer_load_dwordx4 v104, s[36:39], s58 offen lds\n\t"
      "s_add_u32 m0, s59, 37440\n\t"
      "s_nop 0\n\t"
      "buffer_load_dwordx4 v105, s[36:39], s58 offen lds\n\t"
      "s_add_u32 m0, s59, 38480\n\t"
      "s_nop 0\n\t"
      "buffer_load_dwordx4 v106, s[36:39], s58 offen lds\n\t"
      "s_add_u32 m0, s59, 39520\n\t"
      "s_nop 0\n\t"
      "buffer_load_dwordx4 v107, s[36:39], s58 offen lds\n\t"
      "s_add_u32 m0, s59, 40560\n\t"
      "s_nop 0\n\t"
      "buffer_load_dwordx4 v108, s[36:39], s58 offen lds\n\t"
      "s_add_u32 s58, s58, 64512\n\t"
      "s_add_u32 m0, s59, 66560\n\t"
      "s_nop 0\n\t"
      "buffer_load_dwordx4 v101, s[36:39], s58 offen lds\n\t"
      "s_add_u32 m0, s59, 67600\n\t"
      "s_nop 0\n\t"
      "buffer_load_dwordx4 v102, s[36:39], s58 offen lds\n\t"
      "s_add_u32 m0, s59, 68640\n\t"
      "s_nop 0\n\t"
      "buffer_load_dwordx4 v103, s[36:39], s58 offen lds\n\t"
      "s_add_u32 m0, s59, 69680\n\t"
      "s_nop 0\n\t"
      "buffer_load_dwordx4 v104, s[36:39], s58 offen lds\n\t"
      "s_add_u32 m0, s59, 70720\n\t"
      "s_nop 0\n\t"
      "buffer_load_dwordx4 v105, s[36:39], s58 offen lds\n\t"
      "s_add_u32 m0, s59, 71760\n\t"
      "s_nop 0\n\t"
      "buffer_load_dwordx4 v106, s[36:39], s58 offen lds\n\t"
      "s_add_u32 m0, s59, 72800\n\t"
      "s_nop 0\n\t"
      "buffer_load_dwordx4 v107, s[36:39], s58 offen lds\n\t"
      "s_add_u32 m0, s59, 73840\n\t"
      "s_nop 0\n\t"
      "buffer_load_dwordx4 v108, s[36:39], s58 offen lds\n\t"
      "s_add_u32 s58, s58, 1024\n\t"
      "s_add_u32 s59, s59, 99840\n\t"
      "v_mov_b32 v120, 0x7ffff000\n\t"
      "s_waitcnt vmcnt(8)\n\t"
      "s_barrier\n\t"
      "ds_read_b128 v[50:53], v100 offset:0\n\t"
      "ds_read_b128 v[54:57], v100 offset:16640\n\t"
      "ds_read_b128 v[58:61], v100 offset:16\n\t"
      "ds_read_b128 v[62:65], v100 offset:16656\n\t"
      "ds_read_b128 v[66:69], v100 offset:32\n\t"
      "ds_read_b128 v[70:73], v100 offset:16672\n\t"
      ".p2align 6\n\t"
      "Lst_loop:\n\t"
      "s_waitcnt lgkmcnt(4)\n\t"
      "v_mfma_f32_16x16x32_bf16 v[10:13], a[0:3], v[50:53], 0\n\t"
      "s_add_u32 m0, s59, 0\n\t"
      "v_mfma_f32_16x16x32_bf16 v[18:21], a[128:131], v[50:53], 0\n\t"
      "ds_read_b128 v[74:77], v100 offset:48\n\t"
      "v_mfma_f32_16x16x32_bf16 v[14:17], a[0:3], v[54:57], 0\n\t"
      "ds_read_b128 v[78:81], v100 offset:16688\n\t"
      "v_mfma_f32_16x16x32_bf16 v[22:25], a[128:131], v[54:57], 0\n\t"
      "buffer_load_dwordx4 v101, s[36:39], s58 offen lds\n\t"
      "s_waitcnt lgkmcnt(4)\n\t"
      "v_mfma_f32_16x16x32_bf16 v[10:13], a[4:7], v[58:61], v[10:13]\n\t"
      "v_mfma_f32_16x16x32_bf16 v[18:21], a[132:135], v[58:61], v[18:21]\n\t"
      "ds_read_b128 v[50:53], v100 offset:64\n\t"
      "v_cvt_pk_bf16_f32 v42, v26, v27\n\t"
      "v_mfma_f32_16x16x32_bf16 v[14:17], a[4:7], v[62:65], v[14:17]\n\t"
      "ds_read_b128 v[54:57], v100 offset:16704\n\t"
      "v_cvt_pk_bf16_f32 v43, v28, v29\n\t"
      "v_mfma_f32_16x16x32_bf16 v[22:25], a[132:135], v[62:65], v[22:25]\n\t"
      "s_add_u32 m0, s59, 1040\n\t"
      "s_waitcnt lgkmcnt(4)\n\t"
      "v_mfma_f32_16x16x32_bf16 v[10:13], a[8:11], v[66:69], v[10:13]\n\t"
      "v_mfma_f32_16x16x32_bf16 v[18:21], a[136:139], v[66:69], v[18:21]\n\t"
      "ds_read_b128 v[58:61], v100 offset:80\n\t"
      "v_cvt_pk_bf16_f32 v44, v34, v35\n\t"
      "v_mfma_f32_16x16x32_bf16 v[14:17], a[8:11], v[70:73], v[14:17]\n\t"
      "ds_read_b128 v[62:65], v100 offset:16720\n\t"
      "buffer_load_dwordx4 v102, s[36:39], s58 offen lds\n\t"
      "v_mfma_f32_16x16x32_bf16 v[22:25], a[136:139], v[70:73], v[22:25]\n\t"
      "s_waitcnt lgkmcnt(4)\n\t"
      "v_mfma_f32_16x16x32_bf16 v[10:13], a[12:15], v[74:77], v[10:13]\n\t"
      "s_add_u32 m0, s59, 2080\n\t"
      "v_mfma_f32_16x16x32_bf16 v[18:21], a[140:143], v[74:77], v[18:21]\n\t"
      "ds_read_b128 v[66:69], v100 offset:96\n\t"
      "v_cvt_pk_bf16_f32 v45, v36, v37\n\t"
      "v_mfma_f32_16x16x32_bf16 v[14:17], a[12:15], v[78:81], v[14:17]\n\t"
      "ds_read_b128 v[70:73], v100 offset:16736\n\t"
      "v_cvt_pk_bf16_f32 v46, v30, v31\n\t"
      "v_mfma_f32_16x16x32_bf16 v[22:25], a[140:143], v[78:81], v[22:25]\n\t"
      "buffer_load_dwordx4 v103, s[36:39], s58 offen lds\n\t"
      "s_waitcnt lgkmcnt(4)\n\t"
      "v_mfma_f32_16x16x32_bf16 v[10:13], a[16:19], v[50:53], v[10:13]\n\t"
      "v_mfma_f32_16x16x32_bf16 v[18:21], a[144:147], v[50:53], v[18:21]\n\t"
      "ds_read_b128 v[74:77], v100 offset:112\n\t"
      "v_cvt_pk_bf16_f32 v47, v32, v33\n\t"
      "v_mfma_f32_16x16x32_bf16 v[14:17], a[16:19], v[54:57], v[14:17]\n\t"
      "ds_read_b128 v[78:81], v100 offset:16752\n\t"
      "v_cvt_pk_bf16_f32 v48, v38, v39\n\t"
      "v_mfma_f32_16x16x32_bf16 v[22:25], a[144:147], v[54:57], v[22:25]\n\t"
      "s_add_u32 m0, s59, 3120\n\t"
      "s_waitcnt lgkmcnt(4)\n\t"
      "v_mfma_f32_16x16x32_bf16 v[10:13], a[20:23], v[58:61], v[10:13]\n\t"
      "v_mfma_f32_16x16x32_bf16 v[18:21], a[148:151], v[58:61], v[18:21]\n\t"
      "ds_read_b128 v[50:53], v100 offset:128\n\t"
      "v_cvt_pk_bf16_f32 v49, v40, v41\n\t"
      "v_mfma_f32_16x16x32_bf16 v[14:17], a[20:23], v[62:65], v[14:17]\n\t"
      "ds_read_b128 v[54:57], v100 offset:16768\n\t"
      "buffer_load_dwordx4 v104, s[36:39], s58 offen lds\n\t"
      "v_mfma_f32_16x16x32_bf16 v[22:25], a[148:151], v[62:65], v[22:25]\n\t"
      "buffer_store_dwordx4 v[42:45], v120, s[40:43], s61 offen sc1\n\t"
      "s_waitcnt lgkmcnt(4)\n\t"
      "v_mfma_f32_16x16x32_bf16 v[10:13], a[24:27], v[66:69], v[10:13]\n\t"
      "s_add_u32 m0, s59, 4160\n\t"
      "v_mfma_f32_16x16x32_bf16 v[18:21], a[152:155], v[66:69], v[18:21]\n\t"
      "ds_read_b128 v[58:61], v100 offset:144\n\t"
      "v_mfma_f32_16x16x32_bf16 v[14:17], a[24:27], v[70:73], v[14:17]\n\t"
      "ds_read_b128 v[62:65], v100 offset:16784\n\t"
      "v_mfma_f32_16x16x32_bf16 v[22:25], a[152:155], v[70:73], v[22:25]\n\t"
      "buffer_load_dwordx4 v105, s[36:39], s58 offen lds\n\t"
      "s_waitcnt lgkmcnt(4)\n\t"
      "v_mfma_f32_16x16x32_bf16 v[10:13], a[28:31], v[74:77], v[10:13]\n\t"
      "s_add_u32 s61, s61, 4096\n\t"
      "v_mfma_f32_16x16x32_bf16 v[18:21], a[156:159], v[74:77], v[18:21]\n\t"
      "ds_read_b128 v[66:69], v100 offset:160\n\t"
      "v_mfma_f32_16x16x32_bf16 v[14:17], a[28:31], v[78:81], v[14:17]\n\t"
      "ds_read_b128 v[70:73], v100 offset:16800\n\t"
      "v_mfma_f32_16x16x32_bf16 v[22:25], a[156:159], v[78:81], v[22:25]\n\t"
      "s_add_u32 m0, s59, 5200\n\t"
      "s_waitcnt lgkmcnt(4)\n\t"
      "v_mfma_f32_16x16x32_bf16 v[10:13], a[32:35], v[50:53], v[10:13]\n\t"
      "buffer_store_dwordx4 v[46:49], v120, s[40:43], s61 offen sc1\n\t"
      "v_mfma_f32_16x16x32_bf16 v[18:21], a[160:163], v[50:53], v[18:21]\n\t"
      "ds_read_b128 v[74:77], v100 offset:176\n\t"
      "v_mfma_f32_16x16x32_bf16 v[14:17], a[32:35], v[54:57], v[14:17]\n\t"
      "ds_read_b128 v[78:81], v100 offset:16816\n\t"
      "buffer_load_dwordx4 v106, s[36:39], s58 offen lds\n\t"
      "v_mfma_f32_16x16x32_bf16 v[22:25], a[160:163], v[54:57], v[22:25]\n\t"
      "s_waitcnt lgkmcnt(4)\n\t"
      "v_mfma_f32_16x16x32_bf16 v[10:13], a[36:39], v[58:61], v[10:13]\n\t"
      "s_add_u32 m0, s59, 6240\n\t"
      "v_mfma_f32_16x16x32_bf16 v[18:21], a[164:167], v[58:61], v[18:21]\n\t"
      "ds_read_b128 v[50:53], v100 offset:192\n\t"
      "v_mfma_f32_16x16x32_bf16 v[14:17], a[36:39], v[62:65], v[14:17]\n\t"
      "ds_read_b128 v[54:57], v100 offset:16832\n\t"
      "v_mfma_f32_16x16x32_bf16 v[22:25], a[164:167], v[62:65], v[22:25]\n\t"
      "buffer_load_dwordx4 v107, s[36:39], s58 offen lds\n\t"
      "s_waitcnt lgkmcnt(4)\n\t"
      "v_mfma_f32_16x16x32_bf16 v[10:13], a[40:43], v[66:69], v[10:13]\n\t"
      "v_mfma_f32_16x16x32_bf16 v[18:21], a[168:171], v[66:69], v[18:21]\n\t"
      "ds_read_b128 v[58:61], v100 offset:208\n\t"
      "v_mfma_f32_16x16x32_bf16 v[14:17], a[40:43], v[70:73], v[14:17]\n\t"
      "ds_read_b128 v[62:65], v100 offset:16848\n\t"
      "v_mfma_f32_16x16x32_bf16 v[22:25], a[168:171], v[70:73], v[22:25]\n\t"
      "s_add_u32 m0, s59, 7280\n\t"
      "s_waitcnt lgkmcnt(4)\n\t"
      "v_mfma_f32_16x16x32_bf16 v[10:13], a[44:47], v[74:77], v[10:13]\n\t"
      "v_mfma_f32_16x16x32_bf16 v[18:21], a[172:175], v[74:77], v[18:21]\n\t"
      "ds_read_b128 v[66:69], v100 offset:224\n\t"
      "v_mfma_f32_16x16x32_bf16 v[14:17], a[44:47], v[78:81], v[14:17]\n\t"
      "ds_read_b128 v[70:73], v100 offset:16864\n\t"
      "buffer_load_dwordx4 v108, s[36:39], s58 offen lds\n\t"
      "v_mfma_f32_16x16x32_bf16 v[22:25], a[172:175], v[78:81], v[22:25]\n\t"
      "s_waitcnt lgkmcnt(4)\n\t"
      "v_mfma_f32_16x16x32_bf16 v[10:13], a[48:51], v[50:53], v[10:13]\n\t"
      "s_add_u32 s58, s58, 64512\n\t"
      "s_min_u32 s58, s58, 4128768\n\t"
      "v_mfma_f32_16x16x32_bf16 v[18:21], a[176:179], v[50:53], v[18:21]\n\t"
      "ds_read_b128 v[74:77], v100 offset:240\n\t"
      "v_mfma_f32_16x16x32_bf16 v[14:17], a[48:51], v[54:57], v[14:17]\n\t"
      "ds_read_b128 v[78:81], v100 offset:16880\n\t"
      "s_cmp_gt_u32 s56, 2\n\t"
      "v_mfma_f32_16x16x32_bf16 v[22:25], a[176:179], v[54:57], v[22:25]\n\t"
      "s_cbranch_scc0 Lst_w0\n\t"
      "Lst_w0r:\n\t"
      "s_waitcnt vmcnt(21) lgkmcnt(0)\n\t"
      "s_barrier\n\t"
      "v_mfma_f32_16x16x32_bf16 v[10:13], a[52:55], v[58:61], v[10:13]\n\t"
      "s_sub_u32 s59, s59, s60\n\t"
      "s_sub_u32 s59, s59, 33280\n\t"
      "v_mfma_f32_16x16x32_bf16 v[18:21], a[180:183], v[58:61], v[18:21]\n\t"
      "ds_read_b128 v[50:53], v100 offset:33280\n\t"
      "v_mfma_f32_16x16x32_bf16 v[14:17], a[52:55], v[62:65], v[14:17]\n\t"
      "ds_read_b128 v[54:57], v100 offset:49920\n\t"
      "v_mfma_f32_16x16x32_bf16 v[22:25], a[180:183], v[62:65], v[22:25]\n\t"
      "s_cmp_gt_u32 s56, 2\n\t"
      "s_subb_u32 s65, s56, 0\n\t"
      "v_mfma_f32_16x16x32_bf16 v[10:13], a[56:59], v[66:69], v[10:13]\n\t"
      "v_mov_b32 v124, s65\n\t"
      "buffer_store_dword v124, v112, s[44:47], 0 offen sc1\n\t"
      "v_mfma_f32_16x16x32_bf16 v[18:21], a[184:187], v[66:69], v[18:21]\n\t"
      "ds_read_b128 v[58:61], v100 offset:33296\n\t"
      "v_mfma_f32_16x16x32_bf16 v[14:17], a[56:59], v[70:73], v[14:17]\n\t"
      "ds_read_b128 v[62:65], v100 offset:49936\n\t"
      "v_mfma_f32_16x16x32_bf16 v[22:25], a[184:187], v[70:73], v[22:25]\n\t"
      "s_cmp_lg_u32 s56, 1\n\t"
      "s_cbranch_scc0 Lst_f0\n\t"
      "Lst_f0r:\n\t"
      "v_mfma_f32_16x16x32_bf16 v[10:13], a[60:63], v[74:77], v[10:13]\n\t"
      "s_cmp_lt_u32 s56, 60\n\t"
      "s_cselect_b32 s64, 0, 0x7ffff000\n\t"
      "v_mfma_f32_16x16x32_bf16 v[18:21], a[188:191], v[74:77], v[18:21]\n\t"
      "ds_read_b128 v[66:69], v100 offset:33312\n\t"
      "s_add_u32 s57, s56, 4\n\t"
      "v_mfma_f32_16x16x32_bf16 v[14:17], a[60:63], v[78:81], v[14:17]\n\t"
      "ds_read_b128 v[70:73], v100 offset:49952\n\t"
      "s_lshl_b32 s62, s57, 18\n\t"
      "v_mfma_f32_16x16x32_bf16 v[22:25], a[188:191], v[78:81], v[22:25]\n\t"
      "s_waitcnt lgkmcnt(4)\n\t"
      "v_mfma_f32_16x16x32_bf16 v[10:13], a[64:67], v[50:53], v[10:13]\n\t"
      "s_add_u32 m0, s59, 0\n\t"
      "v_mfma_f32_16x16x32_bf16 v[18:21], a[192:195], v[50:53], v[18:21]\n\t"
      "ds_read_b128 v[74:77], v100 offset:33328\n\t"
      "s_add_u32 s62, s62, s50\n\t"
      "v_mfma_f32_16x16x32_bf16 v[14:17], a[64:67], v[54:57], v[14:17]\n\t"
      "ds_read_b128 v[78:81], v100 offset:49968\n\t"
      "s_lshr_b32 s65, s62, 20\n\t"
      "v_mfma_f32_16x16x32_bf16 v[22:25], a[192:195], v[54:57], v[22:25]\n\t"
      "buffer_load_dwordx4 v101, s[36:39], s58 offen lds\n\t"
      "s_waitcnt lgkmcnt(4)\n\t"
      "v_mfma_f32_16x16x32_bf16 v[10:13], a[68:71], v[58:61], v[10:13]\n\t"
      "v_mfma_f32_16x16x32_bf16 v[18:21], a[196:199], v[58:61], v[18:21]\n\t"
      "ds_read_b128 v[50:53], v100 offset:33344\n\t"
      "s_and_b32 s65, s65, 62\n\t"
      "v_mfma_f32_16x16x32_bf16 v[14:17], a[68:71], v[62:65], v[14:17]\n\t"
      "ds_read_b128 v[54:57], v100 offset:49984\n\t"
      "s_lshr_b64 s[66:67], s[48:49], s65\n\t"
      "v_mfma_f32_16x16x32_bf16 v[22:25], a[196:199], v[62:65], v[22:25]\n\t"
      "s_add_u32 m0, s59, 1040\n\t"
      "s_waitcnt lgkmcnt(4)\n\t"
      "v_mfma_f32_16x16x32_bf16 v[10:13], a[72:75], v[66:69], v[10:13]\n\t"
      "v_mfma_f32_16x16x32_bf16 v[18:21], a[200:203], v[66:69], v[18:21]\n\t"
      "ds_read_b128 v[58:61], v100 offset:33360\n\t"
      "s_bfe_u32 s65, s62, 0x10013\n\t"
      "v_mfma_f32_16x16x32_bf16 v[14:17], a[72:75], v[70:73], v[14:17]\n\t"
      "ds_read_b128 v[62:65], v100 offset:50000\n\t"
      "buffer_load_dwordx4 v102, s[36:39], s58 offen lds\n\t"
      "v_mfma_f32_16x16x32_bf16 v[22:25], a[200:203], v[70:73], v[22:25]\n\t"
      "s_waitcnt lgkmcnt(4)\n\t"
      "v_mfma_f32_16x16x32_bf16 v[10:13], a[76:79], v[74:77], v[10:13]\n\t"
      "s_add_u32 m0, s59, 2080\n\t"
      "v_mfma_f32_16x16x32_bf16 v[18:21], a[204:207], v[74:77], v[18:21]\n\t"
      "ds_read_b128 v[66:69], v100 offset:33376\n\t"
      "s_lshl1_add_u32 s66, s65, s66\n\t"
      "v_mfma_f32_16x16x32_bf16 v[14:17], a[76:79], v[78:81], v[14:17]\n\t"
      "ds_read_b128 v[70:73], v100 offset:50016\n\t"
      "s_and_b32 s66, s66, 3\n\t"
      "v_mfma_f32_16x16x32_bf16 v[22:25], a[204:207], v[78:81], v[22:25]\n\t"
      "buffer_load_dwordx4 v103, s[36:39], s58 offen lds\n\t"
      "s_waitcnt lgkmcnt(4)\n\t"
      "v_mfma_f32_16x16x32_bf16 v[10:13], a[80:83], v[50:53], v[10:13]\n\t"
      "v_or_b32 v121, s64, v110\n\t"
      "v_mfma_f32_16x16x32_bf16 v[18:21], a[208:211], v[50:53], v[18:21]\n\t"
      "ds_read_b128 v[74:77], v100 offset:33392\n\t"
      "s_lshl_b32 s66, s66, 13\n\t"
      "v_mfma_f32_16x16x32_bf16 v[14:17], a[80:83], v[54:57], v[14:17]\n\t"
      "ds_read_b128 v[78:81], v100 offset:50032\n\t"
      "s_or_b32 s62, s62, s66\n\t"
      "v_mfma_f32_16x16x32_bf16 v[22:25], a[208:211], v[54:57], v[22:25]\n\t"
      "s_add_u32 m0, s59, 3120\n\t"
      "s_waitcnt lgkmcnt(4)\n\t"
      "v_mfma_f32_16x16x32_bf16 v[10:13], a[84:87], v[58:61], v[10:13]\n\t"
      "v_or_b32 v122, s64, v111\n\t"
      "v_mfma_f32_16x16x32_bf16 v[18:21], a[212:215], v[58:61], v[18:21]\n\t"
      "ds_read_b128 v[50:53], v100 offset:33408\n\t"
      "s_lshl_b32 s63, s57, 16\n\t"
      "v_mfma_f32_16x16x32_bf16 v[14:17], a[84:87], v[62:65], v[14:17]\n\t"
      "ds_read_b128 v[54:57], v100 offset:50048\n\t"
      "buffer_load_dwordx4 v104, s[36:39], s58 offen lds\n\t"
      "v_mfma_f32_16x16x32_bf16 v[22:25], a[212:215], v[62:65], v[22:25]\n\t"
      "s_waitcnt lgkmcnt(4)\n\t"
      "v_mfma_f32_16x16x32_bf16 v[10:13], a[88:91], v[66:69], v[10:13]\n\t"
      "s_add_u32 m0, s59, 4160\n\t"
      "v_mfma_f32_16x16x32_bf16 v[18:21], a[216:219], v[66:69], v[18:21]\n\t"
      "ds_read_b128 v[58:61], v100 offset:33424\n\t"
      "s_lshr_b32 s65, s63, 20\n\t"
      "v_mfma_f32_16x16x32_bf16 v[14:17], a[88:91], v[70:73], v[14:17]\n\t"
      "ds_read_b128 v[62:65], v100 offset:50064\n\t"
      "s_and_b32 s65, s65, 62\n\t"
      "v_mfma_f32_16x16x32_bf16 v[22:25], a[216:219], v[70:73], v[22:25]\n\t"
      "buffer_load_dwordx4 v105, s[36:39], s58 offen lds\n\t"
      "s_waitcnt lgkmcnt(4)\n\t"
      "v_mfma_f32_16x16x32_bf16 v[10:13], a[92:95], v[74:77], v[10:13]\n\t"
      "v_mfma_f32_16x16x32_bf16 v[18:21], a[220:223], v[74:77], v[18:21]\n\t"
      "ds_read_b128 v[66:69], v100 offset:33440\n\t"
      "s_lshr_b64 s[66:67], s[48:49], s65\n\t"
      "v_mfma_f32_16x16x32_bf16 v[14:17], a[92:95], v[78:81], v[14:17]\n\t"
      "ds_read_b128 v[70:73], v100 offset:50080\n\t"
      "s_bfe_u32 s65, s63, 0x10013\n\t"
      "v_mfma_f32_16x16x32_bf16 v[22:25], a[220:223], v[78:81], v[22:25]\n\t"
      "s_add_u32 m0, s59, 5200\n\t"
      "s_waitcnt lgkmcnt(4)\n\t"
      "v_mfma_f32_16x16x32_bf16 v[10:13], a[96:99], v[50:53], v[10:13]\n\t"
      "v_mfma_f32_16x16x32_bf16 v[18:21], a[224:227], v[50:53], v[18:21]\n\t"
      "ds_read_b128 v[74:77], v100 offset:33456\n\t"
      "s_lshl1_add_u32 s66, s65, s66\n\t"
      "v_mfma_f32_16x16x32_bf16 v[14:17], a[96:99], v[54:57], v[14:17]\n\t"
      "ds_read_b128 v[78:81], v100 offset:50096\n\t"
      "buffer_load_dwordx4 v106, s[36:39], s58 offen lds\n\t"
      "v_mfma_f32_16x16x32_bf16 v[22:25], a[224:227], v[54:57], v[22:25]\n\t"
      "s_waitcnt lgkmcnt(4)\n\t"
      "v_mfma_f32_16x16x32_bf16 v[10:13], a[100:103], v[58:61], v[10:13]\n\t"
      "s_add_u32 m0, s59, 6240\n\t"
      "v_mfma_f32_16x16x32_bf16 v[18:21], a[228:231], v[58:61], v[18:21]\n\t"
      "ds_read_b128 v[50:53], v100 offset:33472\n\t"
      "s_and_b32 s66, s66, 3\n\t"
      "v_mfma_f32_16x16x32_bf16 v[14:17], a[100:103], v[62:65], v[14:17]\n\t"
      "ds_read_b128 v[54:57], v100 offset:50112\n\t"
      "s_lshl_b32 s66, s66, 13\n\t"
      "v_mfma_f32_16x16x32_bf16 v[22:25], a[228:231], v[62:65], v[22:25]\n\t"
      "buffer_load_dwordx4 v107, s[36:39], s58 offen lds\n\t"
      "s_waitcnt lgkmcnt(4)\n\t"
      "v_mfma_f32_16x16x32_bf16 v[10:13], a[104:107], v[66:69], v[10:13]\n\t"
      "v_mfma_f32_16x16x32_bf16 v[18:21], a[232:235], v[66:69], v[18:21]\n\t"
      "ds_read_b128 v[58:61], v100 offset:33488\n\t"
      "s_or_b32 s63, s63, s66\n\t"
      "v_mfma_f32_16x16x32_bf16 v[14:17], a[104:107], v[70:73], v[14:17]\n\t"
      "ds_read_b128 v[62:65], v100 offset:50128\n\t"
      "v_mfma_f32_16x16x32_bf16 v[22:25], a[232:235], v[70:73], v[22:25]\n\t"
      "s_add_u32 m0, s59, 7280\n\t"
      "s_waitcnt lgkmcnt(4)\n\t"
      "v_mfma_f32_16x16x32_bf16 v[10:13], a[108:111], v[74:77], v[10:13]\n\t"
      "v_mfma_f32_16x16x32_bf16 v[18:21], a[236:239], v[74:77], v[18:21]\n\t"
      "ds_read_b128 v[66:69], v100 offset:33504\n\t"
      "v_mfma_f32_16x16x32_bf16 v[14:17], a[108:111], v[78:81], v[14:17]\n\t"
      "ds_read_b128 v[70:73], v100 offset:50144\n\t"
      "buffer_load_dwordx4 v108, s[36:39], s58 offen lds\n\t"
      "v_mfma_f32_16x16x32_bf16 v[22:25], a[236:239], v[78:81], v[22:25]\n\t"
      "buffer_store_dwordx4 v[116:119], v121, s[44:47], s62 offen sc1\n\t"
      "s_waitcnt lgkmcnt(4)\n\t"
      "v_mfma_f32_16x16x32_bf16 v[10:13], a[112:115], v[50:53], v[10:13]\n\t"
      "s_add_u32 s58, s58, 1024\n\t"
      "buffer_store_dwordx4 v[116:119], v122, s[44:47], s63 offen sc1\n\t"
      "v_mfma_f32_16x16x32_bf16 v[18:21], a[240:243], v[50:53], v[18:21]\n\t"
      "ds_read_b128 v[74:77], v100 offset:33520\n\t"
      "v_mfma_f32_16x16x32_bf16 v[14:17], a[112:115], v[54:57], v[14:17]\n\t"
      "ds_read_b128 v[78:81], v100 offset:50160\n\t"
      "v_mfma_f32_16x16x32_bf16 v[22:25], a[240:243], v[54:57], v[22:25]\n\t"
      "s_waitcnt vmcnt(21) lgkmcnt(0)\n\t"
      "s_barrier\n\t"
      "v_mfma_f32_16x16x32_bf16 v[10:13], a[116:119], v[58:61], v[10:13]\n\t"
      "v_add_u32 v100, s60, v100\n\t"
      "s_add_u32 s59, s59, 33280\n\t"
      "v_mfma_f32_16x16x32_bf16 v[18:21], a[244:247], v[58:61], v[18:21]\n\t"
      "ds_read_b128 v[50:53], v100 offset:0\n\t"
      "v_mfma_f32_16x16x32_bf16 v[14:17], a[116:119], v[62:65], v[14:17]\n\t"
      "ds_read_b128 v[54:57], v100 offset:16640\n\t"
      "v_mfma_f32_16x16x32_bf16 v[22:25], a[244:247], v[62:65], v[22:25]\n\t"
      "s_sub_u32 s60, 0, s60\n\t"
      "v_mfma_f32_16x16x32_bf16 v[10:13], a[120:123], v[66:69], v[10:13]\n\t"
      "s_lshl_b32 s61, s56, 14\n\t"
      "v_mov_b32 v120, v109\n\t"
      "v_mfma_f32_16x16x32_bf16 v[18:21], a[248:251], v[66:69], v[18:21]\n\t"
      "ds_read_b128 v[58:61], v100 offset:16\n\t"
      "v_mfma_f32_16x16x32_bf16 v[14:17], a[120:123], v[70:73], v[14:17]\n\t"
      "ds_read_b128 v[62:65], v100 offset:16656\n\t"
      "v_mfma_f32_16x16x32_bf16 v[22:25], a[248:251], v[70:73], v[22:25]\n\t"
      "s_add_u32 s56, s56, 1\n\t"
      "v_mfma_f32_16x16x32_bf16 v[26:29], a[124:127], v[74:77], v[10:13]\n\t"
      "s_cmp_lt_u32 s56, 64\n\t"
      "v_mfma_f32_16x16x32_bf16 v[34:37], a[252:255], v[74:77], v[18:21]\n\t"
      "ds_read_b128 v[66:69], v100 offset:32\n\t"
      "v_mfma_f32_16x16x32_bf16 v[30:33], a[124:127], v[78:81], v[14:17]\n\t"
      "ds_read_b128 v[70:73], v100 offset:16672\n\t"
      "v_mfma_f32_16x16x32_bf16 v[38:41], a[252:255], v[78:81], v[22:25]\n\t"
      "s_cbranch_scc1 Lst_loop\n\t"
      "s_nop 7\n\t"
      "v_cvt_pk_bf16_f32 v42, v26, v27\n\t"
      "v_cvt_pk_bf16_f32 v43, v28, v29\n\t"
      "v_cvt_pk_bf16_f32 v44, v34, v35\n\t"
      "v_cvt_pk_bf16_f32 v45, v36, v37\n\t"
      "v_cvt_pk_bf16_f32 v46, v30, v31\n\t"
      "v_cvt_pk_bf16_f32 v47, v32, v33\n\t"
      "v_cvt_pk_bf16_f32 v48, v38, v39\n\t"
      "v_cvt_pk_bf16_f32 v49, v40, v41\n\t"
      "buffer_store_dwordx4 v[42:45], v109, s[40:43], s61 offen sc1\n\t"
      "s_add_u32 s61, s61, 4096\n\t"
      "buffer_store_dwordx4 v[46:49], v109, s[40:43], s61 offen sc1\n\t"
      "v_mov_b32 v124, 64\n\t"
      "s_waitcnt vmcnt(0) lgkmcnt(0)\n\t"
      "s_barrier\n\t"
      "buffer_store_dword v124, v112, s[44:47], 0 offen sc1\n\t"
      "s_branch Lst_end\n\t"
      "Lst_w0:\n\t"
      "s_waitcnt vmcnt(0)\n\t"
      "s_branch Lst_w0r\n\t"
      "Lst_f0:\n\t"
      "v_mov_b32 v126, s52\n\t"
      "v_mov_b32 v127, s53\n\t"
      "buffer_store_dwordx2 v[126:127], v113, s[44:47], 0 offen sc1\n\t"
      "s_branch Lst_f0r\n\t"
      "Lst_end:\n\t"
      : "+{v[100:115]}"(C)
      : "{a[0:31]}"(WA[0]), "{a[32:63]}"(WA[1]), "{a[64:95]}"(WA[2]), "{a[96:127]}"(WA[3]), "{a[128:159]}"(WA[4]), "{a[160:191]}"(WA[5]), "{a[192:223]}"(WA[6]), "{a[224:255]}"(WA[7]),
        "{s[36:39]}"(__builtin_amdgcn_make_buffer_rsrc((void*)F.Ax, 0, S_ * DM * 2, 0x00020000)),
        "{s[40:43]}"(__builtin_amdgcn_make_buffer_rsrc(uni(wsp(F, faro(pm, QA0 + ((3 * h + (mat ^ (mat < 2))) << 20)))), 0, (1 << 20) - 8192, 0x00020000)),
        "{s[44:47]}"(__builtin_amdgcn_make_buffer_rsrc(F.obuf, 0, HBASE + (int)sizeof(Hdr), 0x00020000)), "{s[48:55]}"(S)
      : R8(v, 1), R8(v, 2), R8(v, 3), R8(v, 4), R8(v, 5), R8(v, 6), R8(v, 7), R8(v, 8), R8(v, 9), "v116", "v117", "v118", "v119", R8(v, 12),
        "s56", "s57", "s58", "s59", R8(s, 6), "s86", "s87", "scc", "m0", "memory");
  return pm;
}

__device__ __forceinline__ float dot8(v4u a, v4u b) {
  float d = 0.f;
#pragma unroll
  for (int e = 0; e < 4; ++e) { const unsigned x = a[e], y = b[e]; d = __builtin_amdgcn_fdot2_f32_bf16(__builtin_bit_cast(bfx2, x), __builtin_bit_cast(bfx2, y), d, false); }
  return d;
}
template <int CTRL> __device__ __forceinline__ float qperm(float v) { return __int_as_float(__builtin_amdgcn_update_dpp(0, __float_as_int(v), CTRL, 0xf, 0xf, true)); }
__device__ __forceinline__ unsigned attnB_32(const FParams& F, const Plc pm, int pos0, unsigned* qh) {
  unsigned tn = 0;
  const int tid = opaque_tid(), cc = tid & 127, l3 = tid & 3;
  int p0 = pos0 + (tid >> 7);
  const __amdgpu_buffer_rsrc_t rs = __builtin_amdgcn_make_buffer_rsrc(wsp(F, RB0), 0, S_ * 16384, 0x00020000);
  const int f1 = faro(pm, RB0 + pos0 * 16384) & 8192, f0 = faro(pm, RB0 + (pos0 < 16 ? 0 : pos0 - 16) * 16384) & 8192;
  v4u k[8], qn[8], kn[8], vn[8], h0[7]; f32x2 v[8][4];
#pragma unroll
  for (int i = 0; i < 7; ++i) {
    const int kp = p0 - 14 + 2 * i, o = (kp < 0 ? 0 : kp) * 16384 + f0 + cc * 16;
    k[i] = __builtin_amdgcn_raw_buffer_load_b128(rs, o, DB * 2, 16); h0[i] = __builtin_amdgcn_raw_buffer_load_b128(rs, o, 2 * DB * 2, 16);
  }
  int vo = p0 * 16384 + f1 + cc * 16;
#define LDROW(i) do { kn[i] = __builtin_amdgcn_raw_buffer_load_b128(rs, vo + DB * 2, (i) * 32768, 16); qn[i] = __builtin_amdgcn_raw_buffer_load_b128(rs, vo, (i) * 32768, 16); \
    vn[i] = __builtin_amdgcn_raw_buffer_load_b128(rs, vo, (i) * 32768 + 2 * DB * 2, 16); } while (0)
#define UNPK(d, x) do { _Pragma("unroll") for (int e = 0; e < 4; ++e) d[e] = (f32x2){__uint_as_float((x)[e] << 16), __uint_as_float((x)[e] & 0xffff0000u)}; } while (0)
#pragma unroll
  for (int i = 0; i < 8; ++i) LDROW(i);
#pragma unroll
  for (int i = 0; i < 7; ++i) UNPK(v[i], h0[i]);
  const float scale = 0.17677669529663687f;
#pragma unroll 1
  for (int it = 0; it < 2; ++it) {
    vo = it == 0 ? vo + 16 * 16384 : 0x7ffff000;
    if (it == 1 && tid == 0) tn = __hip_atomic_fetch_add(qh, 1u, RLX_AGENT);
    SB0;
#pragma unroll
    for (int i = 0; i < 8; ++i) {
      const v4u q = qn[i];
      k[(i + 7) & 7] = kn[i]; UNPK(v[(i + 7) & 7], vn[i]);
      const int pos = p0 + 2 * i;
      float d[8];
#pragma unroll
      for (int w = 0; w < 8; ++w) d[w] = dot8(q, k[(i + 7 - w) & 7]);
      SB0;
      LDROW(i);
      SB0;
      float a[4], g[2];
#pragma unroll
      for (int j = 0; j < 4; ++j) a[j] = ((l3 & 1) ? d[2 * j + 1] : d[2 * j]) + qperm<0xB1>((l3 & 1) ? d[2 * j] : d[2 * j + 1]);
#pragma unroll
      for (int j = 0; j < 2; ++j) g[j] = ((l3 & 2) ? a[2 * j + 1] : a[2 * j]) + qperm<0x4E>((l3 & 2) ? a[2 * j] : a[2 * j + 1]);
      unsigned p = pk2(g[0], g[1]);
      p = pk2(__uint_as_float(p << 16) * scale, __uint_as_float(p & 0xffff0000u) * scale);
      g[0] = pos - 2 * l3 >= 0 ? __uint_as_float(p << 16) : -INFINITY; g[1] = pos - 2 * l3 - 8 >= 0 ? __uint_as_float(p & 0xffff0000u) : -INFINITY;
      float mx = fmaxf(g[0], g[1]);
      mx = fmaxf(mx, qperm<0xB1>(mx)); mx = fmaxf(mx, qperm<0x4E>(mx));
      g[0] = __expf(g[0] - mx); g[1] = __expf(g[1] - mx);
      float sum = g[0] + g[1];
      sum += qperm<0xB1>(sum); sum += qperm<0x4E>(sum);
      const float inv = __builtin_amdgcn_rcpf(sum);
      p = pk2(g[0] * inv, g[1] * inv);
      g[0] = __uint_as_float(p << 16); g[1] = __uint_as_float(p & 0xffff0000u);
      f32x2 o[4] = {{0.f, 0.f}, {0.f, 0.f}, {0.f, 0.f}, {0.f, 0.f}};
#pragma unroll
      for (int w = 0; w < 8; ++w) {
        const float gw = g[w >> 2], pw = (w & 3) == 0 ? qperm<0x00>(gw) : ((w & 3) == 1 ? qperm<0x55>(gw) : ((w & 3) == 2 ? qperm<0xAA>(gw) : qperm<0xFF>(gw)));
#pragma unroll
        for (int e = 0; e < 4; ++e) o[e] = __builtin_elementwise_fma((f32x2){pw, pw}, v[(i + 7 - w) & 7][e], o[e]);
      }
      __builtin_amdgcn_raw_buffer_store_b128((v4u){pk2(o[0].x, o[0].y), pk2(o[1].x, o[1].y), pk2(o[2].x, o[2].y), pk2(o[3].x, o[3].y)}, rs, pos * 16384 + f1 + cc * 16, 3 * DB * 2, 16);
      SB0;
    }
    p0 += 16;
  }
#undef LDROW
#undef UNPK
  return tn;
}

constexpr int NTASK = 117;
__constant__ unsigned TASKS[5][NTASK] = {
  {
  2049,2177,2305,2433,2051,2179,2307,2435,2053,2181,2309,2437,5120,5121,5122,5123,5124,5125,5126,5127,5184,5185,5186,5187,5440,5441,5442,5443,5444,5445,5446,5447,
  5504,5505,5506,5507,3394,3395,3456,3457,3458,3459,3520,3521,5632,5633,5634,5635,4736,4737,4738,4739,5568,5569,5570,5571,7252,7253,7254,7255,7260,7261,7262,7263,
  5696,5697,5698,5699,5700,5701,5702,5703,5800,5801,5802,5803,5804,5805,5806,5807,7296,7297,7298,7299,4864,4865,4866,4867,4992,4993,4994,4995,5940,5941,5942,5943,
  5992,5993,5994,5995,5996,5997,5998,5999,6088,6089,6090,6091,6096,6097,6098,6099,6100,6101,6102,6103,65535,
  },
  {
  2560,2688,2816,2944,2563,2691,2819,2947,2566,2694,2822,2950,2569,2697,2825,2953,5188,5189,5190,5191,5252,5253,5254,5255,5312,5313,5314,5315,5508,5509,5510,5511,
  3650,3651,3712,3713,3776,3777,3904,3905,3328,3329,7168,7169,7170,7171,7180,7181,7182,7183,5636,5637,5638,5639,7232,7233,7234,7235,7256,7257,7258,7259,7300,7301,
  7302,7303,7304,7305,7306,7307,5808,5809,5810,5811,5864,5865,5866,5867,5932,5933,5934,5935,6000,6001,6002,6003,6064,6065,6066,6067,65535,65535,65535,65535,65535,65535,
  65535,65535,65535,65535,65535,65535,65535,65535,65535,65535,65535,65535,65535,65535,65535,65535,65535,65535,65535,65535,65535,
  },
  {
  2561,2689,2817,2945,2564,2692,2820,2948,2567,2695,2823,2951,2570,2698,2826,2954,2571,2699,2827,2955,2057,2185,2313,2441,5316,5317,5318,5319,3778,3779,3906,3907,
  4032,4033,3072,3073,3136,3137,3202,3203,3392,3393,7184,7185,7186,7187,7192,7193,7194,7195,3522,3523,5572,5573,5574,5575,7244,7245,7246,7247,7308,7309,7310,7311,
  7312,7313,7314,7315,7316,7317,7318,7319,5868,5869,5870,5871,5872,5873,5874,5875,6004,6005,6006,6007,6056,6057,6058,6059,65535,65535,65535,65535,65535,65535,65535,65535,
  65535,65535,65535,65535,65535,65535,65535,65535,65535,65535,65535,65535,65535,65535,65535,65535,65535,65535,65535,65535,65535,
  },
  {
  2048,2176,2304,2432,2050,2178,2306,2434,2052,2180,2308,2436,2054,2182,2310,2438,5248,5249,5250,5251,2058,2186,2314,2442,5376,5377,5378,5379,3648,3649,3714,3715,
  3840,3841,3968,3969,3138,3139,3264,3265,3330,3331,7172,7173,7174,7175,7188,7189,7190,7191,3584,3585,3586,3587,7236,7237,7238,7239,7248,7249,7250,7251,7320,7321,
  7322,7323,7324,7325,7326,7327,5812,5813,5814,5815,5876,5877,5878,5879,5936,5937,5938,5939,6060,6061,6062,6063,6092,6093,6094,6095,65535,65535,65535,65535,65535,65535,
  65535,65535,65535,65535,65535,65535,65535,65535,65535,65535,65535,65535,65535,65535,65535,65535,65535,65535,65535,65535,65535,
  },
  {
  2562,2690,2818,2946,2565,2693,2821,2949,2568,2696,2824,2952,2055,2183,2311,2439,2056,2184,2312,2440,2059,2187,2315,2443,5380,5381,5382,5383,3842,3843,3970,3971,
  4034,4035,3074,3075,3200,3201,3266,3267,7176,7177,7178,7179,7196,7197,7198,7199,4096,4097,4098,4099,7240,7241,7242,7243,4224,4225,4226,4227,4352,4353,4354,4355,
  4480,4481,4482,4483,4608,4609,4610,4611,5928,5929,5930,5931,6068,6069,6070,6071,6104,6105,6106,6107,6108,6109,6110,6111,65535,65535,65535,65535,65535,65535,65535,65535,
  65535,65535,65535,65535,65535,65535,65535,65535,65535,65535,65535,65535,65535,65535,65535,65535,65535,65535,65535,65535,65535,
  },
};
__device__ __forceinline__ void worker(const FParams& F, char* lds) {
  unsigned long long tok;
  const unsigned long long ep = ld_agent(&F.hdr->epoch[0]);
  const int tid = threadIdx.x, lane = tid & 63, w = __builtin_amdgcn_readfirstlane(tid >> 6);
  const int x = (blockIdx.x & 7) - 3;
  Ctl* ctl = F.ctl;
  int* sh = (int*)(lds + 3 * wg::STAGE_B);
#define TOUCH(ptr, bytes, off) __builtin_amdgcn_raw_ptr_buffer_load_lds(__builtin_amdgcn_make_buffer_rsrc((void*)(ptr), 0, bytes, 0x00020000), \
        (__attribute__((address_space(3))) void*)(lds + 3 * wg::STAGE_B + w * 4096 + 1024), 4, off, 0, 0, 0)
  bool picked = false;
  unsigned* psig = nullptr; int pn = 0;
#define FLUSH_SIG do { if (pn) { asm volatile("s_waitcnt vmcnt(0)" ::: "memory"); __syncthreads(); LATE_SIGNAL; pn = 0; } } while (0)
  Plc pm = {0, 0};
  const v4u ff = {0xffffffffu, 0xffffffffu, 0xffffffffu, 0xffffffffu};
  const int wid = (blockIdx.x >> 3) * 5 + x;
  const unsigned t00 = (unsigned)__builtin_amdgcn_s_memrealtime();
  if (wid < 96) {
    const int tz = opaque_tid();
    if (wid < 16) TOUCH(F.Ax, S_ * DM * 2, (wid * 256 + tz) * 128);
    if (wid >= 64) {
      const int n1 = wid - 64;
      if (F.sh3 & 8)
#pragma unroll
        for (int k = 0; k < 3; ++k) { const u16* wp = k == 0 ? F.Wq : (k == 2 ? F.Wo : F.Wk); TOUCH(wp, DM * HDA * 2, (n1 * 256 + tz) * 1024 + ((384 - (int)(size_t)wp) & 1023)); }
      if (F.sh3 & 2)
#pragma unroll 4
        for (int k = 0; k < 8; ++k) TOUCH(F.Wo, DM * HDA * 2, ((k * 32 + n1) * 256 + tz) * 128);
    }
    if (wid >= 64) while ((int)(t00 + ((F.sh3 >> 16) & 255) * 100 - (unsigned)__builtin_amdgcn_s_memrealtime()) > 0) __builtin_amdgcn_s_sleep(32);
    pm = qkv_task(F, ep, wid < 32 ? 0 : (wid < 64 ? 2 : 1), wid & 31, lds);
    wait_votes(F, uni64(ep) + 1, opaque_tid() & 63, true);
  } else {
    const int tz = opaque_tid();
    {
      const int u = wid - 96;
      const __amdgpu_buffer_rsrc_t rh = __builtin_amdgcn_make_buffer_rsrc(F.obuf, 0, CBASE + (int)sizeof(Ctl), 0x00020000);
      if (u == NPROBER - 1) {
        for (int i = tz; i < (int)sizeof(Ctl) / 16; i += 256) __builtin_amdgcn_raw_buffer_store_b128((v4u){0u, 0u, 0u, 0u}, rh, CBASE + i * 16, 0, 16);
        asm volatile("s_waitcnt vmcnt(0)" ::: "memory");
        __syncthreads();
        if (tid == 0) __hip_atomic_store(&F.hdr->token[0], ep + 1, RLX_AGENT);
      }
      if (w == 0) {
        const __amdgpu_buffer_rsrc_t ra = __builtin_amdgcn_make_buffer_rsrc(F.obuf, 0, ARENA, 0x00020000);
        unsigned xcc, sp = 0; asm volatile("s_getreg_b32 %0, hwreg(HW_REG_XCC_ID)" : "=s"(xcc));
#pragma unroll 1
        for (int pg = 0; pg < (u < NPAGE / 2 ? 2 : 0); ++pg) {
          unsigned best0 = ~0u, best1 = ~0u;
#pragma unroll 1
          for (int k = 0; k < PROBES; ++k) {
            const unsigned long long t0 = __builtin_amdgcn_s_memtime();
            __builtin_amdgcn_raw_buffer_store_b128(ff, ra, tz < 8 ? ((2 * u + pg) << 21) + (1 << 20) + 49152 + (k & 1) * 8192 + tz * 16 : 0x7ffffff0, 0, 16);
            asm volatile("s_waitcnt vmcnt(0)" ::: "memory");
            const unsigned dt = (unsigned)(__builtin_amdgcn_s_memtime() - t0);
            if (k & 1) best1 = dt < best1 ? dt : best1; else best0 = dt < best0 ? dt : best0;
            if (k >= 3 && (k & 1) && (best0 < PNEAR) != (best1 < PNEAR)) break;
          }
          sp |= ((best1 < best0) ^ (((xcc ^ (xcc - (blockIdx.x & 7) + 1)) >> 2) & 1)) << pg;
        }
        if (u >= 48) {
          unsigned bs[4] = {~0u, ~0u, ~0u, ~0u}; int sc = 0;
#pragma unroll 1
          for (int k = 0; k < SPROBES; ++k) {
#pragma unroll
            for (int q = 0; q < 4; ++q) {
              const unsigned long long t0 = __builtin_amdgcn_s_memtime();
              __builtin_amdgcn_raw_buffer_store_b128(ff, ra, tz < 8 ? ((u - 48) << 21) + (1 << 20) + 32768 + q * 8192 + 1024 + tz * 16 : 0x7ffffff0, 0, 16);
              asm volatile("s_waitcnt vmcnt(0)" ::: "memory");
              const unsigned dt = (unsigned)(__builtin_amdgcn_s_memtime() - t0);
              bs[q] = dt < bs[q] ? dt : bs[q];
            }
            const bool f1 = bs[1] + bs[3] > bs[0] + bs[2];
            const int d0 = (int)(bs[0] - bs[2]), d1 = (int)(bs[1] - bs[3]), df = f1 ? d1 : d0, dn = (xcc & 1) ? (f1 ? d0 : d1) : -(f1 ? d0 : d1);
            sc = df + dn;
            if (k >= 2 && ((df > 24 && df < 80 && dn > 24 && dn < 80) || (df < -24 && df > -80 && dn < -24 && dn > -80))) break;
          }
          sp = (sc > 0) ^ ((xcc - (blockIdx.x & 7)) & 1);
        }
        if (tz == 0) __hip_atomic_store(&F.hdr->vote[u], 4 * (ep + 1) + sp, RLX_AGENT);
      }
      __syncthreads();
#pragma unroll
      for (int k = 0; k < 8; ++k) __builtin_amdgcn_raw_buffer_store_b128(ff, rh, u * 16384 + (k & 3) * 4096 + tz * 16, k < 4 ? OB0 : VB0, 16);
      { v4u v = ff;
        if (u < 16) {
          const int i = (u >> 3) * 512 + (u & 1) * 256 + tz, c = tr4(i >> 6) * 4 + ((i >> 4) & 3), sl = i & 15, r = (sl >> 2) * 2 + (sl & 1), hh = (sl >> 1) & 1;
#pragma unroll
          for (int p = 0; p < 4; ++p) v[p] = nosent(*(const unsigned*)(F.mem0 + r * DM + 16 * c + 4 * p + 2 * hh));
        }
        __builtin_amdgcn_raw_buffer_store_b128(v, rh, u * 4096 + tz * 16, MB0, 16); }
      if ((u >> 3) == 2) __builtin_amdgcn_raw_buffer_store_b128((u & 5) == 0 && tz == 0 ? (v4u){0u, 0u, 0u, 0u} : ff, rh, (u & 7) * 4096 + tz * 16, RC0, 16);
      asm volatile("s_waitcnt vmcnt(0)" ::: "memory");
      __syncthreads();
      if (tid == 0) __hip_atomic_store(&F.hdr->fill[u], ep + 1, RLX_AGENT);
    }
    if (F.sh3 & 64)
#pragma unroll
      for (int k = 0; k < 4; ++k) TOUCH(F.Wo, DM * HDA * 2, ((k * 64 + wid - 96) * 256 + tz) * 128);
    while ((int)(t00 + ((F.sh3 >> 8) & 255) * 100 - (unsigned)__builtin_amdgcn_s_memrealtime()) > 0) __builtin_amdgcn_s_sleep(32);
    const int vo = ((wid - 96) * 256 + tz) * 128;
#pragma unroll
    for (int k = 0; k < 8; ++k) TOUCH(k < 2 ? F.Ax : (k < 4 ? F.Bx : (k < 7 ? F.wqkvB : F.woB)), (k < 4 ? S_ * DB : (k < 7 ? DB * NQKVB : DB * DB)) * 2, vo + (k < 4 ? k & 1 : (k < 7 ? k - 4 : 0)) * 64 * 256 * 128);
#pragma unroll
    for (int k = 0; k < 8; ++k) __builtin_amdgcn_raw_buffer_store_b128(ff, __builtin_amdgcn_make_buffer_rsrc(wsp(F, PT0), 0, 2 << 20, 0x00020000), tz * 16, (wid - 96) * 32768 + k * 4096, 16);
    tok = uni64(ep) + 1;
    wait_token(F, tok);
    pm = wait_votes(F, tok, lane, true);
    asm volatile("s_waitcnt vmcnt(0)" ::: "memory");
    __syncthreads();
    wg_signal(&ctl->done[16]);
  }
  leave_epoch(F);
  for (;;) {
    const int lane = opaque_tid() & 63;
    if (!picked) {
      if (tid == 0) sh[0] = (int)__hip_atomic_fetch_add(&ctl->qhead[x][0], 1u, RLX_AGENT);
      __syncthreads();
    }
    const int t = __builtin_amdgcn_readfirstlane(sh[0]);
    lds_barrier();
    picked = true;
    const int code = (int)TASKS[x][t < NTASK ? t : NTASK - 1];
    if ((code >> 10) > 2) FLUSH_SIG;
    if (code == 0xFFFF) return;
    const int tk = (code >> 10) & 7, r = (code >> 6) & 15, n = code & 63;
    const int rb0 = faro(pm, RB0 + (r << 21)), rbd = faro(pm, RB0 + ((r | 1) << 21)) - rb0 - (1 << 21);
    if (tk == 2 || tk == 4) {
      if (tk == 4) wg_wait(lane < 2 ? &ctl->cntAB[r + lane][0] : nullptr, 4, ctl, true);
      wg::gemm_big(tk == 2 ? F.Bx + (size_t)r * 128 * DB : wsp(F, rb0 + NQKVB * 2), tk == 2 ? DB : LDO, (tk == 2 ? F.wqkvB : F.woB) + n * 256, tk == 2 ? NQKVB : DB,
                   tk == 2 ? wsp(F, rb0 + n * 512) : F.out + (size_t)r * 128 * 2 * DM + DM + n * 256, tk == 2 ? LDO : 2 * DM, 128, tk == 2 ? 0 : rbd, tk == 2 ? rbd : 0, false, lds, psig, pn, &ctl->qhead[x][0]);
      psig = &ctl->cntB[r][0]; pn = tk == 2;
    } else if (tk == 3) {
      wg_wait(lane < 2 && r - lane >= 0 ? &ctl->cntB[(r - lane) & 14][0] : nullptr, 12, ctl, false);
      const unsigned tn = attnB_32(F, pm, r * 128 + n * 32, &ctl->qhead[x][0]);
      if (tid == 0) sh[0] = tn;
      lds_barrier();
      psig = &ctl->cntAB[r][0]; pn = 1;
    } else if (tk == 5) {
      wg_wait(lane < 12 ? &ctl->prog[4 * (n & 7) + (lane & 3)][(lane >> 2) * 8] : nullptr, 4 * r + 4, ctl, false);
      const unsigned tn = attnA_block(F, pm, 4 * r + (n < 8 ? 0 : ((n >> 3) - 1) & 3), n >= 8 && n < 40 ? 4 * r + (n >> 3) : 4 * r + 4 - (n >= 40 && n < 48), n < 40 ? 1 : 2, 4 * (n & 7) + w, (u16*)(lds + w * 16384), opaque_tid() & 63, w == 0 ? &ctl->qhead[x][0] : nullptr);
      if (tid == 0) sh[0] = tn;
      lds_barrier();
    } else {
      wg_wait(lane == 0 ? &ctl->done[16] : nullptr, 64, ctl, false);
      proj_task(F, pm, r == 2, n >> 2, n & 3, lds);
      return;
    }
  }
}

__device__ __forceinline__ unsigned rtnow() { return (unsigned)__builtin_amdgcn_s_memrealtime(); }
__device__ __forceinline__ void wait_rt(unsigned t) {
  int rem = (int)(t - rtnow());
  if (rem > 20000) return;
  while (rem > 12) { __builtin_amdgcn_s_sleep(2); rem = (int)(t - rtnow()); }
  while (rem > 0) rem = (int)(t - rtnow());
}
struct Clk { unsigned f, c, t, p; };
template <int AL> __device__ __forceinline__ void wait_fine(unsigned t, Clk& k, int lat) {
  int rem = (int)(t - rtnow());
  if (rem > 20000) return;
  { const int e = (int)(k.c - k.p); if (e > -200 && e < 200) k.f += e >> 1; }
  k.p = k.c + (((t - k.t) * k.f) >> 8); k.t = t; k.c = k.p + 10;
  if (rem < 16) k.p += 1000;
  while (rem > 56) { __builtin_amdgcn_s_sleep(2); rem = (int)(t - rtnow()); }
  asm volatile(".p2align 6\n\t.rept %5\n\ts_nop 0\n\t.endr\n\t"
               "1:\n\t"
               "s_memrealtime vcc\n\t"
               "s_memtime s[98:99]\n\t"
               "s_waitcnt lgkmcnt(0)\n\t"
               "s_sub_i32 vcc_lo, %1, vcc_lo\n\t"
               "s_mul_i32 vcc_lo, vcc_lo, %2\n\t"
               "s_ashr_i32 vcc_lo, vcc_lo, 8\n\t"
               "s_add_i32 vcc_lo, vcc_lo, s98\n\t"
               "s_sub_i32 vcc_hi, vcc_lo, %0\n\t"
               "s_cmp_gt_u32 vcc_hi, %3\n\t"
               "s_cselect_b32 %0, vcc_lo, %0\n\t"
               "s_sub_i32 s98, %0, s98\n\t"
               "s_cmpk_gt_i32 s98, 230\n\t"
               "s_cbranch_scc1 1b\n\t"
               "s_sub_i32 s98, s98, %4\n\t"
               "s_max_i32 s98, s98, 0\n\t"
               "s_min_i32 s98, s98, 192\n\t"
               "s_and_b32 s98, s98, -4\n\t"
               "s_sub_i32 s98, 204, s98\n\t"
               "s_getpc_b64 vcc\n\t"
               "s_add_u32 vcc_lo, vcc_lo, s98\n\t"
               "s_addc_u32 vcc_hi, vcc_hi, 0\n\t"
               "s_setpc_b64 vcc\n\t"
               ".rept 48\n\ts_nop 0\n\t.endr" : "+s"(k.c) : "s"(t), "s"(k.f), "s"(k.f >> 6), "s"(lat), "i"(AL) : "scc", "vcc", "s98", "s99");
}
__device__ __forceinline__ bool bad4(v4u v) { return __builtin_amdgcn_ballot_w64(chk4(0u, v) == 0xffffffffu) != 0ull; }
__device__ __forceinline__ v4u repoll(__amdgpu_buffer_rsrc_t rs, int off, int soff, unsigned* tmo) {
  v4u v;
#pragma unroll 1
  for (unsigned fails = 0;; ++fails) {
    if (fails > 8) __builtin_amdgcn_s_sleep(4);
    v = __builtin_amdgcn_raw_buffer_load_b128(rs, off, soff, 16);
    if (!bad4(v) || spin_fail(fails, tmo, 1u)) break;
  }
  return v;
}
__device__ __forceinline__ int nearh(unsigned plh, int off) { return off + (int)((plh >> (off >> 21)) & 1u) * 8192; }
__device__ __forceinline__ int ownh(unsigned plh, unsigned psd, int off) { return nearh(plh, off) + (int)(((psd >> (off >> 21)) ^ (off >> 19)) & 1u) * 16384; }
#define SET_PLH do { const int o_ = sideo(plc, (lane & 31) << 21, xq != 1); plh = (unsigned)__ballot(o_ & 8192); psd = (unsigned)__ballot(o_ & 16384); asm volatile("" : "+s"(plh), "+s"(psd)); } while (0)
#define MBASE(s) ownh(plh, psd, MB0 + (s) * MREG)
#define VBASE(s) ownh(plh, psd, VB0 + (s) * OREG)
#define RBASE(s) (nearh(plh, RC0) + dbl((s) * 256))
constexpr int RSO_END = HBASE + (int)sizeof(Hdr);
constexpr int FBASE = CBASE + (int)offsetof(Ctl, prog);
#define OBASE(s) ownh(plh, psd, OB0 + (s) * OREG)

__global__ void __launch_bounds__(256, 1) k_fused(FParams P) {
  __shared__ __attribute__((aligned(16))) char Lraw[163840];
  static_assert(SW0 + NLW * 8192 <= 163840 - 256, "");
  unsigned long long pc0;
  asm volatile("s_getpc_b64 %0\nLfused_pc0:" : "=s"(pc0));
  { int clen; asm volatile("s_mov_b32 %0, Lfused_end-Lfused_pc0" : "=s"(clen));
    __builtin_amdgcn_raw_ptr_buffer_load_lds(__builtin_amdgcn_make_buffer_rsrc((void*)pc0, 0, clen, 0x00020000), (__attribute__((address_space(3))) void*)(Lraw + 163840 - 256), 4,
                                             (((blockIdx.x >> 3) & 7) * 256 + threadIdx.x) * 64, 0, 0, 0); }
  unsigned long long tok;
  if ((blockIdx.x & 7) >= 3) {
    worker(P, Lraw);
    return;
  }
  u16 *obuf = P.obuf; unsigned* tmo = &P.ctl->tmo[0]; unsigned long long* prof = P.prof; (void)prof;
  const int tid = threadIdx.x, lane = tid & 63, w = __builtin_amdgcn_readfirstlane(tid >> 6);
  const int xq = blockIdx.x & 7, slot = blockIdx.x >> 3, nq = xq < 2 ? 12 : 8;
  const int b = slot < nq ? 12 * xq + slot : NQK + 20 * xq + (slot - nq);
  const int l15 = lane & 15, lq = lane >> 4;
  const int idx = (lane & 2) ? 0xEEEE : 0x4444;
  asm volatile("" : "+s"(obuf));
  const __amdgpu_buffer_rsrc_t rs_o = __builtin_amdgcn_make_buffer_rsrc(obuf, 0, RSO_END, 0x00020000);
#define rs_m rs_o
#define rs_v rs_o
#define rs_k rs_o
  Plc plc; unsigned plh, psd;
  const int mem_lane_off = lane * 16;
  int mso[4];
#pragma unroll
  for (int k = 0; k < 4; ++k) mso[k] = quad(mpos(xq, 4 * k + w) * 1024);
  unsigned tA = 0;
  Clk clk = {23u * 256u, 0u, 0u, 1000u};
  if (b < NQK) {
    const int h = b;
    const unsigned lds0 = (unsigned)(size_t)Lraw;
    v32u A0, A1, A2, A3, A4, A5, A6, A7, V0, V1, V2, V3, V4;
    unsigned long long tk1, ep, hv, hf, h0;
    {
      const int wr_r = lane >> 2, wr_h2 = ((wr_r >> 1) & 1) | (((wr_r >> 3) & 1) << 1), rd_h2 = ((l15 >> 3) & 1) | ((lq & 1) << 1);
      v16u C, R;
#pragma unroll
      for (int u = 0; u < 4; ++u) { C[u] = ((16 * u + (lane >> 2)) * HDA + h * DHA + 32 * w + (lane & 3) * 8) * 2; C[6 + u] = lds0 + w * 8192 + (8 * lq + (l15 >> 2)) * 128 + (l15 & 3) * 8 + ((u ^ rd_h2) << 5); }
      C[4] = lds0 + w * 8192 + wr_r * 128 + (((2 * (w & 1) + ((lane & 3) >> 1)) ^ wr_h2) << 5) + (lane & 1) * 16; C[5] = C[4] ^ 64;
      C[10] = lds0 + SW0 + w * (NLW * 2048) + lane * 16; C[11] = lane * 8; C[12] = ((lane & 1) * 32 + h) * 8; C[13] = C[14] = C[15] = 0;
#pragma unroll
      for (int j = 0; j < 16; ++j) R[j] = mchunk(xq, 4 * ((j >> 1) & 3) + ((w ^ ((j >> 3) << 1)) & 2) + (j & 1)) * (64 * HDA * 2);
      asm volatile(
      "buffer_load_dwordx4 v[162:165], v226, s[40:43], s48 offen\n\t"
      "buffer_load_dwordx4 v[166:169], v227, s[40:43], s48 offen\n\t"
      "buffer_load_dwordx4 v[170:173], v228, s[40:43], s48 offen\n\t"
      "buffer_load_dwordx4 v[174:177], v229, s[40:43], s48 offen\n\t"
      "buffer_load_dwordx4 v[178:181], v226, s[40:43], s49 offen\n\t"
      "buffer_load_dwordx4 v[182:185], v227, s[40:43], s49 offen\n\t"
      "buffer_load_dwordx4 v[186:189], v228, s[40:43], s49 offen\n\t"
      "buffer_load_dwordx4 v[190:193], v229, s[40:43], s49 offen\n\t"
      "buffer_load_dwordx4 v[130:133], v226, s[44:47], s48 offen\n\t"
      "buffer_load_dwordx4 v[134:137], v227, s[44:47], s48 offen\n\t"
      "buffer_load_dwordx4 v[138:141], v228, s[44:47], s48 offen\n\t"
      "buffer_load_dwordx4 v[142:145], v229, s[44:47], s48 offen\n\t"
      "buffer_load_dwordx4 v[146:149], v226, s[44:47], s49 offen\n\t"
      "buffer_load_dwordx4 v[150:153], v227, s[44:47], s49 offen\n\t"
      "buffer_load_dwordx4 v[154:157], v228, s[44:47], s49 offen\n\t"
      "buffer_load_dwordx4 v[158:161], v229, s[44:47], s49 offen\n\t"
      "buffer_load_dwordx4 v[194:197], v226, s[40:43], s50 offen\n\t"
      "buffer_load_dwordx4 v[198:201], v227, s[40:43], s50 offen\n\t"
      "buffer_load_dwordx4 v[202:205], v228, s[40:43], s50 offen\n\t"
      "buffer_load_dwordx4 v[206:209], v229, s[40:43], s50 offen\n\t"
      "buffer_load_dwordx4 v[210:213], v226, s[40:43], s51 offen\n\t"
      "buffer_load_dwordx4 v[214:217], v227, s[40:43], s51 offen\n\t"
      "buffer_load_dwordx4 v[218:221], v228, s[40:43], s51 offen\n\t"
      "buffer_load_dwordx4 v[222:225], v229, s[40:43], s51 offen\n\t"
      "s_waitcnt vmcnt(23)\n\t"
      "ds_write_b128 v230, v[162:165] offset:0\n\t"
      "s_waitcnt vmcnt(22)\n\t"
      "ds_write_b128 v230, v[166:169] offset:2048\n\t"
      "s_waitcnt vmcnt(21)\n\t"
      "ds_write_b128 v230, v[170:173] offset:4096\n\t"
      "s_waitcnt vmcnt(20)\n\t"
      "ds_write_b128 v230, v[174:177] offset:6144\n\t"
      "s_waitcnt vmcnt(19)\n\t"
      "ds_write_b128 v231, v[178:181] offset:0\n\t"
      "s_waitcnt vmcnt(18)\n\t"
      "ds_write_b128 v231, v[182:185] offset:2048\n\t"
      "s_waitcnt vmcnt(17)\n\t"
      "ds_write_b128 v231, v[186:189] offset:4096\n\t"
      "s_waitcnt vmcnt(16)\n\t"
      "ds_write_b128 v231, v[190:193] offset:6144\n\t"
      "ds_read_b64_tr_b16 a[0:1], v232 offset:0\n\t"
      "ds_read_b64_tr_b16 a[2:3], v232 offset:512\n\t"
      "ds_read_b64_tr_b16 a[4:5], v232 offset:4096\n\t"
      "ds_read_b64_tr_b16 a[6:7], v232 offset:4608\n\t"
      "ds_read_b64_tr_b16 a[8:9], v233 offset:0\n\t"
      "ds_read_b64_tr_b16 a[10:11], v233 offset:512\n\t"
      "ds_read_b64_tr_b16 a[12:13], v233 offset:4096\n\t"
      "ds_read_b64_tr_b16 a[14:15], v233 offset:4608\n\t"
      "ds_read_b64_tr_b16 a[72:73], v234 offset:0\n\t"
      "ds_read_b64_tr_b16 a[74:75], v234 offset:512\n\t"
      "ds_read_b64_tr_b16 a[76:77], v234 offset:4096\n\t"
      "ds_read_b64_tr_b16 a[78:79], v234 offset:4608\n\t"
      "ds_read_b64_tr_b16 v[186:187], v235 offset:0\n\t"
      "ds_read_b64_tr_b16 v[188:189], v235 offset:512\n\t"
      "ds_read_b64_tr_b16 v[190:191], v235 offset:4096\n\t"
      "ds_read_b64_tr_b16 v[192:193], v235 offset:4608\n\t"
      "s_waitcnt lgkmcnt(0)\n\t"
      "ds_write_b128 v236, v[186:189] offset:6144\n\t"
      "ds_write_b128 v236, v[190:193] offset:7168\n\t"
      "s_waitcnt lgkmcnt(0)\n\t"
      "buffer_load_dwordx4 v[162:165], v226, s[44:47], s50 offen\n\t"
      "buffer_load_dwordx4 v[166:169], v227, s[44:47], s50 offen\n\t"
      "buffer_load_dwordx4 v[170:173], v228, s[44:47], s50 offen\n\t"
      "buffer_load_dwordx4 v[174:177], v229, s[44:47], s50 offen\n\t"
      "buffer_load_dwordx4 v[178:181], v226, s[44:47], s51 offen\n\t"
      "buffer_load_dwordx4 v[182:185], v227, s[44:47], s51 offen\n\t"
      "buffer_load_dwordx4 v[186:189], v228, s[44:47], s51 offen\n\t"
      "buffer_load_dwordx4 v[190:193], v229, s[44:47], s51 offen\n\t"
      "s_waitcnt vmcnt(23)\n\t"
      "ds_write_b128 v230, v[130:133] offset:0\n\t"
      "s_waitcnt vmcnt(22)\n\t"
      "ds_write_b128 v230, v[134:137] offset:2048\n\t"
      "s_waitcnt vmcnt(21)\n\t"
      "ds_write_b128 v230, v[138:141] offset:4096\n\t"
      "s_waitcnt vmcnt(20)\n\t"
      "ds_write_b128 v230, v[142:145] offset:6144\n\t"
      "s_waitcnt vmcnt(19)\n\t"
      "ds_write_b128 v231, v[146:149] offset:0\n\t"
      "s_waitcnt vmcnt(18)\n\t"
      "ds_write_b128 v231, v[150:153] offset:2048\n\t"
      "s_waitcnt vmcnt(17)\n\t"
      "ds_write_b128 v231, v[154:157] offset:4096\n\t"
      "s_waitcnt vmcnt(16)\n\t"
      "ds_write_b128 v231, v[158:161] offset:6144\n\t"
      "ds_read_b64_tr_b16 v[2:3], v232 offset:0\n\t"
      "ds_read_b64_tr_b16 v[4:5], v232 offset:512\n\t"
      "ds_read_b64_tr_b16 v[6:7], v232 offset:4096\n\t"
      "ds_read_b64_tr_b16 v[8:9], v232 offset:4608\n\t"
      "ds_read_b64_tr_b16 v[138:139], v233 offset:0\n\t"
      "ds_read_b64_tr_b16 v[140:141], v233 offset:512\n\t"
      "ds_read_b64_tr_b16 v[142:143], v233 offset:4096\n\t"
      "ds_read_b64_tr_b16 v[144:145], v233 offset:4608\n\t"
      "ds_read_b64_tr_b16 v[34:35], v234 offset:0\n\t"
      "ds_read_b64_tr_b16 v[36:37], v234 offset:512\n\t"
      "ds_read_b64_tr_b16 v[38:39], v234 offset:4096\n\t"
      "ds_read_b64_tr_b16 v[40:41], v234 offset:4608\n\t"
      "ds_read_b64_tr_b16 a[80:81], v235 offset:0\n\t"
      "ds_read_b64_tr_b16 a[82:83], v235 offset:512\n\t"
      "ds_read_b64_tr_b16 a[84:85], v235 offset:4096\n\t"
      "ds_read_b64_tr_b16 a[86:87], v235 offset:4608\n\t"
      "s_waitcnt lgkmcnt(0)\n\t"
      "ds_write_b128 v236, v[138:141] offset:0\n\t"
      "ds_write_b128 v236, v[142:145] offset:1024\n\t"
      "s_waitcnt lgkmcnt(0)\n\t"
      "buffer_load_dwordx4 v[130:133], v226, s[40:43], s52 offen\n\t"
      "buffer_load_dwordx4 v[134:137], v227, s[40:43], s52 offen\n\t"
      "buffer_load_dwordx4 v[138:141], v228, s[40:43], s52 offen\n\t"
      "buffer_load_dwordx4 v[142:145], v229, s[40:43], s52 offen\n\t"
      "buffer_load_dwordx4 v[146:149], v226, s[40:43], s53 offen\n\t"
      "buffer_load_dwordx4 v[150:153], v227, s[40:43], s53 offen\n\t"
      "buffer_load_dwordx4 v[154:157], v228, s[40:43], s53 offen\n\t"
      "buffer_load_dwordx4 v[158:161], v229, s[40:43], s53 offen\n\t"
      "s_waitcnt vmcnt(23)\n\t"
      "ds_write_b128 v230, v[194:197] offset:0\n\t"
      "s_waitcnt vmcnt(22)\n\t"
      "ds_write_b128 v230, v[198:201] offset:2048\n\t"
      "s_waitcnt vmcnt(21)\n\t"
      "ds_write_b128 v230, v[202:205] offset:4096\n\t"
      "s_waitcnt vmcnt(20)\n\t"
      "ds_write_b128 v230, v[206:209] offset:6144\n\t"
      "s_waitcnt vmcnt(19)\n\t"
      "ds_write_b128 v231, v[210:213] offset:0\n\t"
      "s_waitcnt vmcnt(18)\n\t"
      "ds_write_b128 v231, v[214:217] offset:2048\n\t"
      "s_waitcnt vmcnt(17)\n\t"
      "ds_write_b128 v231, v[218:221] offset:4096\n\t"
      "s_waitcnt vmcnt(16)\n\t"
      "ds_write_b128 v231, v[222:225] offset:6144\n\t"
      "ds_read_b64_tr_b16 a[16:17], v232 offset:0\n\t"
      "ds_read_b64_tr_b16 a[18:19], v232 offset:512\n\t"
      "ds_read_b64_tr_b16 a[20:21], v232 offset:4096\n\t"
      "ds_read_b64_tr_b16 a[22:23], v232 offset:4608\n\t"
      "ds_read_b64_tr_b16 a[24:25], v233 offset:0\n\t"
      "ds_read_b64_tr_b16 a[26:27], v233 offset:512\n\t"
      "ds_read_b64_tr_b16 a[28:29], v233 offset:4096\n\t"
      "ds_read_b64_tr_b16 a[30:31], v233 offset:4608\n\t"
      "ds_read_b64_tr_b16 a[120:121], v234 offset:0\n\t"
      "ds_read_b64_tr_b16 a[122:123], v234 offset:512\n\t"
      "ds_read_b64_tr_b16 a[124:125], v234 offset:4096\n\t"
      "ds_read_b64_tr_b16 a[126:127], v234 offset:4608\n\t"
      "ds_read_b64_tr_b16 v[218:219], v235 offset:0\n\t"
      "ds_read_b64_tr_b16 v[220:221], v235 offset:512\n\t"
      "ds_read_b64_tr_b16 v[222:223], v235 offset:4096\n\t"
      "ds_read_b64_tr_b16 v[224:225], v235 offset:4608\n\t"
      "s_waitcnt lgkmcnt(0)\n\t"
      "ds_write_b128 v236, v[218:221] offset:12288\n\t"
      "ds_write_b128 v236, v[222:225] offset:13312\n\t"
      "s_waitcnt lgkmcnt(0)\n\t"
      "buffer_load_dwordx4 v[194:197], v226, s[44:47], s52 offen\n\t"
      "buffer_load_dwordx4 v[198:201], v227, s[44:47], s52 offen\n\t"
      "buffer_load_dwordx4 v[202:205], v228, s[44:47], s52 offen\n\t"
      "buffer_load_dwordx4 v[206:209], v229, s[44:47], s52 offen\n\t"
      "buffer_load_dwordx4 v[210:213], v226, s[44:47], s53 offen\n\t"
      "buffer_load_dwordx4 v[214:217], v227, s[44:47], s53 offen\n\t"
      "buffer_load_dwordx4 v[218:221], v228, s[44:47], s53 offen\n\t"
      "buffer_load_dwordx4 v[222:225], v229, s[44:47], s53 offen\n\t"
      "s_waitcnt vmcnt(23)\n\t"
      "ds_write_b128 v230, v[162:165] offset:0\n\t"
      "s_waitcnt vmcnt(22)\n\t"
      "ds_write_b128 v230, v[166:169] offset:2048\n\t"
      "s_waitcnt vmcnt(21)\n\t"
      "ds_write_b128 v230, v[170:173] offset:4096\n\t"
      "s_waitcnt vmcnt(20)\n\t"
      "ds_write_b128 v230, v[174:177] offset:6144\n\t"
      "s_waitcnt vmcnt(19)\n\t"
      "ds_write_b128 v231, v[178:181] offset:0\n\t"
      "s_waitcnt vmcnt(18)\n\t"
      "ds_write_b128 v231, v[182:185] offset:2048\n\t"
      "s_waitcnt vmcnt(17)\n\t"
      "ds_write_b128 v231, v[186:189] offset:4096\n\t"
      "s_waitcnt vmcnt(16)\n\t"
      "ds_write_b128 v231, v[190:193] offset:6144\n\t"
      "ds_read_b64_tr_b16 v[10:11], v232 offset:0\n\t"
      "ds_read_b64_tr_b16 v[12:13], v232 offset:512\n\t"
      "ds_read_b64_tr_b16 v[14:15], v232 offset:4096\n\t"
      "ds_read_b64_tr_b16 v[16:17], v232 offset:4608\n\t"
      "ds_read_b64_tr_b16 v[170:171], v233 offset:0\n\t"
      "ds_read_b64_tr_b16 v[172:173], v233 offset:512\n\t"
      "ds_read_b64_tr_b16 v[174:175], v233 offset:4096\n\t"
      "ds_read_b64_tr_b16 v[176:177], v233 offset:4608\n\t"
      "ds_read_b64_tr_b16 v[58:59], v234 offset:0\n\t"
      "ds_read_b64_tr_b16 v[60:61], v234 offset:512\n\t"
      "ds_read_b64_tr_b16 v[62:63], v234 offset:4096\n\t"
      "ds_read_b64_tr_b16 v[64:65], v234 offset:4608\n\t"
      "ds_read_b64_tr_b16 a[128:129], v235 offset:0\n\t"
      "ds_read_b64_tr_b16 a[130:131], v235 offset:512\n\t"
      "ds_read_b64_tr_b16 a[132:133], v235 offset:4096\n\t"
      "ds_read_b64_tr_b16 a[134:135], v235 offset:4608\n\t"
      "s_waitcnt lgkmcnt(0)\n\t"
      "ds_write_b128 v236, v[170:173] offset:2048\n\t"
      "ds_write_b128 v236, v[174:177] offset:3072\n\t"
      "s_waitcnt lgkmcnt(0)\n\t"
      "buffer_load_dwordx4 v[162:165], v226, s[40:43], s54 offen\n\t"
      "buffer_load_dwordx4 v[166:169], v227, s[40:43], s54 offen\n\t"
      "buffer_load_dwordx4 v[170:173], v228, s[40:43], s54 offen\n\t"
      "buffer_load_dwordx4 v[174:177], v229, s[40:43], s54 offen\n\t"
      "buffer_load_dwordx4 v[178:181], v226, s[40:43], s55 offen\n\t"
      "buffer_load_dwordx4 v[182:185], v227, s[40:43], s55 offen\n\t"
      "buffer_load_dwordx4 v[186:189], v228, s[40:43], s55 offen\n\t"
      "buffer_load_dwordx4 v[190:193], v229, s[40:43], s55 offen\n\t"
      "s_waitcnt vmcnt(23)\n\t"
      "ds_write_b128 v230, v[130:133] offset:0\n\t"
      "s_waitcnt vmcnt(22)\n\t"
      "ds_write_b128 v230, v[134:137] offset:2048\n\t"
      "s_waitcnt vmcnt(21)\n\t"
      "ds_write_b128 v230, v[138:141] offset:4096\n\t"
      "s_waitcnt vmcnt(20)\n\t"
      "ds_write_b128 v230, v[142:145] offset:6144\n\t"
      "s_waitcnt vmcnt(19)\n\t"
      "ds_write_b128 v231, v[146:149] offset:0\n\t"
      "s_waitcnt vmcnt(18)\n\t"
      "ds_write_b128 v231, v[150:153] offset:2048\n\t"
      "s_waitcnt vmcnt(17)\n\t"
      "ds_write_b128 v231, v[154:157] offset:4096\n\t"
      "s_waitcnt vmcnt(16)\n\t"
      "ds_write_b128 v231, v[158:161] offset:6144\n\t"
      "ds_read_b64_tr_b16 a[32:33], v232 offset:0\n\t"
      "ds_read_b64_tr_b16 a[34:35], v232 offset:512\n\t"
      "ds_read_b64_tr_b16 a[36:37], v232 offset:4096\n\t"
      "ds_read_b64_tr_b16 a[38:39], v232 offset:4608\n\t"
      "ds_read_b64_tr_b16 a[40:41], v233 offset:0\n\t"
      "ds_read_b64_tr_b16 a[42:43], v233 offset:512\n\t"
      "ds_read_b64_tr_b16 a[44:45], v233 offset:4096\n\t"
      "ds_read_b64_tr_b16 a[46:47], v233 offset:4608\n\t"
      "ds_read_b64_tr_b16 a[152:153], v234 offset:0\n\t"
      "ds_read_b64_tr_b16 a[154:155], v234 offset:512\n\t"
      "ds_read_b64_tr_b16 a[156:157], v234 offset:4096\n\t"
      "ds_read_b64_tr_b16 a[158:159], v234 offset:4608\n\t"
      "ds_read_b64_tr_b16 v[74:75], v235 offset:0\n\t"
      "ds_read_b64_tr_b16 v[76:77], v235 offset:512\n\t"
      "ds_read_b64_tr_b16 v[78:79], v235 offset:4096\n\t"
      "ds_read_b64_tr_b16 v[80:81], v235 offset:4608\n\t"
      "s_waitcnt lgkmcnt(0)\n\t"
      "buffer_load_dwordx4 v[130:133], v226, s[44:47], s54 offen\n\t"
      "buffer_load_dwordx4 v[134:137], v227, s[44:47], s54 offen\n\t"
      "buffer_load_dwordx4 v[138:141], v228, s[44:47], s54 offen\n\t"
      "buffer_load_dwordx4 v[142:145], v229, s[44:47], s54 offen\n\t"
      "buffer_load_dwordx4 v[146:149], v226, s[44:47], s55 offen\n\t"
      "buffer_load_dwordx4 v[150:153], v227, s[44:47], s55 offen\n\t"
      "buffer_load_dwordx4 v[154:157], v228, s[44:47], s55 offen\n\t"
      "buffer_load_dwordx4 v[158:161], v229, s[44:47], s55 offen\n\t"
      "s_waitcnt vmcnt(23)\n\t"
      "ds_write_b128 v230, v[194:197] offset:0\n\t"
      "s_waitcnt vmcnt(22)\n\t"
      "ds_write_b128 v230, v[198:201] offset:2048\n\t"
      "s_waitcnt vmcnt(21)\n\t"
      "ds_write_b128 v230, v[202:205] offset:4096\n\t"
      "s_waitcnt vmcnt(20)\n\t"
      "ds_write_b128 v230, v[206:209] offset:6144\n\t"
      "s_waitcnt vmcnt(19)\n\t"
      "ds_write_b128 v231, v[210:213] offset:0\n\t"
      "s_waitcnt vmcnt(18)\n\t"
      "ds_write_b128 v231, v[214:217] offset:2048\n\t"
      "s_waitcnt vmcnt(17)\n\t"
      "ds_write_b128 v231, v[218:221] offset:4096\n\t"
      "s_waitcnt vmcnt(16)\n\t"
      "ds_write_b128 v231, v[222:225] offset:6144\n\t"
      "ds_read_b64_tr_b16 v[18:19], v232 offset:0\n\t"
      "ds_read_b64_tr_b16 v[20:21], v232 offset:512\n\t"
      "ds_read_b64_tr_b16 v[22:23], v232 offset:4096\n\t"
      "ds_read_b64_tr_b16 v[24:25], v232 offset:4608\n\t"
      "ds_read_b64_tr_b16 v[202:203], v233 offset:0\n\t"
      "ds_read_b64_tr_b16 v[204:205], v233 offset:512\n\t"
      "ds_read_b64_tr_b16 v[206:207], v233 offset:4096\n\t"
      "ds_read_b64_tr_b16 v[208:209], v233 offset:4608\n\t"
      "ds_read_b64_tr_b16 a[208:209], v234 offset:0\n\t"
      "ds_read_b64_tr_b16 a[210:211], v234 offset:512\n\t"
      "ds_read_b64_tr_b16 a[212:213], v234 offset:4096\n\t"
      "ds_read_b64_tr_b16 a[214:215], v234 offset:4608\n\t"
      "ds_read_b64_tr_b16 v[218:219], v235 offset:0\n\t"
      "ds_read_b64_tr_b16 v[220:221], v235 offset:512\n\t"
      "ds_read_b64_tr_b16 v[222:223], v235 offset:4096\n\t"
      "ds_read_b64_tr_b16 v[224:225], v235 offset:4608\n\t"
      "s_waitcnt lgkmcnt(0)\n\t"
      "ds_write_b128 v236, v[202:205] offset:4096\n\t"
      "ds_write_b128 v236, v[206:209] offset:5120\n\t"
      "ds_write_b128 v236, v[218:221] offset:20480\n\t"
      "ds_write_b128 v236, v[222:225] offset:21504\n\t"
      "s_waitcnt lgkmcnt(0)\n\t"
      "buffer_load_dwordx4 v[194:197], v226, s[40:43], s56 offen\n\t"
      "buffer_load_dwordx4 v[198:201], v227, s[40:43], s56 offen\n\t"
      "buffer_load_dwordx4 v[202:205], v228, s[40:43], s56 offen\n\t"
      "buffer_load_dwordx4 v[206:209], v229, s[40:43], s56 offen\n\t"
      "buffer_load_dwordx4 v[210:213], v226, s[40:43], s57 offen\n\t"
      "buffer_load_dwordx4 v[214:217], v227, s[40:43], s57 offen\n\t"
      "buffer_load_dwordx4 v[218:221], v228, s[40:43], s57 offen\n\t"
      "buffer_load_dwordx4 v[222:225], v229, s[40:43], s57 offen\n\t"
      "s_waitcnt vmcnt(23)\n\t"
      "ds_write_b128 v230, v[162:165] offset:0\n\t"
      "s_waitcnt vmcnt(22)\n\t"
      "ds_write_b128 v230, v[166:169] offset:2048\n\t"
      "s_waitcnt vmcnt(21)\n\t"
      "ds_write_b128 v230, v[170:173] offset:4096\n\t"
      "s_waitcnt vmcnt(20)\n\t"
      "ds_write_b128 v230, v[174:177] offset:6144\n\t"
      "s_waitcnt vmcnt(19)\n\t"
      "ds_write_b128 v231, v[178:181] offset:0\n\t"
      "s_waitcnt vmcnt(18)\n\t"
      "ds_write_b128 v231, v[182:185] offset:2048\n\t"
      "s_waitcnt vmcnt(17)\n\t"
      "ds_write_b128 v231, v[186:189] offset:4096\n\t"
      "s_waitcnt vmcnt(16)\n\t"
      "ds_write_b128 v231, v[190:193] offset:6144\n\t"
      "ds_read_b64_tr_b16 a[48:49], v232 offset:0\n\t"
      "ds_read_b64_tr_b16 a[50:51], v232 offset:512\n\t"
      "ds_read_b64_tr_b16 a[52:53], v232 offset:4096\n\t"
      "ds_read_b64_tr_b16 a[54:55], v232 offset:4608\n\t"
      "ds_read_b64_tr_b16 a[56:57], v233 offset:0\n\t"
      "ds_read_b64_tr_b16 a[58:59], v233 offset:512\n\t"
      "ds_read_b64_tr_b16 a[60:61], v233 offset:4096\n\t"
      "ds_read_b64_tr_b16 a[62:63], v233 offset:4608\n\t"
      "ds_read_b64_tr_b16 a[176:177], v234 offset:0\n\t"
      "ds_read_b64_tr_b16 a[178:179], v234 offset:512\n\t"
      "ds_read_b64_tr_b16 a[180:181], v234 offset:4096\n\t"
      "ds_read_b64_tr_b16 a[182:183], v234 offset:4608\n\t"
      "ds_read_b64_tr_b16 a[184:185], v235 offset:0\n\t"
      "ds_read_b64_tr_b16 a[186:187], v235 offset:512\n\t"
      "ds_read_b64_tr_b16 a[188:189], v235 offset:4096\n\t"
      "ds_read_b64_tr_b16 a[190:191], v235 offset:4608\n\t"
      "s_waitcnt lgkmcnt(0)\n\t"
      "buffer_load_dwordx4 v[162:165], v226, s[44:47], s56 offen\n\t"
      "buffer_load_dwordx4 v[166:169], v227, s[44:47], s56 offen\n\t"
      "buffer_load_dwordx4 v[170:173], v228, s[44:47], s56 offen\n\t"
      "buffer_load_dwordx4 v[174:177], v229, s[44:47], s56 offen\n\t"
      "buffer_load_dwordx4 v[178:181], v226, s[44:47], s57 offen\n\t"
      "buffer_load_dwordx4 v[182:185], v227, s[44:47], s57 offen\n\t"
      "buffer_load_dwordx4 v[186:189], v228, s[44:47], s57 offen\n\t"
      "buffer_load_dwordx4 v[190:193], v229, s[44:47], s57 offen\n\t"
      "s_waitcnt vmcnt(23)\n\t"
      "ds_write_b128 v230, v[130:133] offset:0\n\t"
      "s_waitcnt vmcnt(22)\n\t"
      "ds_write_b128 v230, v[134:137] offset:2048\n\t"
      "s_waitcnt vmcnt(21)\n\t"
      "ds_write_b128 v230, v[138:141] offset:4096\n\t"
      "s_waitcnt vmcnt(20)\n\t"
      "ds_write_b128 v230, v[142:145] offset:6144\n\t"
      "s_waitcnt vmcnt(19)\n\t"
      "ds_write_b128 v231, v[146:149] offset:0\n\t"
      "s_waitcnt vmcnt(18)\n\t"
      "ds_write_b128 v231, v[150:153] offset:2048\n\t"
      "s_waitcnt vmcnt(17)\n\t"
      "ds_write_b128 v231, v[154:157] offset:4096\n\t"
      "s_waitcnt vmcnt(16)\n\t"
      "ds_write_b128 v231, v[158:161] offset:6144\n\t"
      "ds_read_b64_tr_b16 v[26:27], v232 offset:0\n\t"
      "ds_read_b64_tr_b16 v[28:29], v232 offset:512\n\t"
      "ds_read_b64_tr_b16 v[30:31], v232 offset:4096\n\t"
      "ds_read_b64_tr_b16 v[32:33], v232 offset:4608\n\t"
      "ds_read_b64_tr_b16 a[64:65], v233 offset:0\n\t"
      "ds_read_b64_tr_b16 a[66:67], v233 offset:512\n\t"
      "ds_read_b64_tr_b16 a[68:69], v233 offset:4096\n\t"
      "ds_read_b64_tr_b16 a[70:71], v233 offset:4608\n\t"
      "ds_read_b64_tr_b16 v[114:115], v234 offset:0\n\t"
      "ds_read_b64_tr_b16 v[116:117], v234 offset:512\n\t"
      "ds_read_b64_tr_b16 v[118:119], v234 offset:4096\n\t"
      "ds_read_b64_tr_b16 v[120:121], v234 offset:4608\n\t"
      "ds_read_b64_tr_b16 a[240:241], v235 offset:0\n\t"
      "ds_read_b64_tr_b16 a[242:243], v235 offset:512\n\t"
      "ds_read_b64_tr_b16 a[244:245], v235 offset:4096\n\t"
      "ds_read_b64_tr_b16 a[246:247], v235 offset:4608\n\t"
      "s_waitcnt lgkmcnt(0)\n\t"
      "buffer_load_dwordx4 v[130:133], v226, s[40:43], s58 offen\n\t"
      "buffer_load_dwordx4 v[134:137], v227, s[40:43], s58 offen\n\t"
      "buffer_load_dwordx4 v[138:141], v228, s[40:43], s58 offen\n\t"
      "buffer_load_dwordx4 v[142:145], v229, s[40:43], s58 offen\n\t"
      "buffer_load_dwordx4 v[146:149], v226, s[40:43], s59 offen\n\t"
      "buffer_load_dwordx4 v[150:153], v227, s[40:43], s59 offen\n\t"
      "buffer_load_dwordx4 v[154:157], v228, s[40:43], s59 offen\n\t"
      "buffer_load_dwordx4 v[158:161], v229, s[40:43], s59 offen\n\t"
      "s_waitcnt vmcnt(23)\n\t"
      "ds_write_b128 v230, v[194:197] offset:0\n\t"
      "s_waitcnt vmcnt(22)\n\t"
      "ds_write_b128 v230, v[198:201] offset:2048\n\t"
      "s_waitcnt vmcnt(21)\n\t"
      "ds_write_b128 v230, v[202:205] offset:4096\n\t"
      "s_waitcnt vmcnt(20)\n\t"
      "ds_write_b128 v230, v[206:209] offset:6144\n\t"
      "s_waitcnt vmcnt(19)\n\t"
      "ds_write_b128 v231, v[210:213] offset:0\n\t"
      "s_waitcnt vmcnt(18)\n\t"
      "ds_write_b128 v231, v[214:217] offset:2048\n\t"
      "s_waitcnt vmcnt(17)\n\t"
      "ds_write_b128 v231, v[218:221] offset:4096\n\t"
      "s_waitcnt vmcnt(16)\n\t"
      "ds_write_b128 v231, v[222:225] offset:6144\n\t"
      "ds_read_b64_tr_b16 a[88:89], v232 offset:0\n\t"
      "ds_read_b64_tr_b16 a[90:91], v232 offset:512\n\t"
      "ds_read_b64_tr_b16 a[92:93], v232 offset:4096\n\t"
      "ds_read_b64_tr_b16 a[94:95], v232 offset:4608\n\t"
      "ds_read_b64_tr_b16 v[202:203], v233 offset:0\n\t"
      "ds_read_b64_tr_b16 v[204:205], v233 offset:512\n\t"
      "ds_read_b64_tr_b16 v[206:207], v233 offset:4096\n\t"
      "ds_read_b64_tr_b16 v[208:209], v233 offset:4608\n\t"
      "ds_read_b64_tr_b16 a[104:105], v234 offset:0\n\t"
      "ds_read_b64_tr_b16 a[106:107], v234 offset:512\n\t"
      "ds_read_b64_tr_b16 a[108:109], v234 offset:4096\n\t"
      "ds_read_b64_tr_b16 a[110:111], v234 offset:4608\n\t"
      "ds_read_b64_tr_b16 v[218:219], v235 offset:0\n\t"
      "ds_read_b64_tr_b16 v[220:221], v235 offset:512\n\t"
      "ds_read_b64_tr_b16 v[222:223], v235 offset:4096\n\t"
      "ds_read_b64_tr_b16 v[224:225], v235 offset:4608\n\t"
      "s_waitcnt lgkmcnt(0)\n\t"
      "ds_write_b128 v236, v[202:205] offset:8192\n\t"
      "ds_write_b128 v236, v[206:209] offset:9216\n\t"
      "ds_write_b128 v236, v[218:221] offset:10240\n\t"
      "ds_write_b128 v236, v[222:225] offset:11264\n\t"
      "s_waitcnt lgkmcnt(0)\n\t"
      "buffer_load_dwordx4 v[194:197], v226, s[44:47], s58 offen\n\t"
      "buffer_load_dwordx4 v[198:201], v227, s[44:47], s58 offen\n\t"
      "buffer_load_dwordx4 v[202:205], v228, s[44:47], s58 offen\n\t"
      "buffer_load_dwordx4 v[206:209], v229, s[44:47], s58 offen\n\t"
      "buffer_load_dwordx4 v[210:213], v226, s[44:47], s59 offen\n\t"
      "buffer_load_dwordx4 v[214:217], v227, s[44:47], s59 offen\n\t"
      "buffer_load_dwordx4 v[218:221], v228, s[44:47], s59 offen\n\t"
      "buffer_load_dwordx4 v[222:225], v229, s[44:47], s59 offen\n\t"
      "s_waitcnt vmcnt(23)\n\t"
      "ds_write_b128 v230, v[162:165] offset:0\n\t"
      "s_waitcnt vmcnt(22)\n\t"
      "ds_write_b128 v230, v[166:169] offset:2048\n\t"
      "s_waitcnt vmcnt(21)\n\t"
      "ds_write_b128 v230, v[170:173] offset:4096\n\t"
      "s_waitcnt vmcnt(20)\n\t"
      "ds_write_b128 v230, v[174:177] offset:6144\n\t"
      "s_waitcnt vmcnt(19)\n\t"
      "ds_write_b128 v231, v[178:181] offset:0\n\t"
      "s_waitcnt vmcnt(18)\n\t"
      "ds_write_b128 v231, v[182:185] offset:2048\n\t"
      "s_waitcnt vmcnt(17)\n\t"
      "ds_write_b128 v231, v[186:189] offset:4096\n\t"
      "s_waitcnt vmcnt(16)\n\t"
      "ds_write_b128 v231, v[190:193] offset:6144\n\t"
      "ds_read_b64_tr_b16 v[42:43], v232 offset:0\n\t"
      "ds_read_b64_tr_b16 v[44:45], v232 offset:512\n\t"
      "ds_read_b64_tr_b16 v[46:47], v232 offset:4096\n\t"
      "ds_read_b64_tr_b16 v[48:49], v232 offset:4608\n\t"
      "ds_read_b64_tr_b16 a[96:97], v233 offset:0\n\t"
      "ds_read_b64_tr_b16 a[98:99], v233 offset:512\n\t"
      "ds_read_b64_tr_b16 a[100:101], v233 offset:4096\n\t"
      "ds_read_b64_tr_b16 a[102:103], v233 offset:4608\n\t"
      "ds_read_b64_tr_b16 v[50:51], v234 offset:0\n\t"
      "ds_read_b64_tr_b16 v[52:53], v234 offset:512\n\t"
      "ds_read_b64_tr_b16 v[54:55], v234 offset:4096\n\t"
      "ds_read_b64_tr_b16 v[56:57], v234 offset:4608\n\t"
      "ds_read_b64_tr_b16 a[112:113], v235 offset:0\n\t"
      "ds_read_b64_tr_b16 a[114:115], v235 offset:512\n\t"
      "ds_read_b64_tr_b16 a[116:117], v235 offset:4096\n\t"
      "ds_read_b64_tr_b16 a[118:119], v235 offset:4608\n\t"
      "s_waitcnt lgkmcnt(0)\n\t"
      "buffer_load_dwordx4 v[162:165], v226, s[40:43], s60 offen\n\t"
      "buffer_load_dwordx4 v[166:169], v227, s[40:43], s60 offen\n\t"
      "buffer_load_dwordx4 v[170:173], v228, s[40:43], s60 offen\n\t"
      "buffer_load_dwordx4 v[174:177], v229, s[40:43], s60 offen\n\t"
      "buffer_load_dwordx4 v[178:181], v226, s[40:43], s61 offen\n\t"
      "buffer_load_dwordx4 v[182:185], v227, s[40:43], s61 offen\n\t"
      "buffer_load_dwordx4 v[186:189], v228, s[40:43], s61 offen\n\t"
      "buffer_load_dwordx4 v[190:193], v229, s[40:43], s61 offen\n\t"
      "s_waitcnt vmcnt(23)\n\t"
      "ds_write_b128 v230, v[130:133] offset:0\n\t"
      "s_waitcnt vmcnt(22)\n\t"
      "ds_write_b128 v230, v[134:137] offset:2048\n\t"
      "s_waitcnt vmcnt(21)\n\t"
      "ds_write_b128 v230, v[138:141] offset:4096\n\t"
      "s_waitcnt vmcnt(20)\n\t"
      "ds_write_b128 v230, v[142:145] offset:6144\n\t"
      "s_waitcnt vmcnt(19)\n\t"
      "ds_write_b128 v231, v[146:149] offset:0\n\t"
      "s_waitcnt vmcnt(18)\n\t"
      "ds_write_b128 v231, v[150:153] offset:2048\n\t"
      "s_waitcnt vmcnt(17)\n\t"
      "ds_write_b128 v231, v[154:157] offset:4096\n\t"
      "s_waitcnt vmcnt(16)\n\t"
      "ds_write_b128 v231, v[158:161] offset:6144\n\t"
      "ds_read_b64_tr_b16 a[136:137], v232 offset:0\n\t"
      "ds_read_b64_tr_b16 a[138:139], v232 offset:512\n\t"
      "ds_read_b64_tr_b16 a[140:141], v232 offset:4096\n\t"
      "ds_read_b64_tr_b16 a[142:143], v232 offset:4608\n\t"
      "ds_read_b64_tr_b16 v[66:67], v233 offset:0\n\t"
      "ds_read_b64_tr_b16 v[68:69], v233 offset:512\n\t"
      "ds_read_b64_tr_b16 v[70:71], v233 offset:4096\n\t"
      "ds_read_b64_tr_b16 v[72:73], v233 offset:4608\n\t"
      "ds_read_b64_tr_b16 a[144:145], v234 offset:0\n\t"
      "ds_read_b64_tr_b16 a[146:147], v234 offset:512\n\t"
      "ds_read_b64_tr_b16 a[148:149], v234 offset:4096\n\t"
      "ds_read_b64_tr_b16 a[150:151], v234 offset:4608\n\t"
      "ds_read_b64_tr_b16 v[154:155], v235 offset:0\n\t"
      "ds_read_b64_tr_b16 v[156:157], v235 offset:512\n\t"
      "ds_read_b64_tr_b16 v[158:159], v235 offset:4096\n\t"
      "ds_read_b64_tr_b16 v[160:161], v235 offset:4608\n\t"
      "s_waitcnt lgkmcnt(0)\n\t"
      "ds_write_b128 v236, v[154:157] offset:14336\n\t"
      "ds_write_b128 v236, v[158:161] offset:15360\n\t"
      "s_waitcnt lgkmcnt(0)\n\t"
      "buffer_load_dwordx4 v[130:133], v226, s[44:47], s60 offen\n\t"
      "buffer_load_dwordx4 v[134:137], v227, s[44:47], s60 offen\n\t"
      "buffer_load_dwordx4 v[138:141], v228, s[44:47], s60 offen\n\t"
      "buffer_load_dwordx4 v[142:145], v229, s[44:47], s60 offen\n\t"
      "buffer_load_dwordx4 v[146:149], v226, s[44:47], s61 offen\n\t"
      "buffer_load_dwordx4 v[150:153], v227, s[44:47], s61 offen\n\t"
      "buffer_load_dwordx4 v[154:157], v228, s[44:47], s61 offen\n\t"
      "buffer_load_dwordx4 v[158:161], v229, s[44:47], s61 offen\n\t"
      "s_waitcnt vmcnt(23)\n\t"
      "ds_write_b128 v230, v[194:197] offset:0\n\t"
      "s_waitcnt vmcnt(22)\n\t"
      "ds_write_b128 v230, v[198:201] offset:2048\n\t"
      "s_waitcnt vmcnt(21)\n\t"
      "ds_write_b128 v230, v[202:205] offset:4096\n\t"
      "s_waitcnt vmcnt(20)\n\t"
      "ds_write_b128 v230, v[206:209] offset:6144\n\t"
      "s_waitcnt vmcnt(19)\n\t"
      "ds_write_b128 v231, v[210:213] offset:0\n\t"
      "s_waitcnt vmcnt(18)\n\t"
      "ds_write_b128 v231, v[214:217] offset:2048\n\t"
      "s_waitcnt vmcnt(17)\n\t"
      "ds_write_b128 v231, v[218:221] offset:4096\n\t"
      "s_waitcnt vmcnt(16)\n\t"
      "ds_write_b128 v231, v[222:225] offset:6144\n\t"
      "ds_read_b64_tr_b16 v[90:91], v232 offset:0\n\t"
      "ds_read_b64_tr_b16 v[92:93], v232 offset:512\n\t"
      "ds_read_b64_tr_b16 v[94:95], v232 offset:4096\n\t"
      "ds_read_b64_tr_b16 v[96:97], v232 offset:4608\n\t"
      "ds_read_b64_tr_b16 a[192:193], v233 offset:0\n\t"
      "ds_read_b64_tr_b16 a[194:195], v233 offset:512\n\t"
      "ds_read_b64_tr_b16 a[196:197], v233 offset:4096\n\t"
      "ds_read_b64_tr_b16 a[198:199], v233 offset:4608\n\t"
      "ds_read_b64_tr_b16 a[200:201], v234 offset:0\n\t"
      "ds_read_b64_tr_b16 a[202:203], v234 offset:512\n\t"
      "ds_read_b64_tr_b16 a[204:205], v234 offset:4096\n\t"
      "ds_read_b64_tr_b16 a[206:207], v234 offset:4608\n\t"
      "ds_read_b64_tr_b16 v[98:99], v235 offset:0\n\t"
      "ds_read_b64_tr_b16 v[100:101], v235 offset:512\n\t"
      "ds_read_b64_tr_b16 v[102:103], v235 offset:4096\n\t"
      "ds_read_b64_tr_b16 v[104:105], v235 offset:4608\n\t"
      "s_waitcnt lgkmcnt(0)\n\t"
      "buffer_load_dwordx4 v[194:197], v226, s[40:43], s62 offen\n\t"
      "buffer_load_dwordx4 v[198:201], v227, s[40:43], s62 offen\n\t"
      "buffer_load_dwordx4 v[202:205], v228, s[40:43], s62 offen\n\t"
      "buffer_load_dwordx4 v[206:209], v229, s[40:43], s62 offen\n\t"
      "buffer_load_dwordx4 v[210:213], v226, s[40:43], s63 offen\n\t"
      "buffer_load_dwordx4 v[214:217], v227, s[40:43], s63 offen\n\t"
      "buffer_load_dwordx4 v[218:221], v228, s[40:43], s63 offen\n\t"
      "buffer_load_dwordx4 v[222:225], v229, s[40:43], s63 offen\n\t"
      "s_waitcnt vmcnt(23)\n\t"
      "ds_write_b128 v230, v[162:165] offset:0\n\t"
      "s_waitcnt vmcnt(22)\n\t"
      "ds_write_b128 v230, v[166:169] offset:2048\n\t"
      "s_waitcnt vmcnt(21)\n\t"
      "ds_write_b128 v230, v[170:173] offset:4096\n\t"
      "s_waitcnt vmcnt(20)\n\t"
      "ds_write_b128 v230, v[174:177] offset:6144\n\t"
      "s_waitcnt vmcnt(19)\n\t"
      "ds_write_b128 v231, v[178:181] offset:0\n\t"
      "s_waitcnt vmcnt(18)\n\t"
      "ds_write_b128 v231, v[182:185] offset:2048\n\t"
      "s_waitcnt vmcnt(17)\n\t"
      "ds_write_b128 v231, v[186:189] offset:4096\n\t"
      "s_waitcnt vmcnt(16)\n\t"
      "ds_write_b128 v231, v[190:193] offset:6144\n\t"
      "ds_read_b64_tr_b16 a[160:161], v232 offset:0\n\t"
      "ds_read_b64_tr_b16 a[162:163], v232 offset:512\n\t"
      "ds_read_b64_tr_b16 a[164:165], v232 offset:4096\n\t"
      "ds_read_b64_tr_b16 a[166:167], v232 offset:4608\n\t"
      "ds_read_b64_tr_b16 a[168:169], v233 offset:0\n\t"
      "ds_read_b64_tr_b16 a[170:171], v233 offset:512\n\t"
      "ds_read_b64_tr_b16 a[172:173], v233 offset:4096\n\t"
      "ds_read_b64_tr_b16 a[174:175], v233 offset:4608\n\t"
      "ds_read_b64_tr_b16 v[82:83], v234 offset:0\n\t"
      "ds_read_b64_tr_b16 v[84:85], v234 offset:512\n\t"
      "ds_read_b64_tr_b16 v[86:87], v234 offset:4096\n\t"
      "ds_read_b64_tr_b16 v[88:89], v234 offset:4608\n\t"
      "ds_read_b64_tr_b16 v[186:187], v235 offset:0\n\t"
      "ds_read_b64_tr_b16 v[188:189], v235 offset:512\n\t"
      "ds_read_b64_tr_b16 v[190:191], v235 offset:4096\n\t"
      "ds_read_b64_tr_b16 v[192:193], v235 offset:4608\n\t"
      "s_waitcnt lgkmcnt(0)\n\t"
      "ds_write_b128 v236, v[186:189] offset:16384\n\t"
      "ds_write_b128 v236, v[190:193] offset:17408\n\t"
      "s_waitcnt lgkmcnt(0)\n\t"
      "buffer_load_dwordx4 v[162:165], v226, s[44:47], s62 offen\n\t"
      "buffer_load_dwordx4 v[166:169], v227, s[44:47], s62 offen\n\t"
      "buffer_load_dwordx4 v[170:173], v228, s[44:47], s62 offen\n\t"
      "buffer_load_dwordx4 v[174:177], v229, s[44:47], s62 offen\n\t"
      "buffer_load_dwordx4 v[178:181], v226, s[44:47], s63 offen\n\t"
      "buffer_load_dwordx4 v[182:185], v227, s[44:47], s63 offen\n\t"
      "buffer_load_dwordx4 v[186:189], v228, s[44:47], s63 offen\n\t"
      "buffer_load_dwordx4 v[190:193], v229, s[44:47], s63 offen\n\t"
      "s_waitcnt vmcnt(23)\n\t"
      "ds_write_b128 v230, v[130:133] offset:0\n\t"
      "s_waitcnt vmcnt(22)\n\t"
      "ds_write_b128 v230, v[134:137] offset:2048\n\t"
      "s_waitcnt vmcnt(21)\n\t"
      "ds_write_b128 v230, v[138:141] offset:4096\n\t"
      "s_waitcnt vmcnt(20)\n\t"
      "ds_write_b128 v230, v[142:145] offset:6144\n\t"
      "s_waitcnt vmcnt(19)\n\t"
      "ds_write_b128 v231, v[146:149] offset:0\n\t"
      "s_waitcnt vmcnt(18)\n\t"
      "ds_write_b128 v231, v[150:153] offset:2048\n\t"
      "s_waitcnt vmcnt(17)\n\t"
      "ds_write_b128 v231, v[154:157] offset:4096\n\t"
      "s_waitcnt vmcnt(16)\n\t"
      "ds_write_b128 v231, v[158:161] offset:6144\n\t"
      "ds_read_b64_tr_b16 a[216:217], v232 offset:0\n\t"
      "ds_read_b64_tr_b16 a[218:219], v232 offset:512\n\t"
      "ds_read_b64_tr_b16 a[220:221], v232 offset:4096\n\t"
      "ds_read_b64_tr_b16 a[222:223], v232 offset:4608\n\t"
      "ds_read_b64_tr_b16 v[106:107], v233 offset:0\n\t"
      "ds_read_b64_tr_b16 v[108:109], v233 offset:512\n\t"
      "ds_read_b64_tr_b16 v[110:111], v233 offset:4096\n\t"
      "ds_read_b64_tr_b16 v[112:113], v233 offset:4608\n\t"
      "ds_read_b64_tr_b16 a[224:225], v234 offset:0\n\t"
      "ds_read_b64_tr_b16 a[226:227], v234 offset:512\n\t"
      "ds_read_b64_tr_b16 a[228:229], v234 offset:4096\n\t"
      "ds_read_b64_tr_b16 a[230:231], v234 offset:4608\n\t"
      "ds_read_b64_tr_b16 a[232:233], v235 offset:0\n\t"
      "ds_read_b64_tr_b16 a[234:235], v235 offset:512\n\t"
      "ds_read_b64_tr_b16 a[236:237], v235 offset:4096\n\t"
      "ds_read_b64_tr_b16 a[238:239], v235 offset:4608\n\t"
      "s_waitcnt lgkmcnt(0)\n\t"
      "buffer_load_dwordx2 v[242:243], off, s[36:39], s64 sc1\n\t"
      "buffer_load_dwordx2 v[244:245], off, s[36:39], s64 offset:128 sc1\n\t"
      "buffer_load_dwordx2 v[246:247], v237, s[36:39], s64 offen offset:768 sc1\n\t"
      "buffer_load_dwordx2 v[248:249], v237, s[36:39], s64 offen offset:1280 sc1\n\t"
      "buffer_load_dwordx2 v[250:251], v238, s[36:39], s64 offen offset:256 sc1\n\t"
      "s_waitcnt vmcnt(20)\n\t"
      "ds_write_b128 v230, v[194:197] offset:0\n\t"
      "s_waitcnt vmcnt(19)\n\t"
      "ds_write_b128 v230, v[198:201] offset:2048\n\t"
      "s_waitcnt vmcnt(18)\n\t"
      "ds_write_b128 v230, v[202:205] offset:4096\n\t"
      "s_waitcnt vmcnt(17)\n\t"
      "ds_write_b128 v230, v[206:209] offset:6144\n\t"
      "s_waitcnt vmcnt(16)\n\t"
      "ds_write_b128 v231, v[210:213] offset:0\n\t"
      "s_waitcnt vmcnt(15)\n\t"
      "ds_write_b128 v231, v[214:217] offset:2048\n\t"
      "s_waitcnt vmcnt(14)\n\t"
      "ds_write_b128 v231, v[218:221] offset:4096\n\t"
      "s_waitcnt vmcnt(13)\n\t"
      "ds_write_b128 v231, v[222:225] offset:6144\n\t"
      "ds_read_b64_tr_b16 v[130:131], v232 offset:0\n\t"
      "ds_read_b64_tr_b16 v[132:133], v232 offset:512\n\t"
      "ds_read_b64_tr_b16 v[134:135], v232 offset:4096\n\t"
      "ds_read_b64_tr_b16 v[136:137], v232 offset:4608\n\t"
      "ds_read_b64_tr_b16 v[146:147], v233 offset:0\n\t"
      "ds_read_b64_tr_b16 v[148:149], v233 offset:512\n\t"
      "ds_read_b64_tr_b16 v[150:151], v233 offset:4096\n\t"
      "ds_read_b64_tr_b16 v[152:153], v233 offset:4608\n\t"
      "ds_read_b64_tr_b16 v[138:139], v234 offset:0\n\t"
      "ds_read_b64_tr_b16 v[140:141], v234 offset:512\n\t"
      "ds_read_b64_tr_b16 v[142:143], v234 offset:4096\n\t"
      "ds_read_b64_tr_b16 v[144:145], v234 offset:4608\n\t"
      "ds_read_b64_tr_b16 v[218:219], v235 offset:0\n\t"
      "ds_read_b64_tr_b16 v[220:221], v235 offset:512\n\t"
      "ds_read_b64_tr_b16 v[222:223], v235 offset:4096\n\t"
      "ds_read_b64_tr_b16 v[224:225], v235 offset:4608\n\t"
      "s_waitcnt lgkmcnt(0)\n\t"
      "ds_write_b128 v236, v[218:221] offset:18432\n\t"
      "ds_write_b128 v236, v[222:225] offset:19456\n\t"
      "s_waitcnt lgkmcnt(0)\n\t"
      "s_waitcnt vmcnt(12)\n\t"
      "ds_write_b128 v230, v[162:165] offset:0\n\t"
      "s_waitcnt vmcnt(11)\n\t"
      "ds_write_b128 v230, v[166:169] offset:2048\n\t"
      "s_waitcnt vmcnt(10)\n\t"
      "ds_write_b128 v230, v[170:173] offset:4096\n\t"
      "s_waitcnt vmcnt(9)\n\t"
      "ds_write_b128 v230, v[174:177] offset:6144\n\t"
      "s_waitcnt vmcnt(8)\n\t"
      "ds_write_b128 v231, v[178:181] offset:0\n\t"
      "s_waitcnt vmcnt(7)\n\t"
      "ds_write_b128 v231, v[182:185] offset:2048\n\t"
      "s_waitcnt vmcnt(6)\n\t"
      "ds_write_b128 v231, v[186:189] offset:4096\n\t"
      "s_waitcnt vmcnt(5)\n\t"
      "ds_write_b128 v231, v[190:193] offset:6144\n\t"
      "ds_read_b64_tr_b16 v[162:163], v232 offset:0\n\t"
      "ds_read_b64_tr_b16 v[164:165], v232 offset:512\n\t"
      "ds_read_b64_tr_b16 v[166:167], v232 offset:4096\n\t"
      "ds_read_b64_tr_b16 v[168:169], v232 offset:4608\n\t"
      "ds_read_b64_tr_b16 a[248:249], v233 offset:0\n\t"
      "ds_read_b64_tr_b16 a[250:251], v233 offset:512\n\t"
      "ds_read_b64_tr_b16 a[252:253], v233 offset:4096\n\t"
      "ds_read_b64_tr_b16 a[254:255], v233 offset:4608\n\t"
      "ds_read_b64_tr_b16 v[154:155], v234 offset:0\n\t"
      "ds_read_b64_tr_b16 v[156:157], v234 offset:512\n\t"
      "ds_read_b64_tr_b16 v[158:159], v234 offset:4096\n\t"
      "ds_read_b64_tr_b16 v[160:161], v234 offset:4608\n\t"
      "ds_read_b64_tr_b16 v[122:123], v235 offset:0\n\t"
      "ds_read_b64_tr_b16 v[124:125], v235 offset:512\n\t"
      "ds_read_b64_tr_b16 v[126:127], v235 offset:4096\n\t"
      "ds_read_b64_tr_b16 v[128:129], v235 offset:4608\n\t"
      "s_waitcnt lgkmcnt(0)\n\t"
      "ds_write_b128 v236, v[162:165] offset:22528\n\t"
      "ds_write_b128 v236, v[166:169] offset:23552\n\t"
      "s_waitcnt lgkmcnt(0)\n\t"
      "s_waitcnt vmcnt(0) lgkmcnt(0)\n\t"
      "s_barrier\n\t"
      : "={a[0:31]}"(A0), "={a[32:63]}"(A1), "={a[64:95]}"(A2), "={a[96:127]}"(A3), "={a[128:159]}"(A4), "={a[160:191]}"(A5), "={a[192:223]}"(A6), "={a[224:255]}"(A7),
        "={v[2:33]}"(V0), "={v[34:65]}"(V1), "={v[66:97]}"(V2), "={v[98:129]}"(V3), "={v[130:161]}"(V4),
        "={v[242:243]}"(ep), "={v[244:245]}"(tk1), "={v[246:247]}"(hv), "={v[248:249]}"(hf), "={v[250:251]}"(h0)
      : "{v[226:241]}"(C), "{s[36:39]}"(rs_o), "{s[40:43]}"(__builtin_amdgcn_make_buffer_rsrc((void*)P.Wq, 0, DM * HDA * 2, 0x00020000)), "{s[44:47]}"(__builtin_amdgcn_make_buffer_rsrc((void*)P.Wk, 0, DM * HDA * 2, 0x00020000)),
        "{s[48:63]}"(R), "{s64}"(HBASE)
      : V162_225, "memory");
    }
    {
    const int lane = opaque_tid() & 63, l15 = lane & 15, lq = lane >> 4;
    tok = uni64(ep) + 1;
    const bool setup = __all(tk1 == tok && (hv | 3) == 4 * tok + 3 && hf == tok), early = setup && __all(h0 == tok);
    if (!setup)
      for (unsigned spins = 0; spins < (1u << 16); ++spins) {
        tk1 = ld_agent(&P.hdr->token[0]); hv = ld_agent(&P.hdr->vote[lane]); hf = ld_agent(&P.hdr->fill[lane]);
        if (__all(tk1 == tok && (hv | 3) == 4 * tok + 3 && hf == tok)) break;
        __builtin_amdgcn_s_sleep(4);
      }
    plc = count_votes(hv);
    asm volatile("" ::: "memory");
    const __amdgpu_buffer_rsrc_t rs_x = __builtin_amdgcn_make_buffer_rsrc(wsp(P, QA0 + ((3 * h + 1) << 20)), 0, 2 << 20, 0x00020000);
    const int kx0o = faro(plc, QA0 + ((3 * h + 1) << 20)) & 8192, vx0o = (1 << 20) + (faro(plc, QA0 + ((3 * h + 2) << 20)) & 8192);
    v16u C, S; v4u C2; v8u S2;
    C[0] = lane * 16; C[1] = lds0 + w * 4096 + lane * 16;
#pragma unroll
    for (int q = 0; q < 3; ++q) C[2 + q] = lds0 + (w ^ (q + 1)) * 4096 + lane * 16;
    C[5] = lds0 + SW0 + w * (NLW * 2048) + lane * 16;
    C[6] = lds0 + KB0 + (w >> 1) * 4096 + (lq >> 1) * 1024 + (((l15 & 2) + (lq & 1)) * 16 + ((8 * (w & 1) + 2 * (l15 >> 2) + (l15 & 1)) ^ (4 * (l15 & 2)))) * 16;
    C[7] = lds0 + KB0 + 8192 + (lq >> 1) * 4096 + w * 1024 + (l15 & 2) * 128 + (8 * (lq & 1) + (l15 >> 2)) * 16 + (l15 & 1) * 8;
    C[8] = w * 2048 + (lane ^ ((lane >> 5) << 3)) * 16; C[9] = w * 2048 + lane * 16;
    C[10] = lds0 + T0 + w * IMGW + (lq >> 1) * 128 + l15 * 8 + (lq & 1) * 4; C[11] = lds0 + T0 + KIMG + w * IMGW + lq * 128 + (lq >> 1) * 32 + l15 * 8;
    C[12] = lds0 + T0 + (lq >> 1) * IMGW + (l15 & 3) * 128 + (l15 & 2) * 16 + (8 * (lq & 1) + (l15 >> 2)) * 8;
    C[13] = lds0 + T0 + PIMG + (w * 8 + (l15 & 7)) * 16 + lq * 4 + (l15 >> 3) * 512; C[14] = lds0 + T0 + PIMG + (lq * 8 + (l15 & 7)) * 16;
    C[15] = quad(h * 2048) + w * 512 + lane * 4;
    C2[0] = (lane & 8) ? 0x7ffffff0 : quad(h * 2048) + (w * 2 + (l15 >> 2)) * 256 + (lq >> 1) * 128 + (l15 & 2) * 32 + (lq & 1) * 32 + (l15 & 1) * 16;
    C2[1] = (lane & 2) ? 0xEEEE : 0x4444; C2[2] = __float_as_uint(24.f)  ; C2[3] = 0;
    { const unsigned long long pl2 = __ballot((sideo(plc, (lane >> 1) << 21, xq != 1) >> (13 + (lane & 1))) & 1);
      S[0] = (unsigned)pl2; S[1] = (unsigned)(pl2 >> 32); }
#pragma unroll
    for (int k = 0; k < 4; ++k) S[2 + k] = mso[k];
    S[6] = kx0o; S[7] = vx0o - kx0o; S[8] = quad(h * 2048) + (w & 1) * 1024; S[9] = 32 * w + 8192; S[10] = (slot == 0 ? 1 : 0) | (early ? 2 : 0) | xq * 4 | (h == 0 ? 16 : 0);
    S[11] = P.sh1; S[12] = w == 0 ? P.sh2 : P.gain | P.inc << 8 | P.dec << 16; S[13] = w == 0 ? P.pad2 : P.dq2 * 16 | P.dq << 20; S[14] = nearo(plc, RC0); S[15] = FBASE + h * 128;
    S2[0] = (unsigned)tok; S2[1] = (unsigned)(tok >> 32); S2[2] = HBASE + 256 + h * 8; S2[3] = lds0 + KB0 + KVB + w * 2048; S2[4] = CBASE + (int)offsetof(Ctl, tmo);
    S2[5] = (int)((char*)prof - (char*)obuf) + (5 << 17) + (b * 4 + w) * 16; S2[6] = w < 2 ? ~0u : 0u; S2[7] = ~S2[6];
#pragma unroll
    for (int k = 0; k < 16; ++k) { S[k] = __builtin_amdgcn_readfirstlane(S[k]); if (k < 8) S2[k] = __builtin_amdgcn_readfirstlane(S2[k]); }
    v4u S3 = w == 0 ? (v4u){23u * 256u, 0u, 0u, 1000u} : (v4u){P.dmv * 16u, P.dov * 16u, P.dq2 * 16u, P.dq * 16u};
    if (early)
#pragma unroll
      for (int k = 0; k < 4; ++k)
        __builtin_amdgcn_raw_ptr_buffer_load_lds(rs_x, (__attribute__((address_space(3))) void*)(Lraw + KB0 + w * 2048 + (k >> 1) * 8192 + (k & 1) * 1024), 16, C[8 + (k >> 1)], (k < 2 ? kx0o : vx0o) + (k & 1) * 1024, 0, 16);
    asm volatile(
      "s_mov_b32 s68, 0\n\t"
      "s_mov_b32 s69, 0\n\t"
      "s_mov_b32 s70, 1\n\t"
      "s_mov_b32 s17, 0\n\t"
      "s_mov_b32 s77, 16512\n\t"
      "s_mov_b32 s78, 2\n\t"
      "s_mov_b32 s79, 0x3e0293ee\n\t"
      "s_mov_b32 s80, 0\n\t"
      "v_mov_b32 v209, 0\n\t"
      "s_mov_b32 s18, 0x3f803f80\n\t"
      "s_mov_b32 s19, 0x3f803f80\n\t"
      "Lqk_top:\n\t"
      "v_mov_b32 v182, 23040\n\t"
      "s_memrealtime s[94:95]\n\t"
      "ds_read_b128 v[178:181], v182\n\t"
      "ds_read_b128 v[210:213], v239 offset:0\n\t"
      "ds_read_b128 v[214:217], v239 offset:1024\n\t"
      "ds_read_b128 v[218:221], v239 offset:2048\n\t"
      "ds_read_b128 v[222:225], v239 offset:3072\n\t"
      "ds_read_b128 v[226:229], v239 offset:4096\n\t"
      "ds_read_b128 v[230:233], v239 offset:5120\n\t"
      "s_lshl_b32 s84, s68, 16\n\t"
      "s_lshr_b32 s93, s84, 20\n\t"
      "s_and_b32 s93, s93, 62\n\t"
      "s_lshr_b64 s[98:99], s[44:45], s93\n\t"
      "s_bfe_u32 s93, s84, 0x10013\n\t"
      "s_lshl1_add_u32 s98, s93, s98\n\t"
      "s_and_b32 s98, s98, 3\n\t"
      "s_lshl_b32 s98, s98, 13\n\t"
      "s_or_b32 s84, s84, s98\n\t"
      "s_add_u32 s81, s84, s46\n\t"
      "s_add_u32 s82, s84, s47\n\t"
      "s_add_u32 s83, s84, s48\n\t"
      "s_add_u32 s84, s84, s49\n\t"
      "s_lshl_b32 s85, s68, 8\n\t"
      "s_and_b32 s93, s85, 0x2000\n\t"
      "s_add_u32 s85, s85, s93\n\t"
      "s_add_u32 s85, s85, s58\n\t"
      "v_mov_b64 v[162:163], 0\n\t"
      "v_mov_b64 v[164:165], 0\n\t"
      "v_mov_b64 v[166:167], 0\n\t"
      "v_mov_b64 v[168:169], 0\n\t"
      "v_mov_b64 v[170:171], 0\n\t"
      "v_mov_b64 v[172:173], 0\n\t"
      "v_mov_b64 v[174:175], 0\n\t"
      "v_mov_b64 v[176:177], 0\n\t"
      "s_waitcnt lgkmcnt(0)\n\t"
      "s_cmp_lg_u32 s53, 8192\n\t"
      "s_cbranch_scc1 Lqk_keep\n\t"
      "s_bitcmp1_b32 s78, 2\n\t"
      "s_cbranch_scc1 Lqk_slow\n\t"
      "s_bitcmp0_b32 s78, 0\n\t"
      "s_cbranch_scc1 Lqk_wait\n\t"
      "v_sub_u32 v179, v179, v178\n\t"
      "v_sub_u32 v180, v180, v178\n\t"
      "v_sub_u32 v181, v181, v178\n\t"
      "v_max3_i32 v179, v179, v180, v181\n\t"
      "s_add_u32 s98, s96, s76\n\t"
      "v_readfirstlane_b32 s93, v179\n\t"
      "s_sub_u32 s98, s98, s56\n\t"
      "s_max_i32 s93, s93, 0\n\t"
      "s_add_u32 s98, s98, s93\n\t"
      "s_bitcmp0_b32 s54, 0\n\t"
      "s_cbranch_scc1 Lqk_late\n\t"
      "s_add_u32 s93, s93, s16\n\t"
      "v_mov_b32 v178, s93\n\t"
      "s_mov_b64 exec, 1\n\t"
      "buffer_store_dword v178, off, s[36:39], s32 offset:128 sc1\n\t"
      "s_mov_b64 exec, -1\n\t"
      "Lqk_late:\n\t"
      "s_sub_u32 s93, s98, s69\n\t"
      "s_cmp_gt_i32 s93, 0\n\t"
      "s_cbranch_scc1 Lqk_shift\n\t"
      "Lqk_wait:\n\t"
      "s_cmp_eq_u32 s68, 0\n\t"
      "s_cbranch_scc1 Lqk_bar\n\t"
      "s_sub_u32 s94, s69, s94\n\t"
      "s_cmp_gt_i32 s94, 20000\n\t"
      "s_cbranch_scc1 Lqk_bar\n\t"
      "s_sub_u32 s93, s73, s75\n\t"
      "s_add_u32 s98, s93, 199\n\t"
      "s_ashr_i32 s93, s93, 1\n\t"
      "s_cmp_lt_u32 s98, 399\n\t"
      "s_cselect_b32 s93, s93, 0\n\t"
      "s_add_u32 s72, s72, s93\n\t"
      "s_sub_u32 s93, s69, s74\n\t"
      "s_mul_i32 s93, s93, s72\n\t"
      "s_lshr_b32 s93, s93, 8\n\t"
      "s_add_u32 s75, s93, s73\n\t"
      "s_add_u32 s73, s75, 10\n\t"
      "s_mov_b32 s74, s69\n\t"
      "s_cmp_lt_i32 s94, 16\n\t"
      "s_cselect_b32 s93, 1000, 0\n\t"
      "s_add_u32 s75, s75, s93\n\t"
      "s_lshr_b32 s93, s72, 6\n\t"
      "Lqk_slp:\n\t"
      "s_cmp_lt_i32 s94, 57\n\t"
      "s_cbranch_scc1 Lqk_fine\n\t"
      "s_sleep 2\n\t"
      "s_memrealtime s[94:95]\n\t"
      "s_waitcnt lgkmcnt(0)\n\t"
      "s_sub_u32 s94, s69, s94\n\t"
      "s_branch Lqk_slp\n\t"
      ".p2align 6\n\t"
      ".rept 2\n\t"
      "s_nop 0\n\t"
      ".endr\n\t"
      "Lqk_fine:\n\t"
      "s_memrealtime vcc\n\t"
      "s_memtime s[98:99]\n\t"
      "s_waitcnt lgkmcnt(0)\n\t"
      "s_sub_i32 vcc_lo, s69, vcc_lo\n\t"
      "s_mul_i32 vcc_lo, vcc_lo, s72\n\t"
      "s_ashr_i32 vcc_lo, vcc_lo, 8\n\t"
      "s_add_i32 vcc_lo, vcc_lo, s98\n\t"
      "s_sub_i32 vcc_hi, vcc_lo, s73\n\t"
      "s_cmp_gt_u32 vcc_hi, s93\n\t"
      "s_cselect_b32 s73, vcc_lo, s73\n\t"
      "s_sub_i32 s98, s73, s98\n\t"
      "s_cmpk_gt_i32 s98, 230\n\t"
      "s_cbranch_scc1 Lqk_fine\n\t"
      "s_sub_i32 s98, s98, s57\n\t"
      "s_max_i32 s98, s98, 0\n\t"
      "s_min_i32 s98, s98, 192\n\t"
      "s_and_b32 s98, s98, -4\n\t"
      "s_sub_i32 s98, 204, s98\n\t"
      "s_getpc_b64 vcc\n\t"
      "s_add_u32 vcc_lo, vcc_lo, s98\n\t"
      "s_addc_u32 vcc_hi, vcc_hi, 0\n\t"
      "s_setpc_b64 vcc\n\t"
      ".rept 48\n\t"
      "s_nop 0\n\t"
      ".endr\n\t"
      "Lqk_bar:\n\t"
      "s_barrier\n\t"
      "buffer_load_dwordx4 v[178:181], v234, s[36:39], s81 offen sc1\n\t"
      "s_lshl_b32 s86, s68, 18\n\t"
      "s_add_u32 s86, s86, 0x1400000\n\t"
      "s_lshr_b32 s93, s86, 20\n\t"
      "s_and_b32 s93, s93, 62\n\t"
      "s_lshr_b64 s[98:99], s[44:45], s93\n\t"
      "s_bfe_u32 s93, s86, 0x10013\n\t"
      "s_lshl1_add_u32 s98, s93, s98\n\t"
      "s_and_b32 s98, s98, 3\n\t"
      "s_lshl_b32 s98, s98, 13\n\t"
      "s_or_b32 s86, s86, s98\n\t"
      "s_xor_b32 s87, s86, 0x4000\n\t"
      "s_add_u32 s87, s87, s52\n\t"
      "buffer_load_dwordx4 v[182:185], v234, s[36:39], s82 offen sc1\n\t"
      "s_lshl_b32 s88, s68, 18\n\t"
      "s_add_u32 s88, s88, 0x400000\n\t"
      "s_lshr_b32 s93, s88, 20\n\t"
      "s_and_b32 s93, s93, 62\n\t"
      "s_lshr_b64 s[98:99], s[44:45], s93\n\t"
      "s_bfe_u32 s93, s88, 0x10013\n\t"
      "s_lshl1_add_u32 s98, s93, s98\n\t"
      "s_and_b32 s98, s98, 3\n\t"
      "s_lshl_b32 s98, s98, 13\n\t"
      "s_or_b32 s88, s88, s98\n\t"
      "s_xor_b32 s89, s88, 0x4000\n\t"
      "buffer_load_dwordx4 v[186:189], v234, s[36:39], s83 offen sc1\n\t"
      "s_nop 7\n\t"
      "s_and_b32 s78, s78, 14\n\t"
      "s_mov_b32 s34, s69\n\t"
      "buffer_load_dwordx4 v[190:193], v234, s[36:39], s84 offen sc1\n\t"
      "s_waitcnt vmcnt(4)\n\t"
      "v_readlane_b32 s93, v209, 0\n\t"
      "v_readlane_b32 s92, v209, 4\n\t"
      "s_min_u32 s93, s93, s92\n\t"
      "s_max_u32 s70, s70, s93\n\t"
      "s_add_u32 s90, s68, 1\n\t"
      "s_cmp_lt_u32 s90, s70\n\t"
      "s_cselect_b32 s90, s90, s68\n\t"
      "s_cselect_b32 s93, 16, 0\n\t"
      "s_or_b32 s78, s78, s93\n\t"
      "s_lshl_b32 s90, s90, 14\n\t"
      "s_add_u32 s90, s90, s50\n\t"
      "s_add_u32 s91, s90, s51\n\t"
      "s_mov_b32 m0, s63\n\t"
      "s_waitcnt vmcnt(3)\n\t"
      "ds_write_b128 v235, v[178:181] offset:0\n\t"
      "v_smfmac_f32_16x16x64_bf16 v[162:165], v[178:181], a[0:7], v251\n\t"
      "v_smfmac_f32_16x16x64_bf16 v[166:169], v[178:181], a[8:15], v251\n\t"
      "v_smfmac_f32_16x16x64_bf16 v[170:173], v[178:181], v[2:9], v251\n\t"
      "v_smfmac_f32_16x16x64_bf16 v[174:177], v[178:181], v[210:217], v251\n\t"
      "s_waitcnt vmcnt(2)\n\t"
      "v_smfmac_f32_16x16x64_bf16 v[162:165], v[182:185], a[16:23], v251\n\t"
      "ds_write_b128 v235, v[182:185] offset:1024\n\t"
      "v_smfmac_f32_16x16x64_bf16 v[166:169], v[182:185], a[24:31], v251\n\t"
      "v_smfmac_f32_16x16x64_bf16 v[170:173], v[182:185], v[10:17], v251\n\t"
      "ds_read_b128 v[210:213], v239 offset:6144\n\t"
      "v_smfmac_f32_16x16x64_bf16 v[174:177], v[182:185], v[218:225], v251\n\t"
      "s_waitcnt lgkmcnt(2)\n\t"
      "s_barrier\n\t"
      "ds_read_b128 v[194:197], v236 offset:0\n\t"
      "s_waitcnt vmcnt(1)\n\t"
      "v_smfmac_f32_16x16x64_bf16 v[162:165], v[186:189], a[32:39], v251\n\t"
      "ds_write_b128 v235, v[186:189] offset:2048\n\t"
      "v_smfmac_f32_16x16x64_bf16 v[166:169], v[186:189], a[40:47], v251\n\t"
      "ds_read_b128 v[214:217], v239 offset:7168\n\t"
      "v_smfmac_f32_16x16x64_bf16 v[170:173], v[186:189], v[18:25], v251\n\t"
      "ds_read_b128 v[198:201], v237 offset:0\n\t"
      "v_smfmac_f32_16x16x64_bf16 v[174:177], v[186:189], v[226:233], v251\n\t"
      "ds_read_b128 v[218:221], v239 offset:8192\n\t"
      "s_waitcnt vmcnt(0)\n\t"
      "v_smfmac_f32_16x16x64_bf16 v[162:165], v[190:193], a[48:55], v251\n\t"
      "ds_write_b128 v235, v[190:193] offset:3072\n\t"
      "v_smfmac_f32_16x16x64_bf16 v[166:169], v[190:193], a[56:63], v251\n\t"
      "ds_read_b128 v[222:225], v239 offset:9216\n\t"
      "v_smfmac_f32_16x16x64_bf16 v[170:173], v[190:193], v[26:33], v251\n\t"
      "ds_read_b128 v[202:205], v238 offset:0\n\t"
      "v_smfmac_f32_16x16x64_bf16 v[174:177], v[190:193], a[64:71], v251\n\t"
      "s_waitcnt lgkmcnt(7)\n\t"
      "v_smfmac_f32_16x16x64_bf16 v[162:165], v[194:197], a[72:79], v251\n\t"
      "ds_read_b128 v[226:229], v239 offset:10240\n\t"
      "s_waitcnt lgkmcnt(6)\n\t"
      "v_smfmac_f32_16x16x64_bf16 v[166:169], v[194:197], v[210:217], v251\n\t"
      "s_waitcnt lgkmcnt(3)\n\t"
      "s_barrier\n\t"
      "ds_read_b128 v[206:209], v236 offset:1024\n\t"
      "v_smfmac_f32_16x16x64_bf16 v[170:173], v[194:197], v[34:41], v251\n\t"
      "ds_read_b128 v[230:233], v239 offset:11264\n\t"
      "v_smfmac_f32_16x16x64_bf16 v[174:177], v[194:197], a[80:87], v251\n\t"
      "v_smfmac_f32_16x16x64_bf16 v[162:165], v[198:201], a[88:95], v251\n\t"
      "s_waitcnt lgkmcnt(4)\n\t"
      "v_smfmac_f32_16x16x64_bf16 v[166:169], v[198:201], v[218:225], v251\n\t"
      "ds_read_b128 v[210:213], v239 offset:12288\n\t"
      "v_smfmac_f32_16x16x64_bf16 v[170:173], v[198:201], v[42:49], v251\n\t"
      "ds_read_b128 v[214:217], v239 offset:13312\n\t"
      "v_smfmac_f32_16x16x64_bf16 v[174:177], v[198:201], a[96:103], v251\n\t"
      "s_waitcnt lgkmcnt(5)\n\t"
      "v_smfmac_f32_16x16x64_bf16 v[162:165], v[202:205], a[104:111], v251\n\t"
      "ds_read_b128 v[178:181], v237 offset:1024\n\t"
      "s_waitcnt lgkmcnt(3)\n\t"
      "v_smfmac_f32_16x16x64_bf16 v[166:169], v[202:205], v[226:233], v251\n\t"
      "ds_read_b128 v[218:221], v239 offset:14336\n\t"
      "v_smfmac_f32_16x16x64_bf16 v[170:173], v[202:205], v[50:57], v251\n\t"
      "ds_read_b128 v[182:185], v238 offset:1024\n\t"
      "v_smfmac_f32_16x16x64_bf16 v[174:177], v[202:205], a[112:119], v251\n\t"
      "ds_read_b128 v[222:225], v239 offset:15360\n\t"
      "v_smfmac_f32_16x16x64_bf16 v[162:165], v[206:209], a[120:127], v251\n\t"
      "ds_read_b128 v[186:189], v236 offset:2048\n\t"
      "s_waitcnt lgkmcnt(5)\n\t"
      "v_smfmac_f32_16x16x64_bf16 v[166:169], v[206:209], v[210:217], v251\n\t"
      "v_smfmac_f32_16x16x64_bf16 v[170:173], v[206:209], v[58:65], v251\n\t"
      "ds_read_b128 v[190:193], v237 offset:2048\n\t"
      "v_smfmac_f32_16x16x64_bf16 v[174:177], v[206:209], a[128:135], v251\n\t"
      "ds_read_b128 v[226:229], v239 offset:16384\n\t"
      "s_waitcnt lgkmcnt(6)\n\t"
      "v_smfmac_f32_16x16x64_bf16 v[162:165], v[178:181], a[136:143], v251\n\t"
      "ds_read_b128 v[194:197], v238 offset:2048\n\t"
      "v_smfmac_f32_16x16x64_bf16 v[166:169], v[178:181], v[66:73], v251\n\t"
      "ds_read_b128 v[230:233], v239 offset:17408\n\t"
      "s_waitcnt lgkmcnt(6)\n\t"
      "v_smfmac_f32_16x16x64_bf16 v[162:165], v[182:185], a[144:151], v251\n\t"
      "ds_read_b128 v[198:201], v236 offset:3072\n\t"
      "s_waitcnt lgkmcnt(6)\n\t"
      "v_smfmac_f32_16x16x64_bf16 v[166:169], v[182:185], v[218:225], v251\n\t"
      "ds_read_b128 v[202:205], v237 offset:3072\n\t"
      "s_waitcnt lgkmcnt(6)\n\t"
      "v_smfmac_f32_16x16x64_bf16 v[162:165], v[186:189], a[152:159], v251\n\t"
      "ds_read_b128 v[206:209], v238 offset:3072\n\t"
      "v_smfmac_f32_16x16x64_bf16 v[166:169], v[186:189], v[74:81], v251\n\t"
      "s_waitcnt lgkmcnt(6)\n\t"
      "v_smfmac_f32_16x16x64_bf16 v[162:165], v[190:193], a[160:167], v251\n\t"
      "v_smfmac_f32_16x16x64_bf16 v[166:169], v[190:193], a[168:175], v251\n\t"
      "ds_read_b128 v[210:213], v239 offset:18432\n\t"
      "s_waitcnt lgkmcnt(5)\n\t"
      "v_smfmac_f32_16x16x64_bf16 v[162:165], v[194:197], v[82:89], v251\n\t"
      "ds_read_b128 v[214:217], v239 offset:19456\n\t"
      "s_waitcnt lgkmcnt(5)\n\t"
      "v_smfmac_f32_16x16x64_bf16 v[166:169], v[194:197], v[226:233], v251\n\t"
      "s_waitcnt lgkmcnt(4)\n\t"
      "v_smfmac_f32_16x16x64_bf16 v[162:165], v[198:201], a[176:183], v251\n\t"
      "s_waitcnt lgkmcnt(3)\n\t"
      "v_smfmac_f32_16x16x64_bf16 v[162:165], v[202:205], v[130:137], v251\n\t"
      "s_waitcnt lgkmcnt(2)\n\t"
      "v_smfmac_f32_16x16x64_bf16 v[162:165], v[206:209], v[138:145], v251\n\t"
      "v_smfmac_f32_16x16x64_bf16 v[166:169], v[198:201], a[184:191], v251\n\t"
      "ds_read_b128 v[218:221], v239 offset:20480\n\t"
      "v_smfmac_f32_16x16x64_bf16 v[166:169], v[202:205], v[146:153], v251\n\t"
      "ds_read_b128 v[222:225], v239 offset:21504\n\t"
      "v_add_f32 v162, v162, v164\n\t"
      "s_waitcnt lgkmcnt(2)\n\t"
      "v_smfmac_f32_16x16x64_bf16 v[166:169], v[206:209], v[210:217], v251\n\t"
      "v_add_f32 v163, v163, v165\n\t"
      "v_smfmac_f32_16x16x64_bf16 v[170:173], v[178:181], v[90:97], v251\n\t"
      "v_cvt_pk_bf16_f32 v164, v162, v163\n\t"
      "v_smfmac_f32_16x16x64_bf16 v[174:177], v[178:181], a[192:199], v251\n\t"
      "ds_read_b128 v[226:229], v239 offset:22528\n\t"
      "v_add_f32 v166, v166, v168\n\t"
      "v_add_f32 v167, v167, v169\n\t"
      "v_smfmac_f32_16x16x64_bf16 v[170:173], v[182:185], a[200:207], v251\n\t"
      "ds_read_b128 v[230:233], v239 offset:23552\n\t"
      "v_cvt_pk_bf16_f32 v165, v166, v167\n\t"
      "ds_write2_b32 v244, v164, v165 offset1:72\n\t"
      "v_smfmac_f32_16x16x64_bf16 v[174:177], v[182:185], v[98:105], v251\n\t"
      "ds_read_b128 v[178:181], v240 offset:0\n\t"
      "v_cmp_u_f32 vcc, v162, v163\n\t"
      "v_smfmac_f32_16x16x64_bf16 v[170:173], v[186:189], a[208:215], v251\n\t"
      "ds_read_b128 v[182:185], v240 offset:2048\n\t"
      "s_waitcnt lgkmcnt(5)\n\t"
      "v_smfmac_f32_16x16x64_bf16 v[174:177], v[186:189], v[218:225], v251\n\t"
      "s_cbranch_vccnz Lqk_nan\n\t"
      "Lqk_res:\n\t"
      "s_waitcnt lgkmcnt(2)\n\t"
      "s_barrier\n\t"
      "ds_read_b64_tr_b16 v[162:163], v246 offset:0\n\t"
      "ds_read_b64_tr_b16 v[164:165], v246 offset:32\n\t"
      "v_smfmac_f32_16x16x64_bf16 v[170:173], v[190:193], a[216:223], v251\n\t"
      "ds_read_b64_tr_b16 v[166:167], v246 offset:1152\n\t"
      "ds_read_b64_tr_b16 v[168:169], v246 offset:1184\n\t"
      "v_smfmac_f32_16x16x64_bf16 v[174:177], v[190:193], v[106:113], v251\n\t"
      "v_mov_b32 v191, 0\n\t"
      "v_smfmac_f32_16x16x64_bf16 v[170:173], v[194:197], a[224:231], v251\n\t"
      "v_mov_b32 v193, 0\n\t"
      "v_smfmac_f32_16x16x64_bf16 v[174:177], v[194:197], a[232:239], v251\n\t"
      "buffer_load_dword v190, v249, s[36:39], s86 offen sc1\n\t"
      "buffer_load_dword v192, v249, s[36:39], s86 offen offset:256 sc1\n\t"
      "s_waitcnt lgkmcnt(2)\n\t"
      "v_mfma_f32_16x16x32_bf16 v[186:189], v[178:181], v[162:165], 0\n\t"
      "v_smfmac_f32_16x16x64_bf16 v[170:173], v[198:201], v[114:121], v251\n\t"
      "buffer_load_dwordx2 v[196:197], off, s[36:39], s85 offset:8 sc1\n\t"
      "s_waitcnt lgkmcnt(0)\n\t"
      "v_mfma_f32_16x16x32_bf16 v[186:189], v[182:185], v[166:169], v[186:189]\n\t"
      "v_smfmac_f32_16x16x64_bf16 v[170:173], v[202:205], v[226:233], v251\n\t"
      "buffer_load_dwordx4 v242, s[40:43], s90 offen offset:0 sc1 lds\n\t"
      "v_mov_b32 v195, 0\n\t"
      "v_smfmac_f32_16x16x64_bf16 v[170:173], v[206:209], v[154:161], v251\n\t"
      "v_add_f32_dpp v253, v188, v186 row_ror:8 row_mask:0xf bank_mask:0x3\n\t"
      "v_add_f32_dpp v253, v187, v189 row_ror:8 row_mask:0xf bank_mask:0xc\n\t"
      "v_smfmac_f32_16x16x64_bf16 v[174:177], v[198:201], a[240:247], v251\n\t"
      "ds_read_b64_tr_b16 v[178:179], v241 offset:0\n\t"
      "ds_read_b64_tr_b16 v[180:181], v241 offset:64\n\t"
      "buffer_load_dwordx4 v242, s[40:43], s90 offen offset:1024 sc1 lds\n\t"
      "v_fma_f32 v253, v253, s79, -v252\n\t"
      "v_exp_f32 v186, v253\n\t"
      "v_smfmac_f32_16x16x64_bf16 v[174:177], v[202:205], a[248:255], v251\n\t"
      "s_add_u32 m0, s63, 8192\n\t"
      "v_add_f32 v170, v170, v172\n\t"
      "v_smfmac_f32_16x16x64_bf16 v[174:177], v[206:209], v[122:129], v251\n\t"
      "ds_read_b64_tr_b16 v[182:183], v241 offset:512\n\t"
      "ds_read_b64_tr_b16 v[184:185], v241 offset:576\n\t"
      "v_mov_b32_dpp v187, v186 row_ror:8 row_mask:0xf bank_mask:0xf\n\t"
      "v_cvt_pk_bf16_f32 v186, v186, v187\n\t"
      "ds_write_b32 v247, v186\n\t"
      "v_add_f32 v171, v171, v173\n\t"
      "v_cvt_pk_bf16_f32 v172, v170, v171\n\t"
      "v_mov_b64 v[198:199], s[18:19]\n\t"
      "v_mov_b64 v[200:201], s[18:19]\n\t"
      "v_add_f32 v174, v174, v176\n\t"
      "v_add_f32 v175, v175, v177\n\t"
      "v_cvt_pk_bf16_f32 v173, v174, v175\n\t"
      "ds_write_b64 v245, v[172:173]\n\t"
      "buffer_load_dwordx4 v243, s[40:43], s91 offen offset:0 sc1 lds\n\t"
      "s_bitcmp1_b32 s78, 1\n\t"
      "s_cbranch_scc1 Lqk_first\n\t"
      "Lqk_first_r:\n\t"
      "s_waitcnt lgkmcnt(0)\n\t"
      "s_barrier\n\t"
      "ds_read_b64_tr_b16 v[210:211], v246 offset:2304\n\t"
      "ds_read_b64_tr_b16 v[212:213], v246 offset:2336\n\t"
      "ds_read_b64_tr_b16 v[214:215], v246 offset:3456\n\t"
      "ds_read_b64_tr_b16 v[216:217], v246 offset:3488\n\t"
      "ds_read_b128 v[218:221], v248\n\t"
      "s_cmp_eq_u32 s68, 63\n\t"
      "s_cbranch_scc1 Lqk_last\n\t"
      "buffer_load_dwordx4 v243, s[40:43], s91 offen offset:1024 sc1 lds\n\t"
      "s_mov_b64 exec, 17\n\t"
      "buffer_load_dword v209, v234, s[36:39], s59 offen sc1\n\t"
      "s_mov_b64 exec, -1\n\t"
      "s_waitcnt lgkmcnt(3)\n\t"
      "v_mfma_f32_16x16x32_bf16 v[222:225], v[210:213], v[162:165], 0\n\t"
      "s_waitcnt lgkmcnt(1)\n\t"
      "v_mfma_f32_16x16x32_bf16 v[222:225], v[214:217], v[166:169], v[222:225]\n\t"
      "s_waitcnt lgkmcnt(0)\n\t"
      "v_mfma_f32_16x16x32_bf16 v[226:229], v[178:181], v[218:221], 0\n\t"
      "v_mfma_f32_16x16x32_bf16 v[230:233], v[182:185], v[218:221], 0\n\t"
      "v_add_f32_dpp v205, v224, v222 row_ror:8 row_mask:0xf bank_mask:0x3\n\t"
      "v_add_f32_dpp v205, v223, v225 row_ror:8 row_mask:0xf bank_mask:0xc\n\t"
      "v_fma_f32 v205, v205, s79, -v252\n\t"
      "v_exp_f32 v202, v205\n\t"
      "v_mfma_f32_16x16x32_bf16 v[170:173], v[198:201], v[218:221], 0\n\t"
      "s_waitcnt vmcnt(6)\n\t"
      "v_max_u32 v204, v190, v192\n\t"
      "v_mov_b32_dpp v203, v202 row_ror:8 row_mask:0xf bank_mask:0xf\n\t"
      "v_cvt_pk_bf16_f32 v194, v202, v203\n\t"
      "v_cmp_eq_u32 s[94:95], -1, v204\n\t"
      "s_nop 0\n\t"
      "v_mfma_f32_16x16x16_bf16 v[170:173], v[198:199], v[194:195], v[170:173]\n\t"
      "v_mfma_f32_16x16x16_bf16 v[226:229], v[190:191], v[194:195], v[226:229]\n\t"
      "v_mfma_f32_16x16x16_bf16 v[230:233], v[192:193], v[194:195], v[230:233]\n\t"
      "v_rcp_f32 v202, v170\n\t"
      "v_subrev_u32 v203, 0x2b800000, v170\n\t"
      "v_cmp_lt_u32 vcc, 0x14000000, v203\n\t"
      "s_nop 0\n\t"
      "v_mul_f32 v226, v226, v202\n\t"
      "v_mul_f32 v227, v227, v202\n\t"
      "v_mul_f32 v228, v228, v202\n\t"
      "v_mul_f32 v229, v229, v202\n\t"
      "v_cvt_pk_bf16_f32 v174, v226, v227\n\t"
      "v_cvt_pk_bf16_f32 v175, v228, v229\n\t"
      "v_mul_f32 v230, v230, v202\n\t"
      "v_mul_f32 v231, v231, v202\n\t"
      "v_mul_f32 v232, v232, v202\n\t"
      "v_mul_f32 v233, v233, v202\n\t"
      "s_or_b64 s[94:95], s[94:95], vcc\n\t"
      "v_cvt_pk_bf16_f32 v176, v230, v231\n\t"
      "v_cvt_pk_bf16_f32 v177, v232, v233\n\t"
      "s_cbranch_scc1 Lqk_bad\n\t"
      "Lqk_pub:\n\t"
      "buffer_store_dwordx4 v[174:177], v250, s[36:39], s88 offen sc1\n\t"
      "s_memrealtime s[96:97]\n\t"
      "v_log_f32 v202, v170\n\t"
      "v_add_u32 v240, s77, v240\n\t"
      "v_add_f32 v252, v252, v202\n\t"
      "v_floor_f32 v252, v252\n\t"
      "v_add_f32 v252, 0x41800000, v252\n\t"
      "v_min_f32 v252, 0x44000000, v252\n\t"
      "v_max_f32 v252, 0xc4000000, v252\n\t"
      "v_add_u32 v241, s77, v241\n\t"
      "s_nop 0\n\t"
      "buffer_store_dwordx4 v[174:177], v250, s[36:39], s89 offen sc1\n\t"
      "v_mov_b32_dpp v252, v252 row_ror:8 row_mask:0xf bank_mask:0xc\n\t"
      "s_waitcnt vmcnt(7)\n\t"
      "v_readfirstlane_b32 s93, v196\n\t"
      "v_readfirstlane_b32 s76, v197\n\t"
      "s_lshr_b32 s98, s53, 3\n\t"
      "s_add_u32 s98, s98, 22016\n\t"
      "v_mov_b32 v203, s98\n\t"
      "s_max_u32 s98, s93, s76\n\t"
      "s_cmp_eq_u32 s98, -1\n\t"
      "s_cbranch_scc1 Lqk_rec\n\t"
      "Lqk_rec_r:\n\t"
      "s_add_u32 s69, s69, s93\n\t"
      "s_add_u32 s69, s69, s76\n\t"
      "s_and_b32 s32, s54, 12\n\t"
      "s_add_u32 s32, s32, s85\n\t"
      "s_waitcnt lgkmcnt(0)\n\t"
      "v_mov_b32 v202, s96\n\t"
      "ds_write_b32 v203, v202\n\t"
      "s_sub_u32 s16, s96, s34\n\t"
      "s_min_u32 s16, s16, 0x4000\n\t"
      "s_cmp_lg_u32 s80, 0\n\t"
      "s_cselect_b32 s16, 0x4000, s16\n\t"
      "s_mov_b64 exec, s[66:67]\n\t"
      "s_bitcmp1_b32 s53, 5\n\t"
      "s_cbranch_scc1 Lqk_km1\n\t"
      "buffer_store_dwordx4 v[210:213], v234, s[36:39], s87 offen sc1\n\t"
      "s_branch Lqk_km2\n\t"
      "Lqk_km1:\n\t"
      "buffer_store_dwordx4 v[214:217], v234, s[36:39], s87 offen sc1\n\t"
      "Lqk_km2:\n\t"
      "s_mov_b64 exec, -1\n\t"
      "s_bitcmp0_b32 s78, 4\n\t"
      "s_cbranch_scc1 Lqk_kvl\n\t"
      "Lqk_kvl_r:\n\t"
      "s_add_u32 s93, s96, 70\n\t"
      "s_cmp_eq_u32 s68, 0\n\t"
      "s_cselect_b32 s69, s93, s69\n\t"
      "s_cselect_b32 s78, 5, 1\n\t"
      "s_sub_u32 s63, s63, s77\n\t"
      "s_sub_u32 s77, 0, s77\n\t"
      "s_add_u32 s68, s68, 1\n\t"
      "s_mov_b32 s80, 0\n\t"
      "s_waitcnt lgkmcnt(0)\n\t"
      "s_barrier\n\t"
      "s_branch Lqk_top\n\t"
      "Lqk_shift:\n\t"
      "s_add_u32 s69, s69, s55\n\t"
      "s_branch Lqk_late\n\t"
      "Lqk_keep:\n\t"
      "s_and_b32 s93, s54, s78\n\t"
      "s_bitcmp0_b32 s93, 0\n\t"
      "s_cbranch_scc1 Lqk_bar\n\t"
      "s_cmp_lg_u32 s53, 8288\n\t"
      "s_cbranch_scc1 Lqk_bar\n\t"
      "s_bitcmp1_b32 s54, 4\n\t"
      "s_cbranch_scc0 Lqk_bar\n\t"
      "s_cmp_lt_u32 s68, 3\n\t"
      "s_cbranch_scc1 Lqk_keep2\n\t"
      "s_sub_u32 s69, s68, 2\n\t"
      "s_lshl_b32 s69, s69, 8\n\t"
      "s_and_b32 s93, s69, 0x2000\n\t"
      "s_add_u32 s69, s69, s93\n\t"
      "s_add_u32 s69, s69, s58\n\t"
      "buffer_load_dwordx4 v[178:181], off, s[36:39], s69 offset:128 sc1\n\t"
      "buffer_load_dwordx4 v[182:185], off, s[36:39], s69 offset:144 sc1\n\t"
      "s_mov_b32 s86, -1\n\t"
      "s_mov_b32 s87, -1\n\t"
      "s_mov_b32 s88, 0\n\t"
      "s_waitcnt vmcnt(0)\n\t"
      "s_lshr_b32 s89, s72, 4\n\t"
      "s_add_u32 s89, s89, 8\n\t"
      "v_readfirstlane_b32 s93, v178\n\t"
      "s_cmp_gt_u32 s93, s89\n\t"
      "s_addc_u32 s88, s88, 0\n\t"
      "s_cmp_gt_u32 s93, s89\n\t"
      "s_cselect_b32 s93, -1, s93\n\t"
      "s_max_i32 s86, s86, s93\n\t"
      "v_readfirstlane_b32 s93, v179\n\t"
      "s_cmp_gt_u32 s93, s89\n\t"
      "s_addc_u32 s88, s88, 0\n\t"
      "s_cmp_gt_u32 s93, s89\n\t"
      "s_cselect_b32 s93, -1, s93\n\t"
      "s_max_i32 s86, s86, s93\n\t"
      "v_readfirstlane_b32 s93, v180\n\t"
      "s_cmp_gt_u32 s93, s89\n\t"
      "s_addc_u32 s88, s88, 0\n\t"
      "s_cmp_gt_u32 s93, s89\n\t"
      "s_cselect_b32 s93, -1, s93\n\t"
      "s_max_i32 s86, s86, s93\n\t"
      "s_mov_b32 s90, s88\n\t"
      "s_lshr_b32 s89, s73, 4\n\t"
      "s_add_u32 s89, s89, 8\n\t"
      "v_readfirstlane_b32 s93, v182\n\t"
      "s_cmp_gt_u32 s93, s89\n\t"
      "s_addc_u32 s88, s88, 0\n\t"
      "s_cmp_gt_u32 s93, s89\n\t"
      "s_cselect_b32 s93, -1, s93\n\t"
      "s_max_i32 s87, s87, s93\n\t"
      "v_readfirstlane_b32 s93, v183\n\t"
      "s_cmp_gt_u32 s93, s89\n\t"
      "s_addc_u32 s88, s88, 0\n\t"
      "s_cmp_gt_u32 s93, s89\n\t"
      "s_cselect_b32 s93, -1, s93\n\t"
      "s_max_i32 s87, s87, s93\n\t"
      "v_readfirstlane_b32 s93, v184\n\t"
      "s_cmp_gt_u32 s93, s89\n\t"
      "s_addc_u32 s88, s88, 0\n\t"
      "s_cmp_gt_u32 s93, s89\n\t"
      "s_cselect_b32 s93, -1, s93\n\t"
      "s_max_i32 s87, s87, s93\n\t"
      "s_cmp_eq_u32 s68, 3\n\t"
      "s_cselect_b32 s88, s90, s88\n\t"
      "s_cselect_b32 s87, -1, s87\n\t"
      "s_bfe_u32 s91, s56, 0x80008\n\t"
      "s_bfe_u32 s92, s56, 0x80010\n\t"
      "s_and_b32 s90, s56, 255\n\t"
      "s_cmp_lt_u32 s68, 8\n\t"
      "s_cselect_b32 s90, 10, s90\n\t"
      "s_sub_u32 s92, 0, s92\n\t"
      "s_cmp_lg_u32 s88, 0\n\t"
      "s_cselect_b32 s91, s91, s92\n\t"
      "s_cselect_b32 s88, 32, 0\n\t"
      "s_add_u32 s74, s74, s91\n\t"
      "s_bfe_u32 s93, s57, 0x100000\n\t"
      "s_max_i32 s74, s74, s93\n\t"
      "s_min_i32 s74, s74, 960\n\t"
      "s_lshl_b32 s93, s86, 4\n\t"
      "s_sub_u32 s93, s72, s93\n\t"
      "s_sub_u32 s93, s93, s74\n\t"
      "s_mul_i32 s89, s93, s90\n\t"
      "s_ashr_i32 s89, s89, 4\n\t"
      "s_min_i32 s93, s93, s89\n\t"
      "s_max_i32 s93, s93, -128\n\t"
      "s_cmp_lt_i32 s86, 0\n\t"
      "s_cselect_b32 s93, 0, s93\n\t"
      "s_sub_u32 s72, s72, s93\n\t"
      "s_and_b32 s93, s88, s17\n\t"
      "s_add_u32 s72, s72, s93\n\t"
      "s_add_u32 s75, s75, s91\n\t"
      "s_bfe_u32 s93, s57, 0x100010\n\t"
      "s_max_i32 s75, s75, s93\n\t"
      "s_min_i32 s75, s75, 960\n\t"
      "s_lshl_b32 s93, s87, 4\n\t"
      "s_sub_u32 s93, s73, s93\n\t"
      "s_sub_u32 s93, s93, s75\n\t"
      "s_mul_i32 s89, s93, s90\n\t"
      "s_ashr_i32 s89, s89, 4\n\t"
      "s_min_i32 s93, s93, s89\n\t"
      "s_max_i32 s93, s93, -128\n\t"
      "s_cmp_lt_i32 s87, 0\n\t"
      "s_cselect_b32 s93, 0, s93\n\t"
      "s_sub_u32 s73, s73, s93\n\t"
      "s_and_b32 s93, s88, s17\n\t"
      "s_add_u32 s73, s73, s93\n\t"
      "s_mov_b32 s17, s88\n\t"
      "Lqk_keep2:\n\t"
      "s_lshr_b32 s93, s72, 4\n\t"
      "v_mov_b32 v178, s93\n\t"
      "s_lshr_b32 s93, s73, 4\n\t"
      "v_mov_b32 v179, s93\n\t"
      "s_add_u32 s93, s68, 1\n\t"
      "s_lshl_b32 s93, s93, 8\n\t"
      "s_and_b32 s93, s93, 0x3fff\n\t"
      "s_and_b32 s89, s93, 0x2000\n\t"
      "s_add_u32 s93, s93, s89\n\t"
      "s_add_u32 s93, s93, s58\n\t"
      "s_mov_b64 exec, 1\n\t"
      "buffer_store_dwordx2 v[178:179], off, s[36:39], s93 offset:8 sc1\n\t"
      "s_mov_b64 exec, -1\n\t"
      "s_nop 1\n\t"
      "s_branch Lqk_bar\n\t"
      "Lqk_slow:\n\t"
      "s_memrealtime s[94:95]\n\t"
      "s_waitcnt lgkmcnt(0)\n\t"
      "s_sub_u32 s94, s69, s94\n\t"
      "s_sub_u32 s94, s94, 1\n\t"
      "s_cmp_lt_u32 s94, 20000\n\t"
      "s_cbranch_scc1 Lqk_slow\n\t"
      "s_mov_b32 s71, 0\n\t"
      "Lqk_slow2:\n\t"
      "buffer_load_dword v178, off, s[36:39], s85 sc1\n\t"
      "s_waitcnt vmcnt(0)\n\t"
      "v_readfirstlane_b32 s69, v178\n\t"
      "s_memrealtime s[94:95]\n\t"
      "s_waitcnt lgkmcnt(0)\n\t"
      "s_cmp_lg_u32 s69, -1\n\t"
      "s_cbranch_scc1 Lqk_wait\n\t"
      "s_add_u32 s71, s71, 1\n\t"
      "s_and_b32 s93, s71, 31\n\t"
      "s_cmp_lg_u32 s93, 31\n\t"
      "s_cbranch_scc1 Lqk_sp1\n\t"
      "s_cmp_lt_u32 s71, 0x8000\n\t"
      "s_cbranch_scc1 Lqk_sp1b\n\t"
      "v_mov_b32 v178, 1\n\t"
      "buffer_store_dword v178, off, s[36:39], s64 sc1\n\t"
      "s_branch Lqk_bar\n\t"
      "Lqk_sp1b:\n\t"
      "buffer_load_dword v178, off, s[36:39], s64 sc1\n\t"
      "s_waitcnt vmcnt(0)\n\t"
      "v_readfirstlane_b32 s93, v178\n\t"
      "s_cmp_lg_u32 s93, 0\n\t"
      "s_cbranch_scc1 Lqk_bar\n\t"
      "Lqk_sp1:\n\t"
      "s_sleep 2\n\t"
      "s_branch Lqk_slow2\n\t"
      "Lqk_nan:\n\t"
      "s_bitcmp1_b32 s78, 3\n\t"
      "s_cbranch_scc1 Lqk_res\n\t"
      "s_nop 7\n\t"
      "s_waitcnt vmcnt(0) lgkmcnt(0)\n\t"
      "s_mov_b32 s71, s80\n\t"
      "s_add_u32 s71, s71, 1\n\t"
      "s_and_b32 s93, s71, 31\n\t"
      "s_cmp_lg_u32 s93, 31\n\t"
      "s_cbranch_scc1 Lqk_sp2\n\t"
      "s_cmp_lt_u32 s71, 0x8000\n\t"
      "s_cbranch_scc1 Lqk_sp2b\n\t"
      "v_mov_b32 v178, 1\n\t"
      "buffer_store_dword v178, off, s[36:39], s64 sc1\n\t"
      "s_branch Lqk_end\n\t"
      "Lqk_sp2b:\n\t"
      "buffer_load_dword v178, off, s[36:39], s64 sc1\n\t"
      "s_waitcnt vmcnt(0)\n\t"
      "v_readfirstlane_b32 s93, v178\n\t"
      "s_cmp_lg_u32 s93, 0\n\t"
      "s_cbranch_scc1 Lqk_end\n\t"
      "Lqk_sp2:\n\t"
      "s_mov_b32 s80, s71\n\t"
      "s_and_b32 s78, s78, 2\n\t"
      "s_add_u32 s69, s69, s55\n\t"
      "s_cmp_lt_u32 s80, 2\n\t"
      "s_cbranch_scc1 Lqk_nan2\n\t"
      "v_mov_b32 v253, 0\n\t"
      "ds_read_b128 v[178:181], v234 offset:0\n\t"
      "ds_read_b128 v[182:185], v234 offset:1024\n\t"
      "ds_read_b128 v[186:189], v234 offset:2048\n\t"
      "ds_read_b128 v[190:193], v234 offset:3072\n\t"
      "ds_read_b128 v[194:197], v234 offset:4096\n\t"
      "ds_read_b128 v[198:201], v234 offset:5120\n\t"
      "ds_read_b128 v[202:205], v234 offset:6144\n\t"
      "ds_read_b128 v[206:209], v234 offset:7168\n\t"
      "s_waitcnt lgkmcnt(0)\n\t"
      "v_max3_u32 v253, v253, v178, v179\n\t"
      "v_max3_u32 v253, v253, v180, v181\n\t"
      "v_max3_u32 v253, v253, v182, v183\n\t"
      "v_max3_u32 v253, v253, v184, v185\n\t"
      "v_max3_u32 v253, v253, v186, v187\n\t"
      "v_max3_u32 v253, v253, v188, v189\n\t"
      "v_max3_u32 v253, v253, v190, v191\n\t"
      "v_max3_u32 v253, v253, v192, v193\n\t"
      "v_max3_u32 v253, v253, v194, v195\n\t"
      "v_max3_u32 v253, v253, v196, v197\n\t"
      "v_max3_u32 v253, v253, v198, v199\n\t"
      "v_max3_u32 v253, v253, v200, v201\n\t"
      "v_max3_u32 v253, v253, v202, v203\n\t"
      "v_max3_u32 v253, v253, v204, v205\n\t"
      "v_max3_u32 v253, v253, v206, v207\n\t"
      "v_max3_u32 v253, v253, v208, v209\n\t"
      "ds_read_b128 v[178:181], v234 offset:8192\n\t"
      "ds_read_b128 v[182:185], v234 offset:9216\n\t"
      "ds_read_b128 v[186:189], v234 offset:10240\n\t"
      "ds_read_b128 v[190:193], v234 offset:11264\n\t"
      "ds_read_b128 v[194:197], v234 offset:12288\n\t"
      "ds_read_b128 v[198:201], v234 offset:13312\n\t"
      "ds_read_b128 v[202:205], v234 offset:14336\n\t"
      "ds_read_b128 v[206:209], v234 offset:15360\n\t"
      "s_waitcnt lgkmcnt(0)\n\t"
      "v_max3_u32 v253, v253, v178, v179\n\t"
      "v_max3_u32 v253, v253, v180, v181\n\t"
      "v_max3_u32 v253, v253, v182, v183\n\t"
      "v_max3_u32 v253, v253, v184, v185\n\t"
      "v_max3_u32 v253, v253, v186, v187\n\t"
      "v_max3_u32 v253, v253, v188, v189\n\t"
      "v_max3_u32 v253, v253, v190, v191\n\t"
      "v_max3_u32 v253, v253, v192, v193\n\t"
      "v_max3_u32 v253, v253, v194, v195\n\t"
      "v_max3_u32 v253, v253, v196, v197\n\t"
      "v_max3_u32 v253, v253, v198, v199\n\t"
      "v_max3_u32 v253, v253, v200, v201\n\t"
      "v_max3_u32 v253, v253, v202, v203\n\t"
      "v_max3_u32 v253, v253, v204, v205\n\t"
      "v_max3_u32 v253, v253, v206, v207\n\t"
      "v_max3_u32 v253, v253, v208, v209\n\t"
      "v_cmp_eq_u32 vcc, -1, v253\n\t"
      "s_cbranch_vccnz Lqk_nan2\n\t"
      "s_bitset1_b32 s78, 3\n\t"
      "Lqk_nan2:\n\t"
      "v_mov_b32 v209, 0\n\t"
      "s_barrier\n\t"
      "s_branch Lqk_top\n\t"
      "Lqk_first:\n\t"
      "s_bitset0_b32 s78, 1\n\t"
      "s_bitcmp1_b32 s54, 1\n\t"
      "s_cbranch_scc1 Lqk_first3\n\t"
      "s_mov_b32 s71, 0\n\t"
      "Lqk_first1:\n\t"
      "buffer_load_dwordx2 v[210:211], off, s[36:39], s62 sc1\n\t"
      "buffer_load_dwordx2 v[212:213], off, s[36:39], s62 offset:256 sc1\n\t"
      "s_waitcnt vmcnt(0)\n\t"
      "v_cmp_eq_u64 vcc, s[60:61], v[210:211]\n\t"
      "v_cmp_eq_u64 s[94:95], s[60:61], v[212:213]\n\t"
      "s_nop 1\n\t"
      "s_and_b64 vcc, vcc, s[94:95]\n\t"
      "s_cbranch_scc1 Lqk_first2\n\t"
      "s_add_u32 s71, s71, 1\n\t"
      "s_and_b32 s93, s71, 31\n\t"
      "s_cmp_lg_u32 s93, 31\n\t"
      "s_cbranch_scc1 Lqk_sp3\n\t"
      "s_cmp_lt_u32 s71, 0x8000\n\t"
      "s_cbranch_scc1 Lqk_sp3b\n\t"
      "v_mov_b32 v210, 3\n\t"
      "buffer_store_dword v210, off, s[36:39], s64 sc1\n\t"
      "s_branch Lqk_first2\n\t"
      "Lqk_sp3b:\n\t"
      "buffer_load_dword v210, off, s[36:39], s64 sc1\n\t"
      "s_waitcnt vmcnt(0)\n\t"
      "v_readfirstlane_b32 s93, v210\n\t"
      "s_cmp_lg_u32 s93, 0\n\t"
      "s_cbranch_scc1 Lqk_first2\n\t"
      "Lqk_sp3:\n\t"
      "s_sleep 4\n\t"
      "s_branch Lqk_first1\n\t"
      "Lqk_first2:\n\t"
      "s_sub_u32 s63, s63, s77\n\t"
      "s_mov_b32 m0, s63\n\t"
      "s_nop 0\n\t"
      "buffer_load_dwordx4 v242, s[40:43], s90 offen offset:0 sc1 lds\n\t"
      "buffer_load_dwordx4 v242, s[40:43], s90 offen offset:1024 sc1 lds\n\t"
      "s_add_u32 m0, s63, 8192\n\t"
      "s_nop 0\n\t"
      "buffer_load_dwordx4 v243, s[40:43], s91 offen offset:0 sc1 lds\n\t"
      "buffer_load_dwordx4 v243, s[40:43], s91 offen offset:1024 sc1 lds\n\t"
      "s_add_u32 s63, s63, s77\n\t"
      "s_add_u32 m0, s63, 8192\n\t"
      "Lqk_first3:\n\t"
      "s_waitcnt vmcnt(0) lgkmcnt(0)\n\t"
      "s_barrier\n\t"
      "ds_read_b128 v[178:181], v240 offset:0\n\t"
      "ds_read_b128 v[182:185], v240 offset:2048\n\t"
      "s_waitcnt lgkmcnt(0)\n\t"
      "v_mfma_f32_16x16x32_bf16 v[186:189], v[178:181], v[162:165], 0\n\t"
      "v_mfma_f32_16x16x32_bf16 v[186:189], v[182:185], v[166:169], v[186:189]\n\t"
      "s_nop 7\n\t"
      "ds_read_b64_tr_b16 v[178:179], v241 offset:0\n\t"
      "ds_read_b64_tr_b16 v[180:181], v241 offset:64\n\t"
      "ds_read_b64_tr_b16 v[182:183], v241 offset:512\n\t"
      "ds_read_b64_tr_b16 v[184:185], v241 offset:576\n\t"
      "v_add_f32_dpp v253, v188, v186 row_ror:8 row_mask:0xf bank_mask:0x3\n\t"
      "v_add_f32_dpp v253, v187, v189 row_ror:8 row_mask:0xf bank_mask:0xc\n\t"
      "v_fma_f32 v253, v253, s79, -v252\n\t"
      "v_exp_f32 v186, v253\n\t"
      "s_nop 1\n\t"
      "v_mov_b32_dpp v187, v186 row_ror:8 row_mask:0xf bank_mask:0xf\n\t"
      "v_cvt_pk_bf16_f32 v186, v186, v187\n\t"
      "ds_write_b32 v247, v186\n\t"
      "s_branch Lqk_first_r\n\t"
      "Lqk_last:\n\t"
      "s_waitcnt lgkmcnt(0)\n\t"
      "s_mov_b64 exec, s[66:67]\n\t"
      "s_bitcmp1_b32 s53, 5\n\t"
      "s_cbranch_scc1 Lqk_kl1\n\t"
      "buffer_store_dwordx4 v[210:213], v234, s[36:39], s87 offen sc1\n\t"
      "s_branch Lqk_kl2\n\t"
      "Lqk_kl1:\n\t"
      "buffer_store_dwordx4 v[214:217], v234, s[36:39], s87 offen sc1\n\t"
      "Lqk_kl2:\n\t"
      "s_mov_b64 exec, -1\n\t"
      "s_branch Lqk_end\n\t"
      "Lqk_bad:\n\t"
      "s_bitset1_b32 s78, 5\n\t"
      "s_mov_b64 s[96:97], vcc\n\t"
      "s_mov_b32 s71, 0\n\t"
      "Lqk_bad1:\n\t"
      "s_waitcnt vmcnt(0)\n\t"
      "v_max_u32 v204, v190, v192\n\t"
      "v_cmp_eq_u32 vcc, -1, v204\n\t"
      "s_cbranch_vccz Lqk_bad2\n\t"
      "s_add_u32 s71, s71, 1\n\t"
      "s_and_b32 s93, s71, 31\n\t"
      "s_cmp_lg_u32 s93, 31\n\t"
      "s_cbranch_scc1 Lqk_sp4\n\t"
      "s_cmp_lt_u32 s71, 0x8000\n\t"
      "s_cbranch_scc1 Lqk_sp4b\n\t"
      "v_mov_b32 v204, 1\n\t"
      "buffer_store_dword v204, off, s[36:39], s64 sc1\n\t"
      "s_branch Lqk_bad2\n\t"
      "Lqk_sp4b:\n\t"
      "buffer_load_dword v204, off, s[36:39], s64 sc1\n\t"
      "s_waitcnt vmcnt(0)\n\t"
      "v_readfirstlane_b32 s93, v204\n\t"
      "s_cmp_lg_u32 s93, 0\n\t"
      "s_cbranch_scc1 Lqk_bad2\n\t"
      "Lqk_sp4:\n\t"
      "s_sleep 2\n\t"
      "buffer_load_dword v190, v249, s[36:39], s86 offen sc1\n\t"
      "buffer_load_dword v192, v249, s[36:39], s86 offen offset:256 sc1\n\t"
      "s_branch Lqk_bad1\n\t"
      "Lqk_bad2:\n\t"
      "s_cmp_eq_u64 s[96:97], 0\n\t"
      "s_cbranch_scc1 Lqk_bad3\n\t"
      "v_max_f32 v202, v253, v205\n\t"
      "s_nop 1\n\t"
      "v_mov_b32_dpp v203, v202 row_ror:8 row_mask:0xf bank_mask:0xf\n\t"
      "v_max_f32 v202, v202, v203\n\t"
      "v_mov_b32 v203, v202\n\t"
      "s_nop 1\n\t"
      "v_permlane16_swap_b32 v202, v203\n\t"
      "v_max_f32 v202, v202, v203\n\t"
      "v_mov_b32 v203, v202\n\t"
      "s_nop 1\n\t"
      "v_permlane32_swap_b32 v202, v203\n\t"
      "v_max_f32 v202, v202, v203\n\t"
      "s_lshr_b32 s93, s53, 3\n\t"
      "v_add_u32 v203, s93, v234\n\t"
      "ds_write_b32 v203, v202 offset:20992\n\t"
      "s_waitcnt lgkmcnt(0)\n\t"
      "s_barrier\n\t"
      "ds_read_b128 v[186:189], v234 offset:22016\n\t"
      "s_waitcnt lgkmcnt(0)\n\t"
      "v_max_f32 v202, v186, v187\n\t"
      "v_max3_f32 v202, v202, v188, v189\n\t"
      "v_sub_f32 v253, v253, v202\n\t"
      "v_sub_f32 v205, v205, v202\n\t"
      "v_add_f32 v252, v252, v202\n\t"
      "v_exp_f32 v186, v253\n\t"
      "s_nop 1\n\t"
      "v_mov_b32_dpp v187, v186 row_ror:8 row_mask:0xf bank_mask:0xf\n\t"
      "v_cvt_pk_bf16_f32 v186, v186, v187\n\t"
      "ds_write_b32 v247, v186\n\t"
      "s_waitcnt lgkmcnt(0)\n\t"
      "s_barrier\n\t"
      "ds_read_b128 v[218:221], v248\n\t"
      "Lqk_bad3:\n\t"
      "s_waitcnt vmcnt(0) lgkmcnt(0)\n\t"
      "v_mfma_f32_16x16x32_bf16 v[226:229], v[178:181], v[218:221], 0\n\t"
      "v_mfma_f32_16x16x32_bf16 v[230:233], v[182:185], v[218:221], 0\n\t"
      "s_nop 1\n\t"
      "v_exp_f32 v202, v205\n\t"
      "v_mfma_f32_16x16x32_bf16 v[170:173], v[198:201], v[218:221], 0\n\t"
      "v_max_u32 v204, v190, v192\n\t"
      "v_mov_b32_dpp v203, v202 row_ror:8 row_mask:0xf bank_mask:0xf\n\t"
      "v_cvt_pk_bf16_f32 v194, v202, v203\n\t"
      "v_cmp_eq_u32 s[94:95], -1, v204\n\t"
      "s_nop 0\n\t"
      "v_mfma_f32_16x16x16_bf16 v[170:173], v[198:199], v[194:195], v[170:173]\n\t"
      "v_mfma_f32_16x16x16_bf16 v[226:229], v[190:191], v[194:195], v[226:229]\n\t"
      "v_mfma_f32_16x16x16_bf16 v[230:233], v[192:193], v[194:195], v[230:233]\n\t"
      "v_rcp_f32 v202, v170\n\t"
      "v_subrev_u32 v203, 0x2b800000, v170\n\t"
      "v_cmp_lt_u32 vcc, 0x14000000, v203\n\t"
      "s_nop 0\n\t"
      "v_mul_f32 v226, v226, v202\n\t"
      "v_mul_f32 v227, v227, v202\n\t"
      "v_mul_f32 v228, v228, v202\n\t"
      "v_mul_f32 v229, v229, v202\n\t"
      "v_cvt_pk_bf16_f32 v174, v226, v227\n\t"
      "v_cvt_pk_bf16_f32 v175, v228, v229\n\t"
      "v_mul_f32 v230, v230, v202\n\t"
      "v_mul_f32 v231, v231, v202\n\t"
      "v_mul_f32 v232, v232, v202\n\t"
      "v_mul_f32 v233, v233, v202\n\t"
      "s_or_b64 s[94:95], s[94:95], vcc\n\t"
      "v_cvt_pk_bf16_f32 v176, v230, v231\n\t"
      "v_cvt_pk_bf16_f32 v177, v232, v233\n\t"
      "s_branch Lqk_pub\n\t"
      "Lqk_rec:\n\t"
      "s_mov_b32 s71, 0\n\t"
      "Lqk_rec1:\n\t"
      "buffer_load_dwordx2 v[196:197], off, s[36:39], s85 offset:8 sc1\n\t"
      "s_waitcnt vmcnt(0)\n\t"
      "v_readfirstlane_b32 s99, v196\n\t"
      "v_readfirstlane_b32 s76, v197\n\t"
      "s_max_u32 s98, s99, s76\n\t"
      "s_cmp_lg_u32 s98, -1\n\t"
      "s_cbranch_scc1 Lqk_rec2\n\t"
      "s_add_u32 s71, s71, 1\n\t"
      "s_and_b32 s93, s71, 31\n\t"
      "s_cmp_lg_u32 s93, 31\n\t"
      "s_cbranch_scc1 Lqk_sp5\n\t"
      "s_cmp_lt_u32 s71, 0x8000\n\t"
      "s_cbranch_scc1 Lqk_sp5b\n\t"
      "v_mov_b32 v202, 1\n\t"
      "buffer_store_dword v202, off, s[36:39], s64 sc1\n\t"
      "s_branch Lqk_rec2\n\t"
      "Lqk_sp5b:\n\t"
      "buffer_load_dword v202, off, s[36:39], s64 sc1\n\t"
      "s_waitcnt vmcnt(0)\n\t"
      "v_readfirstlane_b32 s93, v202\n\t"
      "s_cmp_lg_u32 s93, 0\n\t"
      "s_cbranch_scc1 Lqk_rec2\n\t"
      "Lqk_sp5:\n\t"
      "s_sleep 2\n\t"
      "s_branch Lqk_rec1\n\t"
      "Lqk_rec2:\n\t"
      "s_mov_b32 s93, s99\n\t"
      "s_branch Lqk_rec_r\n\t"
      "Lqk_kvl:\n\t"
      "s_mov_b32 s71, 0\n\t"
      "Lqk_kvl1:\n\t"
      "s_mov_b64 exec, 17\n\t"
      "buffer_load_dword v209, v234, s[36:39], s59 offen sc1\n\t"
      "s_mov_b64 exec, -1\n\t"
      "s_waitcnt vmcnt(0)\n\t"
      "v_readlane_b32 s93, v209, 0\n\t"
      "v_readlane_b32 s92, v209, 4\n\t"
      "s_min_u32 s93, s93, s92\n\t"
      "s_max_u32 s70, s70, s93\n\t"
      "s_add_u32 s93, s68, 1\n\t"
      "s_cmp_lt_u32 s93, s70\n\t"
      "s_cbranch_scc1 Lqk_kvl2\n\t"
      "s_add_u32 s71, s71, 1\n\t"
      "s_and_b32 s93, s71, 31\n\t"
      "s_cmp_lg_u32 s93, 31\n\t"
      "s_cbranch_scc1 Lqk_sp6\n\t"
      "s_cmp_lt_u32 s71, 0x8000\n\t"
      "s_cbranch_scc1 Lqk_sp6b\n\t"
      "v_mov_b32 v204, 3\n\t"
      "buffer_store_dword v204, off, s[36:39], s64 sc1\n\t"
      "s_branch Lqk_kvl2\n\t"
      "Lqk_sp6b:\n\t"
      "buffer_load_dword v204, off, s[36:39], s64 sc1\n\t"
      "s_waitcnt vmcnt(0)\n\t"
      "v_readfirstlane_b32 s93, v204\n\t"
      "s_cmp_lg_u32 s93, 0\n\t"
      "s_cbranch_scc1 Lqk_kvl2\n\t"
      "Lqk_sp6:\n\t"
      "s_sleep 8\n\t"
      "s_branch Lqk_kvl1\n\t"
      "Lqk_kvl2:\n\t"
      "s_add_u32 s90, s90, 0x4000\n\t"
      "s_add_u32 s91, s91, 0x4000\n\t"
      "s_mov_b32 m0, s63\n\t"
      "s_nop 0\n\t"
      "buffer_load_dwordx4 v242, s[40:43], s90 offen offset:0 sc1 lds\n\t"
      "buffer_load_dwordx4 v242, s[40:43], s90 offen offset:1024 sc1 lds\n\t"
      "s_add_u32 m0, s63, 8192\n\t"
      "s_nop 0\n\t"
      "buffer_load_dwordx4 v243, s[40:43], s91 offen offset:0 sc1 lds\n\t"
      "buffer_load_dwordx4 v243, s[40:43], s91 offen offset:1024 sc1 lds\n\t"
      "s_branch Lqk_kvl_r\n\t"
      "Lqk_end:\n\t"
      "s_mov_b64 exec, -1\n\t"
      "s_waitcnt vmcnt(0) lgkmcnt(0)\n\t"
      : "+{v[234:249]}"(C), "+{v[250:253]}"(C2), "+{s[60:67]}"(S2), "+{s[72:75]}"(S3)
      : "{a[0:31]}"(A0), "{a[32:63]}"(A1), "{a[64:95]}"(A2), "{a[96:127]}"(A3), "{a[128:159]}"(A4), "{a[160:191]}"(A5), "{a[192:223]}"(A6), "{a[224:255]}"(A7),
        "{v[2:33]}"(V0), "{v[34:65]}"(V1), "{v[66:97]}"(V2), "{v[98:129]}"(V3), "{v[130:161]}"(V4), "{s[36:39]}"(rs_o), "{s[40:43]}"(rs_x), "{s[44:59]}"(S)
      : V162_225, "v226", "v227", "v228", "v229", "v230", "v231", "v232", "v233", "s16", "s17", "s18", "s19", "s32", "s34", "s68", "s69", "s70", "s71", "s76", "s77", "s78", "s79", R8(s, 8), R8(s, 9), "vcc", "scc", "m0", "memory"
      );
    }
  } else {
    const int c = b - NQK, cg = 2 * tr4(c >> 2) + ((c >> 1) & 1), rh = c & 1;
    unsigned long long tk1, ep, hv, hf;
    v8u wv[16], wo[32];
#define OHEAD(i) (4 * ((xq == 0 ? 2 - (i) : 3 * xq + (i)) & 7) + w)
    {
      char* const wb_ = Lraw + w * 40960;
      const unsigned wbl = (unsigned)(size_t)Lraw + w * 40960;
      auto issue = [&](int j) {
        char* dst = wb_ + (j & 1) * 16384;
        if (j < 2) { const int r = lane >> 3, h2a = (r >> 1) & 1;
#pragma unroll
          for (int q = 0; q < 16; ++q) {
            const int h2 = h2a | ((q & 1) << 1), ch = (((((lane & 7) >> 1) ^ h2) << 1) | (lane & 1));
            const int row = 64 * mchunk(xq, 4 * (2 * j + (q >> 3)) + w) + 8 * (q & 7) + r;
            __builtin_amdgcn_global_load_lds((const unsigned*)(P.Wv + (size_t)row * HDA + 64 * c + ch * 8), (unsigned*)(dst + q * 1024), 16, 0, NT & 2);
          } }
        else {
#pragma unroll
          for (int q = 0; q < 16; ++q) {
            const int hd = OHEAD(2 * (j - 2) + (q >> 3));
            __builtin_amdgcn_global_load_lds((const unsigned*)(P.Wo + (size_t)(128 * hd + 16 * (q & 7) + (lane >> 2)) * DM + 32 * cg + (lane & 3) * 8), (unsigned*)(dst + q * 1024), 16, 0, (NT >> 1) & 2);
          } } };
      auto qtouch = [&](int s0, int s1) {
        const int nv = 32 - nq, v = slot - nq;
#pragma unroll
        for (int k = 0; k < 5; ++k) {
          const int id = v + k * nv, q = id % nq, i = s0 + id / nq, hq = 12 * xq + q, part = w;
          const int row = 64 * mchunk(xq, 4 * ((i >> 1) & 3) + (part ^ 2)) + lane;
          __builtin_amdgcn_raw_ptr_buffer_load_lds(__builtin_amdgcn_make_buffer_rsrc((void*)((i & 1) ? P.Wk : P.Wq), 0, DM * HDA * 2, 0x00020000),
              (__attribute__((address_space(3))) void*)(wb_ + 32768), 4, i < s1 ? row * (HDA * 2) + hq * 256 + (part < 2 ? 0 : 128) : 0x7ffffff0, 0, 0, 0);
        } };
#define TRR(d, a, o) asm volatile("ds_read_b64_tr_b16 %0, %1 offset:%2" : "=&v"(d) : "v"(a), "i"(o) : "memory")
      const int rd_h2 = ((l15 >> 3) & 1) | ((lq & 1) << 1), qq = l15 >> 2;
      const unsigned rv = wbl + (8 * lq + (l15 >> 2)) * 128 + (l15 & 3) * 8;
      const unsigned ro_ = wbl + (16 * lq + 4 * (qq >> 1) + (qq & 1)) * 64 + (l15 & 3) * 8;
      issue(0); issue(1);
#pragma unroll
      for (int j = 0; j < 6; ++j) {
        const int TJ = QTJ;
        if (j == 5) asm volatile("s_waitcnt vmcnt(0)" ::: "memory");
        else if (j == TJ + 1) asm volatile("s_waitcnt vmcnt(21)" ::: "memory");
        else if (j == TJ) asm volatile("s_waitcnt vmcnt(21)" ::: "memory");
        else asm volatile("s_waitcnt vmcnt(16)" ::: "memory");
#pragma unroll
        for (int hf_ = 0; hf_ < 2; ++hf_) {
          v2u e[4][4];
#pragma unroll
          for (int i4 = 0; i4 < 4; ++i4) {
            const int i = 4 * hf_ + i4;
            if (j < 2) {
              const unsigned a = rv + (j & 1) * 16384 + (i >> 2) * 8192 + (((i & 3) ^ 3 ^ w ^ rd_h2) << 5);
              TRR(e[i4][0], a, 0); TRR(e[i4][1], a, 4 * 128); TRR(e[i4][2], a, 32 * 128); TRR(e[i4][3], a, 36 * 128);
            } else {
              const unsigned a = ro_ + (j & 1) * 16384 + (i >> 2) * 8192 + ((i >> 1) & 1) * 512 + (i & 1) * 32;
              TRR(e[i4][0], a, 0); TRR(e[i4][1], a, 2 * 64); TRR(e[i4][2], a, 64 * 64); TRR(e[i4][3], a, 66 * 64);
            }
          }
          asm volatile("s_waitcnt lgkmcnt(0)" : "+v"(e[0][0]), "+v"(e[0][1]), "+v"(e[0][2]), "+v"(e[0][3]), "+v"(e[1][0]), "+v"(e[1][1]), "+v"(e[1][2]), "+v"(e[1][3]),
                       "+v"(e[2][0]), "+v"(e[2][1]), "+v"(e[2][2]), "+v"(e[2][3]), "+v"(e[3][0]), "+v"(e[3][1]), "+v"(e[3][2]), "+v"(e[3][3]) :: "memory");
#pragma unroll
          for (int i4 = 0; i4 < 4; ++i4) {
            const int i = 4 * hf_ + i4;
            const v8u z = {e[i4][0].x, e[i4][0].y, e[i4][1].x, e[i4][1].y, e[i4][2].x, e[i4][2].y, e[i4][3].x, e[i4][3].y};
            if (j < 2) wv[8 * j + i] = z; else { wo[8 * (j - 2) + i] = z; asm volatile("" : "+a"(wo[8 * (j - 2) + i])); }
          }
          SB0;
        }
        SB0;
        if (j + 2 < 6) issue(j + 2);
        if (j == TJ - 1) qtouch(P.stag2 & 255, (P.stag2 >> 8) & 255);
        if (j == 3) { ep = ld_agent(&P.hdr->epoch[0]); tk1 = ld_agent(&P.hdr->token[0]); hv = ld_agent(&P.hdr->vote[lane]); hf = ld_agent(&P.hdr->fill[lane]); }
        SB0;
      }
#undef TRR
      lds_barrier();
    }
    const bool keeper = (c == 0 && w == 3);
    int* failw = (int*)(Lraw + T0);
    if (tid < 4) failw[tid] = 0;
    tok = uni64(ep) + 1;
    plc = count_votes(hv);
    if (!__all(tk1 == tok && (hv | 3) == 4 * tok + 3 && hf == tok)) { wait_token(P, tok); plc = wait_votes(P, tok, lane, true); }
    v32u WA[8], WV[4];
#pragma unroll
    for (int i = 0; i < 256; ++i) { WA[i >> 5][i & 31] = wo[i >> 3][i & 7]; if (i < 128) WV[i >> 5][i & 31] = wv[i >> 3][i & 7]; }
    const unsigned lds0 = (unsigned)(size_t)Lraw, ob = lds0 + T0 + 1024 + 4096;
    v16u C;
    C[0] = lane * 16; C[1] = (lq * 2 + rh) * 256 + l15 * 16; C[2] = idx; C[3] = lds0 + w * 512 + lane * 8; C[4] = lds0 + (w ^ 3) * 512 + lane * 8;
    C[5] = ob + w * 576 + lq * 136 + l15 * 8 - (lane >> 5) * 4096; C[6] = ob + lq * 136 + ((lane & 3) * 4 + ((lane >> 3) & 1) * 2) * 8 + ((lane >> 2) & 1) * 4;
    C[7] = quad(c * 1024) + w * 256 + lane * 4; C[8] = quad((c >> 1) * 512) + (lane >> 5) * 256 + rh * 128 + (lane & 31) * 4; C[9] = (slot == nq && tid == 0) ? 144 + 4 * xq : 0x7ffffff0;
    C[10] = lds0 + T0; C[11] = C[6] + 1152; C[12] = lds0 + 2048 + (w ^ 2) * 512 + lane * 8; C[13] = lds0 + 4096 + (w ^ 1) * 512 + lane * 8; C[14] = C[15] = 0;
    v16u S;
#pragma unroll
    for (int i = 0; i < 8; ++i) { S[i] = quad(OHEAD(i) * 2048); if (i < 4) S[8 + i] = mso[i]; }
    { const unsigned long long pl2 = __ballot((sideo(plc, (lane >> 1) << 21, xq != 1) >> (13 + (lane & 1))) & 1);
      S[12] = (unsigned)pl2; S[13] = (unsigned)(pl2 >> 32); S[14] = nearo(plc, RC0); S[15] = (hside(c >> 1) ^ (xq != 1)) << 14; }
#pragma unroll
    for (int k = 0; k < 16; ++k) S[k] = __builtin_amdgcn_readfirstlane(S[k]);
    v8u Q = {(unsigned)P.pad2, (unsigned)P.stag, w == 0 ? 2u : 10u, CBASE + (unsigned)offsetof(Ctl, tmo), (unsigned)P.sh1, (unsigned)P.dv,
             (unsigned)((char*)prof - (char*)obuf) + (6u << 17) + (c * 4 + w) * 32, keeper ? (unsigned)((P.dmv + P.pad) | (P.dov + (P.pad >> 2)) << 10 | P.rec << 20) : 0u};
#pragma unroll
    for (int k = 0; k < 8; ++k) Q[k] = __builtin_amdgcn_readfirstlane(Q[k]);
    lds_barrier();
      asm volatile(
      "s_mov_b32 s72, 0\n\t"
      "s_mov_b32 s73, 0\n\t"
      "s_mov_b32 s98, 0\n\t"
      "s_mov_b32 s57, 0\n\t"
      "s_mov_b32 s58, 0\n\t"
      "s_mov_b32 s60, 0\n\t"
      "s_mov_b32 s61, 0\n\t"
      "s_mov_b32 s63, 0\n\t"
      "s_mov_b32 s99, 0\n\t"
      "s_mov_b32 s56, 2\n\t"
      "s_mov_b32 s59, 5888\n\t"
      "s_mov_b32 s62, 1000\n\t"
      "Lvo_top:\n\t"
      "s_lshl_b32 s82, s72, 18\n\t"
      "s_add_u32 s82, s82, 0x3c0000\n\t"
      "s_lshr_b32 s76, s82, 20\n\t"
      "s_and_b32 s76, s76, 62\n\t"
      "s_lshr_b64 s[80:81], s[52:53], s76\n\t"
      "s_bfe_u32 s76, s82, 0x10013\n\t"
      "s_lshl1_add_u32 s80, s76, s80\n\t"
      "s_and_b32 s80, s80, 3\n\t"
      "s_lshl_b32 s80, s80, 13\n\t"
      "s_or_b32 s82, s82, s80\n\t"
      "s_lshl_b32 s87, s72, 16\n\t"
      "s_lshr_b32 s76, s87, 20\n\t"
      "s_and_b32 s76, s76, 62\n\t"
      "s_lshr_b64 s[80:81], s[52:53], s76\n\t"
      "s_bfe_u32 s76, s87, 0x10013\n\t"
      "s_lshl1_add_u32 s80, s76, s80\n\t"
      "s_and_b32 s80, s80, 3\n\t"
      "s_lshl_b32 s80, s80, 13\n\t"
      "s_or_b32 s87, s87, s80\n\t"
      "s_lshl_b32 s86, s72, 18\n\t"
      "s_add_u32 s86, s86, 0x1400000\n\t"
      "s_lshr_b32 s76, s86, 20\n\t"
      "s_and_b32 s76, s76, 62\n\t"
      "s_lshr_b64 s[80:81], s[52:53], s76\n\t"
      "s_bfe_u32 s76, s86, 0x10013\n\t"
      "s_lshl1_add_u32 s80, s76, s80\n\t"
      "s_and_b32 s80, s80, 3\n\t"
      "s_lshl_b32 s80, s80, 13\n\t"
      "s_or_b32 s86, s86, s80\n\t"
      "s_lshl_b32 s84, s72, 8\n\t"
      "s_and_b32 s76, s84, 0x2000\n\t"
      "s_add_u32 s84, s84, s76\n\t"
      "s_add_u32 s84, s84, s54\n\t"
      "s_xor_b32 s88, s87, 0x4000\n\t"
      "s_xor_b32 s86, s86, s55\n\t"
      "s_add_u32 s83, s82, s40\n\t"
      "s_add_u32 s89, s87, s48\n\t"
      "s_add_u32 s90, s87, s49\n\t"
      "s_add_u32 s91, s87, s50\n\t"
      "s_add_u32 s92, s87, s51\n\t"
      "s_memrealtime s[80:81]\n\t"
      "v_mov_b64 v[162:163], 0\n\t"
      "v_mov_b64 v[164:165], 0\n\t"
      "v_mov_b64 v[166:167], 0\n\t"
      "v_mov_b64 v[168:169], 0\n\t"
      "v_mov_b64 v[170:171], 0\n\t"
      "v_mov_b64 v[172:173], 0\n\t"
      "v_mov_b64 v[174:175], 0\n\t"
      "v_mov_b64 v[176:177], 0\n\t"
      "v_mov_b64 v[178:179], 0\n\t"
      "v_mov_b64 v[180:181], 0\n\t"
      "v_mov_b64 v[182:183], 0\n\t"
      "v_mov_b64 v[184:185], 0\n\t"
      "v_mov_b64 v[186:187], 0\n\t"
      "v_mov_b64 v[188:189], 0\n\t"
      "v_mov_b64 v[190:191], 0\n\t"
      "v_mov_b64 v[192:193], 0\n\t"
      "s_waitcnt lgkmcnt(0)\n\t"
      "s_and_b32 s76, s56, 3\n\t"
      "s_cbranch_scc1 Lvo_entry\n\t"
      "s_sub_u32 s77, s57, s80\n\t"
      "s_cmp_gt_i32 s77, 20000\n\t"
      "s_cbranch_scc1 Lvo_o\n\t"
      "s_sub_u32 s76, s60, s62\n\t"
      "s_add_u32 s80, s76, 199\n\t"
      "s_ashr_i32 s76, s76, 1\n\t"
      "s_cmp_lt_u32 s80, 399\n\t"
      "s_cselect_b32 s76, s76, 0\n\t"
      "s_add_u32 s59, s59, s76\n\t"
      "s_sub_u32 s76, s57, s61\n\t"
      "s_mul_i32 s76, s76, s59\n\t"
      "s_lshr_b32 s76, s76, 8\n\t"
      "s_add_u32 s62, s76, s60\n\t"
      "s_add_u32 s60, s62, 10\n\t"
      "s_mov_b32 s61, s57\n\t"
      "s_cmp_lt_i32 s77, 16\n\t"
      "s_cselect_b32 s76, 1000, 0\n\t"
      "s_add_u32 s62, s62, s76\n\t"
      "s_lshr_b32 s76, s59, 6\n\t"
      "Lvo_slp:\n\t"
      "s_cmp_lt_i32 s77, 57\n\t"
      "s_cbranch_scc1 Lvo_fo\n\t"
      "s_sleep 2\n\t"
      "s_memrealtime s[80:81]\n\t"
      "s_waitcnt lgkmcnt(0)\n\t"
      "s_sub_u32 s77, s57, s80\n\t"
      "s_branch Lvo_slp\n\t"
      ".p2align 6\n\t"
      ".rept 2\n\t"
      "s_nop 0\n\t"
      ".endr\n\t"
      "Lvo_fo:\n\t"
      "s_memrealtime vcc\n\t"
      "s_memtime s[80:81]\n\t"
      "s_waitcnt lgkmcnt(0)\n\t"
      "Lvo_fo2:\n\t"
      "s_sub_i32 vcc_lo, s57, vcc_lo\n\t"
      "s_mul_i32 vcc_lo, vcc_lo, s59\n\t"
      "s_ashr_i32 vcc_lo, vcc_lo, 8\n\t"
      "s_add_i32 vcc_lo, vcc_lo, s80\n\t"
      "s_sub_i32 vcc_hi, vcc_lo, s60\n\t"
      "s_cmp_gt_u32 vcc_hi, s76\n\t"
      "s_cselect_b32 s60, vcc_lo, s60\n\t"
      "s_sub_i32 s80, s60, s80\n\t"
      "s_cmpk_gt_i32 s80, 230\n\t"
      "s_cbranch_scc1 Lvo_fo\n\t"
      "s_sub_i32 s80, s80, s64\n\t"
      "s_max_i32 s80, s80, 0\n\t"
      "s_min_i32 s80, s80, 192\n\t"
      "s_and_b32 s80, s80, -4\n\t"
      "s_sub_i32 s80, 204, s80\n\t"
      "s_getpc_b64 vcc\n\t"
      "s_add_u32 vcc_lo, vcc_lo, s80\n\t"
      "s_addc_u32 vcc_hi, vcc_hi, 0\n\t"
      "s_setpc_b64 vcc\n\t"
      ".rept 48\n\t"
      "s_nop 0\n\t"
      ".endr\n\t"
      "Lvo_o:\n\t"
      "buffer_load_dwordx4 v[130:133], v195, s[36:39], s83 offen sc1\n\t"
      "s_add_u32 s83, s82, s41\n\t"
      "s_add_u32 s77, s58, s65\n\t"
      "s_sub_u32 s78, s77, s61\n\t"
      "s_mul_i32 s78, s78, s59\n\t"
      "s_lshr_b32 s78, s78, 8\n\t"
      "s_add_u32 s78, s78, s60\n\t"
      "s_lshr_b32 s76, s59, 6\n\t"
      "buffer_load_dwordx4 v[134:137], v195, s[36:39], s83 offen sc1\n\t"
      "s_add_u32 s83, s82, s42\n\t"
      "s_nop 7\n\t"
      "buffer_load_dwordx4 v[138:141], v195, s[36:39], s83 offen sc1\n\t"
      "s_add_u32 s83, s82, s43\n\t"
      "s_nop 7\n\t"
      "buffer_load_dwordx4 v[142:145], v195, s[36:39], s83 offen sc1\n\t"
      "s_add_u32 s83, s82, s44\n\t"
      "s_nop 7\n\t"
      "buffer_load_dwordx4 v[146:149], v195, s[36:39], s83 offen sc1\n\t"
      "s_add_u32 s83, s82, s45\n\t"
      "s_nop 7\n\t"
      "buffer_load_dwordx4 v[150:153], v195, s[36:39], s83 offen sc1\n\t"
      "s_add_u32 s83, s82, s46\n\t"
      "s_nop 7\n\t"
      "buffer_load_dwordx4 v[154:157], v195, s[36:39], s83 offen sc1\n\t"
      "s_add_u32 s83, s82, s47\n\t"
      "s_nop 7\n\t"
      "buffer_load_dwordx4 v[158:161], v195, s[36:39], s83 offen sc1\n\t"
      "s_waitcnt vmcnt(7)\n\t"
      "v_smfmac_f32_16x16x64_bf16 v[166:169], v[130:133], a[8:15], v196\n\t"
      "v_smfmac_f32_16x16x64_bf16 v[170:173], v[130:133], a[16:23], v196\n\t"
      "v_smfmac_f32_16x16x64_bf16 v[162:165], v[130:133], a[0:7], v196\n\t"
      "v_smfmac_f32_16x16x64_bf16 v[174:177], v[130:133], a[24:31], v196\n\t"
      "s_waitcnt vmcnt(6)\n\t"
      "v_smfmac_f32_16x16x64_bf16 v[166:169], v[134:137], a[40:47], v196\n\t"
      "v_smfmac_f32_16x16x64_bf16 v[170:173], v[134:137], a[48:55], v196\n\t"
      "v_smfmac_f32_16x16x64_bf16 v[162:165], v[134:137], a[32:39], v196\n\t"
      "v_smfmac_f32_16x16x64_bf16 v[174:177], v[134:137], a[56:63], v196\n\t"
      "s_waitcnt vmcnt(5)\n\t"
      "v_smfmac_f32_16x16x64_bf16 v[166:169], v[138:141], a[72:79], v196\n\t"
      "v_smfmac_f32_16x16x64_bf16 v[170:173], v[138:141], a[80:87], v196\n\t"
      "v_smfmac_f32_16x16x64_bf16 v[162:165], v[138:141], a[64:71], v196\n\t"
      "v_smfmac_f32_16x16x64_bf16 v[174:177], v[138:141], a[88:95], v196\n\t"
      "s_waitcnt vmcnt(4)\n\t"
      "v_smfmac_f32_16x16x64_bf16 v[166:169], v[142:145], a[104:111], v196\n\t"
      "v_smfmac_f32_16x16x64_bf16 v[170:173], v[142:145], a[112:119], v196\n\t"
      "v_smfmac_f32_16x16x64_bf16 v[162:165], v[142:145], a[96:103], v196\n\t"
      "v_smfmac_f32_16x16x64_bf16 v[174:177], v[142:145], a[120:127], v196\n\t"
      "s_waitcnt vmcnt(3)\n\t"
      "v_smfmac_f32_16x16x64_bf16 v[166:169], v[146:149], a[136:143], v196\n\t"
      "v_smfmac_f32_16x16x64_bf16 v[170:173], v[146:149], a[144:151], v196\n\t"
      "v_smfmac_f32_16x16x64_bf16 v[162:165], v[146:149], a[128:135], v196\n\t"
      "v_smfmac_f32_16x16x64_bf16 v[174:177], v[146:149], a[152:159], v196\n\t"
      "s_waitcnt vmcnt(2)\n\t"
      "v_smfmac_f32_16x16x64_bf16 v[166:169], v[150:153], a[168:175], v196\n\t"
      "v_smfmac_f32_16x16x64_bf16 v[170:173], v[150:153], a[176:183], v196\n\t"
      "v_smfmac_f32_16x16x64_bf16 v[162:165], v[150:153], a[160:167], v196\n\t"
      "v_smfmac_f32_16x16x64_bf16 v[174:177], v[150:153], a[184:191], v196\n\t"
      "s_waitcnt vmcnt(1)\n\t"
      "v_smfmac_f32_16x16x64_bf16 v[166:169], v[154:157], a[200:207], v196\n\t"
      "v_smfmac_f32_16x16x64_bf16 v[170:173], v[154:157], a[208:215], v196\n\t"
      "v_smfmac_f32_16x16x64_bf16 v[162:165], v[154:157], a[192:199], v196\n\t"
      "v_smfmac_f32_16x16x64_bf16 v[174:177], v[154:157], a[216:223], v196\n\t"
      "s_waitcnt vmcnt(0)\n\t"
      "v_smfmac_f32_16x16x64_bf16 v[166:169], v[158:161], a[232:239], v196\n\t"
      "v_smfmac_f32_16x16x64_bf16 v[170:173], v[158:161], a[240:247], v196\n\t"
      "v_smfmac_f32_16x16x64_bf16 v[162:165], v[158:161], a[224:231], v196\n\t"
      "v_add_f32 v166, v166, v168\n\t"
      "v_add_f32 v167, v167, v169\n\t"
      "v_smfmac_f32_16x16x64_bf16 v[174:177], v[158:161], a[248:255], v196\n\t"
      "v_add_f32 v170, v170, v172\n\t"
      "v_add_f32 v171, v171, v173\n\t"
      "v_cmp_u_f32 vcc, v166, v167\n\t"
      "s_nop 0\n\t"
      "v_permlane32_swap_b32 v170, v166\n\t"
      "v_permlane32_swap_b32 v171, v167\n\t"
      "v_add_f32 v162, v162, v164\n\t"
      "v_add_f32 v163, v163, v165\n\t"
      "v_add_f32 v162, v162, v166\n\t"
      "v_add_f32 v163, v163, v167\n\t"
      "v_add_f32 v174, v174, v176\n\t"
      "v_add_f32 v175, v175, v177\n\t"
      "v_add_f32 v174, v174, v170\n\t"
      "v_add_f32 v175, v175, v171\n\t"
      "ds_write2st64_b64 v199, v[162:163], v[174:175] offset1:8\n\t"
      "s_cbranch_vccnz Lvo_onan\n\t"
      "Lvo_ores:\n\t"
      "s_cmp_lg_u32 s66, 2\n\t"
      "s_waitcnt lgkmcnt(0)\n\t"
      "s_barrier\n\t"
      "s_cbranch_scc1 Lvo_np\n\t"
      "ds_read2_b32 v[210:211], v200 offset0:0 offset1:2\n\t"
      "ds_read2_b32 v[212:213], v200 offset0:144 offset1:146\n\t"
      "ds_read2_b32 v[214:215], v205 offset0:0 offset1:2\n\t"
      "ds_read2_b32 v[216:217], v205 offset0:144 offset1:146\n\t"
      "s_waitcnt lgkmcnt(2)\n\t"
      "v_add_f32 v210, v210, v212\n\t"
      "v_add_f32 v211, v211, v213\n\t"
      "s_waitcnt lgkmcnt(0)\n\t"
      "v_add_f32 v214, v214, v216\n\t"
      "v_add_f32 v215, v215, v217\n\t"
      "v_add_f32 v210, v210, v214\n\t"
      "v_add_f32 v211, v211, v215\n\t"
      "v_cvt_pk_bf16_f32 v210, v210, v211\n\t"
      "buffer_store_dword v210, v202, s[36:39], s87 offen sc1\n\t"
      "buffer_store_dword v210, v202, s[36:39], s88 offen sc1\n\t"
      "Lvo_np:\n\t"
      "s_memrealtime vcc\n\t"
      "s_memtime s[80:81]\n\t"
      "s_waitcnt lgkmcnt(0)\n\t"
      "s_mov_b32 s63, vcc_lo\n\t"
      "s_add_u32 vcc_hi, vcc_lo, s66\n\t"
      "s_sub_u32 vcc_hi, vcc_hi, s58\n\t"
      "s_cmp_gt_i32 vcc_hi, 0\n\t"
      "s_cbranch_scc1 Lvo_late\n\t"
      "Lvo_fv2:\n\t"
      "s_sub_i32 vcc_lo, s77, vcc_lo\n\t"
      "s_mul_i32 vcc_lo, vcc_lo, s59\n\t"
      "s_ashr_i32 vcc_lo, vcc_lo, 8\n\t"
      "s_add_i32 vcc_lo, vcc_lo, s80\n\t"
      "s_sub_i32 vcc_hi, vcc_lo, s78\n\t"
      "s_cmp_gt_u32 vcc_hi, s76\n\t"
      "s_cselect_b32 s78, vcc_lo, s78\n\t"
      "s_sub_i32 s80, s78, s80\n\t"
      "s_cmpk_gt_i32 s80, 230\n\t"
      "s_cbranch_scc1 Lvo_fv\n\t"
      "s_sub_i32 s80, s80, s64\n\t"
      "s_max_i32 s80, s80, 0\n\t"
      "s_min_i32 s80, s80, 192\n\t"
      "s_and_b32 s80, s80, -4\n\t"
      "s_sub_i32 s80, 204, s80\n\t"
      "s_getpc_b64 vcc\n\t"
      "s_add_u32 vcc_lo, vcc_lo, s80\n\t"
      "s_addc_u32 vcc_hi, vcc_hi, 0\n\t"
      "s_setpc_b64 vcc\n\t"
      ".rept 48\n\t"
      "s_nop 0\n\t"
      ".endr\n\t"
      "Lvo_v:\n\t"
      "buffer_load_dwordx4 v[130:133], v194, s[36:39], s89 offen sc1\n\t"
      "s_sub_u32 s76, s63, s57\n\t"
      "s_and_b32 s76, s76, 0xffff\n\t"
      "v_mov_b32 v218, s76\n\t"
      "s_nop 3\n\t"
      "buffer_load_dwordx4 v[134:137], v194, s[36:39], s90 offen sc1\n\t"
      "s_nop 7\n\t"
      "buffer_load_dwordx4 v[138:141], v194, s[36:39], s91 offen sc1\n\t"
      "s_nop 7\n\t"
      "buffer_load_dwordx4 v[142:145], v194, s[36:39], s92 offen sc1\n\t"
      "buffer_load_dwordx2 v[220:221], off, s[36:39], s84 offset:8 sc1\n\t"
      "buffer_store_dword v218, v203, s[36:39], s84 offen sc1\n\t"
      "s_waitcnt vmcnt(5)\n\t"
      "v_smfmac_f32_16x16x64_bf16 v[178:181], v[130:133], v[2:9], v196\n\t"
      "v_smfmac_f32_16x16x64_bf16 v[182:185], v[130:133], v[10:17], v196\n\t"
      "v_smfmac_f32_16x16x64_bf16 v[186:189], v[130:133], v[18:25], v196\n\t"
      "v_smfmac_f32_16x16x64_bf16 v[190:193], v[130:133], v[26:33], v196\n\t"
      "s_waitcnt vmcnt(4)\n\t"
      "v_smfmac_f32_16x16x64_bf16 v[178:181], v[134:137], v[34:41], v196\n\t"
      "v_smfmac_f32_16x16x64_bf16 v[182:185], v[134:137], v[42:49], v196\n\t"
      "v_smfmac_f32_16x16x64_bf16 v[186:189], v[134:137], v[50:57], v196\n\t"
      "v_smfmac_f32_16x16x64_bf16 v[190:193], v[134:137], v[58:65], v196\n\t"
      "s_waitcnt vmcnt(3)\n\t"
      "v_smfmac_f32_16x16x64_bf16 v[178:181], v[138:141], v[66:73], v196\n\t"
      "v_smfmac_f32_16x16x64_bf16 v[182:185], v[138:141], v[74:81], v196\n\t"
      "v_smfmac_f32_16x16x64_bf16 v[186:189], v[138:141], v[82:89], v196\n\t"
      "v_smfmac_f32_16x16x64_bf16 v[190:193], v[138:141], v[90:97], v196\n\t"
      "s_waitcnt vmcnt(2)\n\t"
      "v_smfmac_f32_16x16x64_bf16 v[178:181], v[142:145], v[98:105], v196\n\t"
      "v_smfmac_f32_16x16x64_bf16 v[182:185], v[142:145], v[106:113], v196\n\t"
      "v_smfmac_f32_16x16x64_bf16 v[186:189], v[142:145], v[114:121], v196\n\t"
      "v_add_f32 v178, v178, v180\n\t"
      "v_add_f32 v179, v179, v181\n\t"
      "v_smfmac_f32_16x16x64_bf16 v[190:193], v[142:145], v[122:129], v196\n\t"
      "ds_write_b64 v197, v[178:179] offset:0\n\t"
      "v_add_f32 v182, v182, v184\n\t"
      "v_add_f32 v183, v183, v185\n\t"
      "ds_write_b64 v197, v[182:183] offset:2048\n\t"
      "v_cmp_u_f32 vcc, v178, v179\n\t"
      "v_add_f32 v186, v186, v188\n\t"
      "v_add_f32 v187, v187, v189\n\t"
      "ds_write_b64 v197, v[186:187] offset:4096\n\t"
      "s_nop 0\n\t"
      "v_add_f32 v190, v190, v192\n\t"
      "v_add_f32 v191, v191, v193\n\t"
      "s_cbranch_vccnz Lvo_vnan\n\t"
      "Lvo_vres:\n\t"
      "s_waitcnt lgkmcnt(0)\n\t"
      "s_barrier\n\t"
      "ds_read_b64 v[210:211], v198\n\t"
      "ds_read_b64 v[212:213], v206\n\t"
      "ds_read_b64 v[214:215], v207\n\t"
      "ds_read_b32 v222, v204\n\t"
      "s_waitcnt lgkmcnt(3)\n\t"
      "v_add_f32 v210, v210, v190\n\t"
      "v_add_f32 v211, v211, v191\n\t"
      "s_waitcnt lgkmcnt(1)\n\t"
      "v_add_f32 v212, v212, v214\n\t"
      "v_add_f32 v213, v213, v215\n\t"
      "v_add_f32 v210, v210, v212\n\t"
      "v_add_f32 v211, v211, v213\n\t"
      "v_cvt_pk_bf16_f32 v210, v210, v211\n\t"
      "buffer_store_dword v210, v201, s[36:39], s86 offen sc1\n\t"
      "s_waitcnt lgkmcnt(0)\n\t"
      "s_memrealtime s[74:75]\n\t"
      "v_readfirstlane_b32 s95, v222\n\t"
      "s_waitcnt vmcnt(2)\n\t"
      "v_readfirstlane_b32 s93, v220\n\t"
      "v_readfirstlane_b32 s94, v221\n\t"
      "s_add_u32 s72, s72, 1\n\t"
      "s_max_u32 s76, s93, s94\n\t"
      "s_cmp_eq_u32 s76, -1\n\t"
      "s_cbranch_scc1 Lvo_rec\n\t"
      "Lvo_rec_r:\n\t"
      "s_waitcnt lgkmcnt(0)\n\t"
      "s_lshl_b32 s76, s72, 16\n\t"
      "s_sub_u32 s76, s95, s76\n\t"
      "s_sub_u32 s76, s76, s73\n\t"
      "s_max_i32 s76, s76, 0\n\t"
      "s_mul_i32 s76, s76, s68\n\t"
      "s_add_u32 s57, s58, s76\n\t"
      "s_add_u32 s57, s57, s93\n\t"
      "s_add_u32 s58, s57, s94\n\t"
      "s_mov_b32 s73, 0\n\t"
      "s_mov_b32 s98, 0\n\t"
      "s_mov_b32 s99, 0\n\t"
      "s_cmp_eq_u32 s72, 1\n\t"
      "s_cselect_b32 s56, 1, 0\n\t"
      "s_cselect_b32 s58, s74, s58\n\t"
      "s_cmp_lt_u32 s72, 64\n\t"
      "s_cbranch_scc1 Lvo_top\n\t"
      "s_branch Lvo_end\n\t"
      "Lvo_entry:\n\t"
      "s_cmp_eq_u32 s76, 1\n\t"
      "s_cbranch_scc1 Lvo_o\n\t"
      "s_branch Lvo_v\n\t"
      "Lvo_late:\n\t"
      "s_cmp_eq_u32 s72, 1\n\t"
      "s_cbranch_scc1 Lvo_one\n\t"
      "Lvo_late1:\n\t"
      "s_add_u32 s58, s58, s68\n\t"
      "s_add_u32 s73, s73, 1\n\t"
      "s_sub_u32 vcc_hi, vcc_hi, s68\n\t"
      "s_cmp_gt_i32 vcc_hi, 0\n\t"
      "s_cbranch_scc1 Lvo_late1\n\t"
      "Lvo_shift:\n\t"
      "s_add_u32 s76, s72, 1\n\t"
      "s_lshl_b32 s76, s76, 16\n\t"
      "s_add_u32 s76, s76, s73\n\t"
      "v_mov_b32 v210, s76\n\t"
      "s_mov_b64 exec, 1\n\t"
      "ds_max_i32 v204, v210\n\t"
      "s_mov_b64 exec, -1\n\t"
      "s_add_u32 s79, s58, s65\n\t"
      "s_cmp_lt_u32 s72, 2\n\t"
      "s_cbranch_scc1 Lvo_cwv\n\t"
      "s_add_u32 s77, s58, s65\n\t"
      "s_sub_u32 s78, s77, s61\n\t"
      "s_mul_i32 s78, s78, s59\n\t"
      "s_lshr_b32 s78, s78, 8\n\t"
      "s_add_u32 s78, s78, s60\n\t"
      "s_lshr_b32 s76, s59, 6\n\t"
      "Lvo_fv:\n\t"
      "s_memrealtime vcc\n\t"
      "s_memtime s[80:81]\n\t"
      "s_waitcnt lgkmcnt(0)\n\t"
      "s_sub_u32 vcc_hi, s77, vcc_lo\n\t"
      "s_cmp_gt_i32 vcc_hi, 20000\n\t"
      "s_cbranch_scc1 Lvo_v\n\t"
      "s_branch Lvo_fv2\n\t"
      "Lvo_cwv:\n\t"
      "s_memrealtime s[80:81]\n\t"
      "s_waitcnt lgkmcnt(0)\n\t"
      "s_sub_u32 s80, s79, s80\n\t"
      "s_sub_u32 s80, s80, 1\n\t"
      "s_cmp_lt_u32 s80, 20000\n\t"
      "s_cbranch_scc1 Lvo_cwv\n\t"
      "s_branch Lvo_v\n\t"
      "Lvo_onan:\n\t"
      "v_max_u32 v210, v130, v131\n\t"
      "v_max3_u32 v210, v210, v132, v133\n\t"
      "v_max3_u32 v210, v210, v134, v135\n\t"
      "v_max3_u32 v210, v210, v136, v137\n\t"
      "v_max3_u32 v210, v210, v138, v139\n\t"
      "v_max3_u32 v210, v210, v140, v141\n\t"
      "v_max3_u32 v210, v210, v142, v143\n\t"
      "v_max3_u32 v210, v210, v144, v145\n\t"
      "v_max3_u32 v210, v210, v146, v147\n\t"
      "v_max3_u32 v210, v210, v148, v149\n\t"
      "v_max3_u32 v210, v210, v150, v151\n\t"
      "v_max3_u32 v210, v210, v152, v153\n\t"
      "v_max3_u32 v210, v210, v154, v155\n\t"
      "v_max3_u32 v210, v210, v156, v157\n\t"
      "v_max3_u32 v210, v210, v158, v159\n\t"
      "v_max3_u32 v210, v210, v160, v161\n\t"
      "v_cmp_eq_u32 vcc, -1, v210\n\t"
      "s_cbranch_vccz Lvo_ores\n\t"
      "s_mov_b32 s97, s98\n\t"
      "s_mov_b32 s79, s76\n\t"
      "s_add_u32 s97, s97, 1\n\t"
      "s_and_b32 s76, s97, 31\n\t"
      "s_cmp_lg_u32 s76, 31\n\t"
      "s_cbranch_scc1 Lvo_sp7\n\t"
      "s_cmp_lt_u32 s97, 0x8000\n\t"
      "s_cbranch_scc1 Lvo_sp7b\n\t"
      "v_mov_b32 v210, 1\n\t"
      "buffer_store_dword v210, off, s[36:39], s67 sc1\n\t"
      "s_branch Lvo_odead\n\t"
      "Lvo_sp7b:\n\t"
      "buffer_load_dword v210, off, s[36:39], s67 sc1\n\t"
      "s_waitcnt vmcnt(0)\n\t"
      "v_readfirstlane_b32 s76, v210\n\t"
      "s_cmp_lg_u32 s76, 0\n\t"
      "s_cbranch_scc1 Lvo_odead\n\t"
      "Lvo_sp7:\n\t"
      "s_mov_b32 s98, s97\n\t"
      "v_mov_b64 v[162:163], 0\n\t"
      "v_mov_b64 v[164:165], 0\n\t"
      "v_mov_b64 v[166:167], 0\n\t"
      "v_mov_b64 v[168:169], 0\n\t"
      "v_mov_b64 v[170:171], 0\n\t"
      "v_mov_b64 v[172:173], 0\n\t"
      "v_mov_b64 v[174:175], 0\n\t"
      "v_mov_b64 v[176:177], 0\n\t"
      "s_cmp_eq_u32 s72, 1\n\t"
      "s_cbranch_scc1 Lvo_opoll\n\t"
      "s_add_u32 s83, s82, s40\n\t"
      "s_mul_i32 s79, s98, s68\n\t"
      "s_add_u32 s79, s79, s57\n\t"
      "s_sub_u32 s79, s79, s69\n\t"
      "Lvo_cwo:\n\t"
      "s_memrealtime s[80:81]\n\t"
      "s_waitcnt lgkmcnt(0)\n\t"
      "s_sub_u32 s80, s79, s80\n\t"
      "s_sub_u32 s80, s80, 1\n\t"
      "s_cmp_lt_u32 s80, 20000\n\t"
      "s_cbranch_scc1 Lvo_cwo\n\t"
      "s_branch Lvo_o\n\t"
      "Lvo_opoll:\n\t"
      "s_mov_b32 s97, 0\n\t"
      "Lvo_opoll1:\n\t"
      "s_sleep 4\n\t"
      "buffer_load_dwordx4 v[130:133], v195, s[36:39], s83 offen sc1\n\t"
      "s_waitcnt vmcnt(0)\n\t"
      "v_max_u32 v210, v130, v131\n\t"
      "v_max3_u32 v210, v210, v132, v133\n\t"
      "v_cmp_eq_u32 vcc, -1, v210\n\t"
      "s_cbranch_vccz Lvo_opoll2\n\t"
      "s_add_u32 s97, s97, 1\n\t"
      "s_and_b32 s76, s97, 31\n\t"
      "s_cmp_lg_u32 s76, 31\n\t"
      "s_cbranch_scc1 Lvo_sp8\n\t"
      "s_cmp_lt_u32 s97, 0x8000\n\t"
      "s_cbranch_scc1 Lvo_sp8b\n\t"
      "v_mov_b32 v210, 1\n\t"
      "buffer_store_dword v210, off, s[36:39], s67 sc1\n\t"
      "s_branch Lvo_opoll2\n\t"
      "Lvo_sp8b:\n\t"
      "buffer_load_dword v210, off, s[36:39], s67 sc1\n\t"
      "s_waitcnt vmcnt(0)\n\t"
      "v_readfirstlane_b32 s76, v210\n\t"
      "s_cmp_lg_u32 s76, 0\n\t"
      "s_cbranch_scc1 Lvo_opoll2\n\t"
      "Lvo_sp8:\n\t"
      "s_branch Lvo_opoll1\n\t"
      "Lvo_opoll2:\n\t"
      "s_add_u32 s83, s82, s40\n\t"
      "s_branch Lvo_o\n\t"
      "Lvo_odead:\n\t"
      "s_mov_b32 s76, s79\n\t"
      "s_branch Lvo_ores\n\t"
      "Lvo_vnan:\n\t"
      "v_max_u32 v210, v130, v131\n\t"
      "v_max3_u32 v210, v210, v132, v133\n\t"
      "v_max3_u32 v210, v210, v134, v135\n\t"
      "v_max3_u32 v210, v210, v136, v137\n\t"
      "v_max3_u32 v210, v210, v138, v139\n\t"
      "v_max3_u32 v210, v210, v140, v141\n\t"
      "v_max3_u32 v210, v210, v142, v143\n\t"
      "v_max3_u32 v210, v210, v144, v145\n\t"
      "v_cmp_eq_u32 vcc, -1, v210\n\t"
      "s_cbranch_vccz Lvo_vres\n\t"
      "s_mov_b32 s97, s98\n\t"
      "s_mov_b32 s79, s76\n\t"
      "s_add_u32 s97, s97, 1\n\t"
      "s_and_b32 s76, s97, 31\n\t"
      "s_cmp_lg_u32 s76, 31\n\t"
      "s_cbranch_scc1 Lvo_sp9\n\t"
      "s_cmp_lt_u32 s97, 0x8000\n\t"
      "s_cbranch_scc1 Lvo_sp9b\n\t"
      "v_mov_b32 v210, 1\n\t"
      "buffer_store_dword v210, off, s[36:39], s67 sc1\n\t"
      "s_branch Lvo_vdead\n\t"
      "Lvo_sp9b:\n\t"
      "buffer_load_dword v210, off, s[36:39], s67 sc1\n\t"
      "s_waitcnt vmcnt(0)\n\t"
      "v_readfirstlane_b32 s76, v210\n\t"
      "s_cmp_lg_u32 s76, 0\n\t"
      "s_cbranch_scc1 Lvo_vdead\n\t"
      "Lvo_sp9:\n\t"
      "s_mov_b32 s98, s97\n\t"
      "v_mov_b64 v[178:179], 0\n\t"
      "v_mov_b64 v[180:181], 0\n\t"
      "v_mov_b64 v[182:183], 0\n\t"
      "v_mov_b64 v[184:185], 0\n\t"
      "v_mov_b64 v[186:187], 0\n\t"
      "v_mov_b64 v[188:189], 0\n\t"
      "v_mov_b64 v[190:191], 0\n\t"
      "v_mov_b64 v[192:193], 0\n\t"
      "s_add_u32 s58, s58, s68\n\t"
      "s_add_u32 s73, s73, 1\n\t"
      "s_waitcnt vmcnt(0)\n\t"
      "s_branch Lvo_shift\n\t"
      "Lvo_vdead:\n\t"
      "s_mov_b32 s76, s79\n\t"
      "s_branch Lvo_vres\n\t"
      "Lvo_one:\n\t"
      "s_mov_b32 s97, 0\n\t"
      "s_cmp_eq_u32 s71, 0\n\t"
      "s_cbranch_scc1 Lvo_one1\n\t"
      "s_lshr_b32 s58, s71, 20\n\t"
      "s_add_u32 s58, s58, s63\n\t"
      "s_min_u32 s58, s58, -2\n\t"
      "v_mov_b32 v210, s58\n\t"
      "v_mov_b32 v211, 0\n\t"
      "s_and_b32 s76, s71, 1023\n\t"
      "v_mov_b32 v212, s76\n\t"
      "s_bfe_u32 s76, s71, 0xa000a\n\t"
      "v_mov_b32 v213, s76\n\t"
      "s_mov_b64 exec, 1\n\t"
      "buffer_store_dwordx4 v[210:213], off, s[36:39], s84 sc1\n\t"
      "s_mov_b64 exec, -1\n\t"
      "s_branch Lvo_one2\n\t"
      "Lvo_one1:\n\t"
      "buffer_load_dword v210, off, s[36:39], s84 sc1\n\t"
      "s_waitcnt vmcnt(0)\n\t"
      "v_readfirstlane_b32 s58, v210\n\t"
      "s_cmp_lg_u32 s58, -1\n\t"
      "s_cbranch_scc1 Lvo_one2\n\t"
      "s_add_u32 s97, s97, 1\n\t"
      "s_and_b32 s76, s97, 31\n\t"
      "s_cmp_lg_u32 s76, 31\n\t"
      "s_cbranch_scc1 Lvo_sp10\n\t"
      "s_cmp_lt_u32 s97, 0x8000\n\t"
      "s_cbranch_scc1 Lvo_sp10b\n\t"
      "v_mov_b32 v210, 1\n\t"
      "buffer_store_dword v210, off, s[36:39], s67 sc1\n\t"
      "s_branch Lvo_one2\n\t"
      "Lvo_sp10b:\n\t"
      "buffer_load_dword v210, off, s[36:39], s67 sc1\n\t"
      "s_waitcnt vmcnt(0)\n\t"
      "v_readfirstlane_b32 s76, v210\n\t"
      "s_cmp_lg_u32 s76, 0\n\t"
      "s_cbranch_scc1 Lvo_one2\n\t"
      "Lvo_sp10:\n\t"
      "s_sleep 2\n\t"
      "s_branch Lvo_one1\n\t"
      "Lvo_one2:\n\t"
      "s_add_u32 s79, s58, s65\n\t"
      "s_branch Lvo_cwv\n\t"
      "Lvo_rec:\n\t"
      "s_mov_b32 s97, 0\n\t"
      "Lvo_rec1:\n\t"
      "s_sleep 2\n\t"
      "buffer_load_dwordx2 v[220:221], off, s[36:39], s84 offset:8 sc1\n\t"
      "s_waitcnt vmcnt(0)\n\t"
      "v_readfirstlane_b32 s93, v220\n\t"
      "v_readfirstlane_b32 s94, v221\n\t"
      "s_max_u32 s76, s93, s94\n\t"
      "s_cmp_lg_u32 s76, -1\n\t"
      "s_cbranch_scc1 Lvo_rec_r\n\t"
      "s_add_u32 s97, s97, 1\n\t"
      "s_and_b32 s76, s97, 31\n\t"
      "s_cmp_lg_u32 s76, 31\n\t"
      "s_cbranch_scc1 Lvo_sp11\n\t"
      "s_cmp_lt_u32 s97, 0x8000\n\t"
      "s_cbranch_scc1 Lvo_sp11b\n\t"
      "v_mov_b32 v210, 1\n\t"
      "buffer_store_dword v210, off, s[36:39], s67 sc1\n\t"
      "s_branch Lvo_rec_r\n\t"
      "Lvo_sp11b:\n\t"
      "buffer_load_dword v210, off, s[36:39], s67 sc1\n\t"
      "s_waitcnt vmcnt(0)\n\t"
      "v_readfirstlane_b32 s76, v210\n\t"
      "s_cmp_lg_u32 s76, 0\n\t"
      "s_cbranch_scc1 Lvo_rec_r\n\t"
      "Lvo_sp11:\n\t"
      "s_branch Lvo_rec1\n\t"
      "Lvo_end:\n\t"
      "s_waitcnt vmcnt(0) lgkmcnt(0)\n\t"
      :
      : "{a[0:31]}"(WA[0]), "{a[32:63]}"(WA[1]), "{a[64:95]}"(WA[2]), "{a[96:127]}"(WA[3]), "{a[128:159]}"(WA[4]), "{a[160:191]}"(WA[5]), "{a[192:223]}"(WA[6]), "{a[224:255]}"(WA[7]),
        "{v[2:33]}"(WV[0]), "{v[34:65]}"(WV[1]), "{v[66:97]}"(WV[2]), "{v[98:129]}"(WV[3]), "{v[194:209]}"(C), "{s[36:39]}"(rs_o), "{s[40:55]}"(S), "{s[64:71]}"(Q)
      : R8(v, 13), R8(v, 14), R8(v, 15), R8(v, 16), R8(v, 17), R8(v, 18), "v190", "v191", "v192", "v193", "v210", "v211", "v212", "v213", "v214", "v215", "v216", "v217", "v218", "v219", "v220", "v221", "v222", "v223", "v224", "v225",
        "s56", "s57", "s58", "s59", "s60", "s61", "s62", "s63", "s72", "s73", "s74", "s75", "s76", "s77", "s78", "s79", R8(s, 8), R8(s, 9), "vcc", "scc", "memory"
      );
  }
  leave_epoch(P);
}
__device__ __attribute__((used, noinline)) void code_end_marker() { asm volatile("Lfused_end:"); }

extern "C" void kernel_launch(void* const* d_in, const int* in_sizes, int n_in,
                              void* d_out, int out_size, void* d_ws, size_t ws_size,
                              hipStream_t stream) {
  const u16* A_x = (const u16*)d_in[0];
  const u16* A_mem0 = (const u16*)d_in[1];
  const u16* A_Wq = (const u16*)d_in[2];
  const u16* A_Wk = (const u16*)d_in[3];
  const u16* A_Wv = (const u16*)d_in[4];
  const u16* A_Wo = (const u16*)d_in[5];
  const u16* B_x = (const u16*)d_in[7];
  const u16* B_wqkv = (const u16*)d_in[8];
  const u16* B_wo = (const u16*)d_in[9];
  u16* out = (u16*)d_out;

  char* ws = (char*)d_ws;
  size_t off = 0;
  auto alloc = [&](size_t bytes) { char* p = ws + off; off += (bytes + 255) & ~(size_t)255; return p; };
  off = (size_t)(-(long long)(size_t)ws) & ((1u << 21) - 1);
  char* obuf = alloc(CBASE);
  Ctl* ctl = (Ctl*)alloc(sizeof(Ctl));
  Hdr* hdr = (Hdr*)alloc(sizeof(Hdr));
  unsigned long long* prof = (unsigned long long*)alloc(96 * 4 * 72 * 8 + 8 * 256 * 16 + 64 * 384 * 8);

  FParams cp;
  cp.Ax = A_x; cp.Bx = B_x; cp.Wq = A_Wq; cp.Wk = A_Wk; cp.Wv = A_Wv; cp.Wo = A_Wo; cp.wqkvB = B_wqkv; cp.woB = B_wo;
  cp.obuf = (u16*)obuf; cp.hdr = hdr; cp.mem0 = A_mem0; cp.out = out;
  cp.ctl = ctl; cp.prof = prof;
  cp.dq = DQ; cp.dq2 = DQ2; cp.dmv = DMV; cp.dov = DOV; cp.gain = GAIN; cp.inc = MINC; cp.dec = MDEC; cp.rec = RECOV; cp.stag = STAG; cp.sh1 = SH1; cp.sh2 = SH2; cp.sh3 = SH3; cp.dv = DVV; cp.pad = RANKM; cp.stag2 = STAG2; cp.pad2 = FLAT;
  k_fused<<<256, 256, 0, stream>>>(cp);
}
```

```cpp
#include <hip/hip_runtime.h>
#include <hip/hip_bf16.h>
#include <stdint.h>
#include <stddef.h>

#define DQ 12
#define DQ2 12
#define DMV 118
#define DOV 76
#define GAIN 4
#define MINC 16
#define MDEC 4
#define RECOV 70
#define STAG 6
#define O2D 7
#define KVLATE 0
#define WD 2
#define NT 0
#define OUTAUX 0
#define ALQ 2
#define ALO 2
#define ALQ2 0
#define ALO2 5
#define DIRN 1
#define QTJ 2
#define VROT 0
#define PLIM 48u
#define FROW 0
#define FBS 58
#define PSL2 4
#define PSL 6
#define STAG2 (8 + (16 << 8))
#define FLAT 110
#define FINE 3
#define OFFM 16.f
#define SH1 96
#define SH2 8
#define SH3 (10 + (13 << 8) + (8 << 16))
#define DVV 12
#define RANKM 24
#define TKS 5
#define DECIDER 1
#define VSLOT 17
typedef unsigned short u16;
typedef __attribute__((ext_vector_type(4))) float f32x4;
typedef unsigned v4u __attribute__((ext_vector_type(4)));

constexpr int S_ = 2048, DM = 1024, HA = 32, DHA = 128, HDA = 4096, MEM = 8, SEG = 32, NSEG = 64;
constexpr int NQKVA = 3 * HDA;
constexpr int LDO = 2 * HDA;
constexpr int DB = 1024, HB = 32, DHB = 32, NQKVB = 3 * DB;

#define PROBES 8
#define PNEAR 590u
#define SPROBES 6
constexpr int OREG = 262144, MREG = 65536, NPAGE = 84, NPROBER = 64;
struct Plc { unsigned long long e, x; };
__device__ __forceinline__ int nearo(const Plc pm, int off) { return off + (int)(((unsigned)(pm.e >> (off >> 22)) ^ ((unsigned)(pm.x >> (off >> 22)) & (off >> 21))) & 1u) * 8192; }
__device__ __forceinline__ int faro(const Plc pm, int off) { return nearo(pm, off) ^ 8192; }
__device__ __forceinline__ int sideo(const Plc pm, int off, int sd) { const int pg = off >> 21; return nearo(pm, off) + (int)(((unsigned)(pm.e >> (48 + (pg < 15 ? pg : 15))) ^ (off >> 19) ^ sd) & 1u) * 16384; }
__device__ __forceinline__ int hside(int h) { return (unsigned)(h - 12) >= 12u; }
constexpr int MB0 = 0, OB0 = NSEG * MREG, VB0 = OB0 + NSEG * OREG, RC0 = VB0 + NSEG * OREG  , CBASE = RC0 + 32768;
constexpr int QA0 = 20 << 21, RB0 = QA0 + (48 << 21), PT0 = RB0 + (16 << 21), ARENA = PT0;
typedef __attribute__((ext_vector_type(8))) __bf16 bfx8;
typedef __attribute__((ext_vector_type(2))) __bf16 bfx2;
typedef __attribute__((ext_vector_type(2))) float f32x2;
typedef unsigned v2u __attribute__((ext_vector_type(2)));
typedef unsigned v8u __attribute__((ext_vector_type(8)));
constexpr int NQK = 32, NVO = 64;
constexpr int NLW = 12, VXLD = 144;
constexpr int IMGW = 576, T0 = 16384, KIMG = 4 * IMGW, PIMG = 8 * IMGW, KB0 = 23104, KVB = 16512, SW0 = KB0 + 2 * KVB;
#define R8(c, n) #c #n "0", #c #n "1", #c #n "2", #c #n "3", #c #n "4", #c #n "5", #c #n "6", #c #n "7", #c #n "8", #c #n "9"
#define V162_225 "v162", "v163", "v164", "v165", "v166", "v167", "v168", "v169", R8(v, 17), R8(v, 18), R8(v, 19), R8(v, 20), R8(v, 21), "v220", "v221", "v222", "v223", "v224", "v225"
typedef unsigned v16u __attribute__((ext_vector_type(16)));
typedef unsigned v32u __attribute__((ext_vector_type(32)));

__device__ __forceinline__ void lds_barrier() { asm volatile("s_waitcnt lgkmcnt(0)" ::: "memory"); __builtin_amdgcn_s_barrier(); asm volatile("" ::: "memory"); }
template <int N> __device__ __forceinline__ float dpp_ror(float v) {
  return __int_as_float(__builtin_amdgcn_update_dpp(0, __float_as_int(v), 0x120 + N, 0xf, 0xf, false));
}
__device__ __forceinline__ unsigned pk2(float a, float b) { f32x2 t = {a, b}; return __builtin_bit_cast(unsigned, __builtin_convertvector(t, bfx2)); }
__device__ __forceinline__ unsigned hi2(float a, float b) { return __builtin_amdgcn_perm(__float_as_uint(b), __float_as_uint(a), 0x07060302u); }
__device__ __forceinline__ unsigned nosent(unsigned x) { return x < 0xfffffffeu ? x : 0xfffffffeu; }
__device__ __forceinline__ unsigned umax3(unsigned a, unsigned b, unsigned c) { unsigned m = a > b ? a : b; return m > c ? m : c; }
__device__ __forceinline__ unsigned chk4(unsigned m, v4u v) { return umax3(umax3(m, v.x, v.y), v.z, v.w); }
__device__ __forceinline__ unsigned long long now() { return __builtin_amdgcn_s_memtime(); }
__device__ __forceinline__ f32x4 mf(v4u a, v4u b, f32x4 c) {
  return __builtin_amdgcn_mfma_f32_16x16x32_bf16(__builtin_bit_cast(bfx8, a), __builtin_bit_cast(bfx8, b), c, 0, 0, 0);
}
__device__ __forceinline__ v2u trrd(const u16* p) {
  typedef __attribute__((ext_vector_type(4))) short v4i16_t;
  return __builtin_bit_cast(v2u, __builtin_amdgcn_ds_read_tr16_b64_v4i16((__attribute__((address_space(3))) v4i16_t*)p));
}
__device__ __forceinline__ v8u cat8(v4u a, v4u b) { return __builtin_shufflevector(a, b, 0, 1, 2, 3, 4, 5, 6, 7); }
__device__ __forceinline__ float xmax16(float x) { auto r = __builtin_amdgcn_permlane16_swap(__float_as_uint(x), __float_as_uint(x), false, false); return fmaxf(__uint_as_float(r[0]), __uint_as_float(r[1])); }
__device__ __forceinline__ float xmax32(float x) { auto r = __builtin_amdgcn_permlane32_swap(__float_as_uint(x), __float_as_uint(x), false, false); return fmaxf(__uint_as_float(r[0]), __uint_as_float(r[1])); }
__device__ __forceinline__ float xsum16(float x) { auto r = __builtin_amdgcn_permlane16_swap(__float_as_uint(x), __float_as_uint(x), false, false); return __uint_as_float(r[0]) + __uint_as_float(r[1]); }
__device__ __forceinline__ float xsum32(float x) { auto r = __builtin_amdgcn_permlane32_swap(__float_as_uint(x), __float_as_uint(x), false, false); return __uint_as_float(r[0]) + __uint_as_float(r[1]); }
__device__ __forceinline__ int dbl(int off) { return ((off >> 13) << 14) | (off & 8191); }
__device__ __forceinline__ int quad(int off) { return ((off >> 13) << 15) | (off & 8191); }
__device__ __forceinline__ int tr4(int p) { return (p & 3) * 4 + (p >> 2); }
__device__ __forceinline__ int mpos(int x, int j) { return (x == 0 ? 4 - j : 5 * x + j) & 15; }
__device__ __forceinline__ int mchunk(int x, int j) { return tr4(mpos(x, j)); }
__device__ __forceinline__ bool spin_fail(unsigned spins, unsigned* tmo, unsigned code) {
  if ((spins & 31u) != 31u) return false;
  if (spins > (1u << 15)) { __hip_atomic_store(tmo, code, __ATOMIC_RELAXED, __HIP_MEMORY_SCOPE_AGENT); return true; }
  return __builtin_amdgcn_readfirstlane(__hip_atomic_load(tmo, __ATOMIC_RELAXED, __HIP_MEMORY_SCOPE_AGENT)) != 0u;
}
#define SB0 __builtin_amdgcn_sched_barrier(0)
__device__ __forceinline__ int opaque_lane() { int t; asm volatile("v_mbcnt_lo_u32_b32 %0, -1, 0\n\tv_mbcnt_hi_u32_b32 %0, -1, %0" : "=v"(t)); return t; }
#define opaque_tid() (opaque_lane() + 64 * wv)

#define PACEQ asm volatile("s_nop 7\n\ts_nop 3")
#define PACE asm volatile("s_nop 7\n\ts_nop 3")

template <class T> __device__ __forceinline__ T* uni(T* p) {
  const unsigned long long v = (unsigned long long)p;
  return (T*)(((unsigned long long)(unsigned)__builtin_amdgcn_readfirstlane((unsigned)(v >> 32)) << 32) | (unsigned)__builtin_amdgcn_readfirstlane((unsigned)v));
}
namespace wg {
constexpr int A_B = 128 * 128, STAGE_B = 384 * 128;
#define LATE_SIGNAL do { if (tid < pn) __hip_atomic_fetch_add(psig + 64 * tid, 1u, __ATOMIC_RELAXED, __HIP_MEMORY_SCOPE_AGENT); } while (0)
#define EARLY_PICK unsigned tn = 0; if (qh && tid == 0) tn = __hip_atomic_fetch_add(qh, 1u, __ATOMIC_RELAXED, __HIP_MEMORY_SCOPE_AGENT); SB0
#define END_TILE do { SB0; if (tid == 0) *(unsigned*)(lds + 3 * STAGE_B) = tn; lds_barrier(); } while (0)
constexpr int A2_B = 256 * 128, B2_B = 32 * 512, B2_0 = 3 * A2_B;
__device__ __forceinline__ void gemm_big(const u16* A, int lda, const u16* B, int ldb, void* C, int ldc, int cbs, int adel, int cdel  , bool frag, char* lds, unsigned* psig, int pn, unsigned* qh, int wv) {
  const int tid = opaque_tid(), lane = tid & 63, w = wv, wm = w >> 1, wn = w & 1;
  const int l15 = lane & 15, lq = lane >> 4;
  constexpr int nk = 16;
  A = uni(A); B = uni(B); C = uni(C);
  lda = __builtin_amdgcn_readfirstlane(lda); ldb = __builtin_amdgcn_readfirstlane(ldb); ldc = __builtin_amdgcn_readfirstlane(ldc); cbs = __builtin_amdgcn_readfirstlane(cbs);
  const __amdgpu_buffer_rsrc_t rsa = __builtin_amdgcn_make_buffer_rsrc((void*)A, 0, (255 * lda + nk * 64) * 2 + __builtin_amdgcn_readfirstlane(adel), 0x00020000);
  const __amdgpu_buffer_rsrc_t rsb = __builtin_amdgcn_make_buffer_rsrc((void*)B, 0, ((nk * 64 - 1) * ldb + 256) * 2, 0x00020000);
  int va[2], vb[2];
  { const int c0 = (lane & 7) ^ (lane >> 4), cb0 = (lane & 31) ^ ((lane >> 5) << 1) ^ ((w & 1) << 3);
#pragma unroll
    for (int i = 0; i < 2; ++i) { va[i] = ((64 * w + (lane >> 3)) * lda + (c0 ^ (4 * i)) * 8) * 2 + wm * adel; vb[i] = ((8 * w + (lane >> 5)) * ldb + (cb0 ^ (4 * i)) * 8) * 2; } }
  const int sa = 16 * lda, sb = 4 * ldb, skb = 64 * ldb;
  typedef __attribute__((address_space(3))) void* ldsp_t;
#define PA(q, ka, la) __builtin_amdgcn_raw_ptr_buffer_load_lds(rsa, (ldsp_t)(lds + (la) + (64 * w + 8 * (q)) * 128), 16, va[(q) & 1], (ka) + (q) * sa, 0, 0)
#define PB(q, kb, lb) __builtin_amdgcn_raw_ptr_buffer_load_lds(rsb, (ldsp_t)(lds + B2_0 + (lb) + (8 * w + 2 * (q)) * 512), 16, vb[(q) & 1], (kb) + (q) * sb, 0, 0)
#pragma unroll
  for (int q = 0; q < 8; ++q) PA(q, 0, 0);
#pragma unroll
  for (int g = 0; g < 4; ++g) {
#pragma unroll
    for (int q = 0; q < 4; ++q) PB(q, g * skb, g * B2_B);
    if (g > 0)
#pragma unroll
      for (int q = 0; q < 4; ++q) PA(4 * ((g - 1) & 1) + q, (1 + (g - 1) / 2) * 128, (1 + (g - 1) / 2) * A2_B);
  }
  f32x4 acc[8][8];
#pragma unroll
  for (int j = 0; j < 8; ++j) {
#pragma unroll
    for (int i = 0; i < 8; ++i) acc[i][j] = (f32x4){0.f, 0.f, 0.f, 0.f};
    asm volatile("" : "+a"(acc[0][j]), "+a"(acc[1][j]), "+a"(acc[2][j]), "+a"(acc[3][j]), "+a"(acc[4][j]), "+a"(acc[5][j]), "+a"(acc[6][j]), "+a"(acc[7][j]));
  }
  v4u fa[2][8]; v2u bl[2][8], bh[2][8]; unsigned ra[2], rb[8];
  { const unsigned lds0 = (unsigned)(size_t)lds; const int sw = (l15 >> 1) & 7, hh = (l15 >> 2) | ((lq & 1) << 2);
    ra[0] = lds0 + (128 * wm + l15) * 128 + ((lq ^ sw) << 4); ra[1] = lds0 + (128 * wm + l15) * 128 + (((4 + lq) ^ sw) << 4);
#pragma unroll
    for (int i = 0; i < 8; ++i) rb[i] = lds0 + B2_0 + (8 * lq + (l15 >> 2)) * 512 + ((((16 * wn + 2 * i) ^ (2 * hh)) + ((l15 >> 1) & 1)) << 4) + (l15 & 1) * 8; }
#define RDB(ks, i) asm volatile("ds_read_b64_tr_b16 %0, %2\n\tds_read_b64_tr_b16 %1, %2 offset:2048" : "=&v"(bl[ks][i]), "=&v"(bh[ks][i]) : "v"(rb[i]) : "memory")
#define RDA(ks, j) asm volatile("ds_read_b128 %0, %1 offset:%2" : "=&v"(fa[ks][j]) : "v"(ra[ks]), "i"((j) * 16 * 128) : "memory")
#define RDF(ks, r) do { if ((r) == 0) RDA(ks, 0); else if ((r) < 9) RDB(ks, (r) - 1); else RDA(ks, (r) - 8); } while (0)
#define WAITF(ks, txt) asm volatile(txt : "+v"(bl[ks][0]), "+v"(bh[ks][0]), "+v"(bl[ks][1]), "+v"(bh[ks][1]), "+v"(bl[ks][2]), "+v"(bh[ks][2]), "+v"(bl[ks][3]), "+v"(bh[ks][3]), \
    "+v"(bl[ks][4]), "+v"(bh[ks][4]), "+v"(bl[ks][5]), "+v"(bh[ks][5]), "+v"(bl[ks][6]), "+v"(bh[ks][6]), "+v"(bl[ks][7]), "+v"(bh[ks][7]), \
    "+v"(fa[ks][0]), "+v"(fa[ks][1]), "+v"(fa[ks][2]), "+v"(fa[ks][3]), "+v"(fa[ks][4]), "+v"(fa[ks][5]), "+v"(fa[ks][6]), "+v"(fa[ks][7]) :: "memory")
  asm volatile("s_waitcnt vmcnt(24)\n\ts_barrier" ::: "memory");
  LATE_SIGNAL;
#pragma unroll
  for (int r = 0; r < 16; ++r) RDF(0, r);
#pragma unroll
  for (int i = 0; i < 8; ++i) rb[i] += B2_B;
  int la = 2 * A2_B, ka = 2 * 128, lb = 0, kb = 4 * skb, lra = 0, da = 0, db = 0;
  WAITF(0, "s_waitcnt lgkmcnt(0) vmcnt(16)\n\ts_barrier");
  int kt = nk;
  do {
#pragma unroll
    for (int ks = 0; ks < 2; ++ks) {
      SB0;
#pragma unroll
      for (int u = 0; u < 64; ++u) {
        const int j = u >> 3, i = u & 7;
        asm volatile("v_mfma_f32_16x16x32_bf16 %0, %1, %2, %0" : "+a"(acc[i][j]) : "v"((v4u){bl[ks][i].x, bl[ks][i].y, bh[ks][i].x, bh[ks][i].y}), "v"(fa[ks][j]));
        SB0;
        if (u % 3 == 0 && u < 48) { RDF(ks ^ 1, u / 3); SB0; }
        if (u % 6 == 1 && u < 48) {
          asm volatile("" : "+s"(la), "+s"(ka), "+s"(lb), "+s"(kb));
          if ((u / 6) & 1) PA((ks ^ 1) * 4 + (u / 12), ka, la); else PB(u / 12, kb, lb);
          SB0;
        }
        if (u == 48) {
          asm volatile("" : "+s"(lb), "+s"(kb)); lb = (lb + B2_B) & (4 * B2_B - 1); db = lb == 3 * B2_B ? -3 * B2_B : B2_B; kb += skb; asm volatile("" : "+s"(lb), "+s"(kb), "+s"(db)); SB0; }
        if (u > 48 && u <= 56) { rb[u - 49] += db; asm volatile("" : "+v"(rb[u - 49])); SB0; }
        if (ks == 0 && u == 57) { asm volatile("" : "+s"(lra), "+s"(ka)); la = lra; da = lra == 2 * A2_B ? -2 * A2_B : A2_B; lra += da; ka += 128; asm volatile("" : "+s"(lra), "+s"(la), "+s"(da), "+s"(ka)); SB0; }
        if (ks == 0 && (u == 58 || u == 59)) { ra[u - 58] += da; asm volatile("" : "+v"(ra[u - 58])); SB0; }
        if (u == 60) { db = kt == 3 ? 0x7f000000 : 0; asm volatile("" : "+s"(db)); SB0; }
        if (ks == 0 && (u == 61 || u == 62)) { va[u - 61] |= db; asm volatile("" : "+v"(va[u - 61])); SB0; }
        if (ks == 1 && (u == 61 || u == 62)) { vb[u - 61] |= db; asm volatile("" : "+v"(vb[u - 61])); SB0; }
      }
      if (ks == 0) WAITF(1, "s_waitcnt lgkmcnt(0) vmcnt(16)\n\ts_barrier"); else WAITF(0, "s_waitcnt lgkmcnt(0) vmcnt(16)\n\ts_barrier");
    }
  } while (--kt > 0);
  asm volatile("s_nop 7" : "+a"(acc[4][7]), "+a"(acc[5][7]), "+a"(acc[6][7]), "+a"(acc[7][7]));
  SB0;
#undef PA
#undef PB
#undef RDB
#undef RDA
#undef RDF
#undef WAITF
  asm volatile("s_waitcnt vmcnt(0)" ::: "memory");
  __builtin_amdgcn_s_barrier();
  asm volatile("" ::: "memory");
  EARLY_PICK;
  char* st = lds + w * 32768;
  {
    const __amdgpu_buffer_rsrc_t rs = __builtin_amdgcn_make_buffer_rsrc(C, 0, ((ldc == DHA ? 479 : 255) * ldc + cbs + 128) * 2 + __builtin_amdgcn_readfirstlane(cdel), 0x00020000);
    const int rs2 = ldc == DHA ? 16384 : 64 * ldc;
    const int rd = frag ? l15 * 256 + ((lq ^ l15) << 4) : lq * 256 + ((l15 ^ lq) << 4);
    const int go = wm * (4 * rs2 + cdel) + wn * cbs * 2 + (frag ? lane * 16 : (lq * ldc + l15 * 8) * 2);
    const int rstep = frag ? 0 : 1024, gstep = frag ? 1024 : 8 * ldc;
    v4u v[4];
#pragma unroll
    for (int j = 0; j < 10; ++j) {
      if (j >= 2)
#pragma unroll
        for (int q = 0; q < 4; ++q) __builtin_amdgcn_raw_buffer_store_b128(v[q], rs, go, ((j - 2) >> 1) * rs2 + ((j - 2) & 1) * 32 * ldc + q * gstep, 16);
      SB0;
      if (j >= 1 && j < 9)
#pragma unroll
        for (int q = 0; q < 4; ++q) v[q] = *(const v4u*)(st + (j - 1) * 4096 + ((rd + q * rstep) ^ (q * 64)));
      SB0;
      if (j < 8) {
        asm volatile("" : "+a"(acc[0][j]), "+a"(acc[1][j]), "+a"(acc[2][j]), "+a"(acc[3][j]), "+a"(acc[4][j]), "+a"(acc[5][j]), "+a"(acc[6][j]), "+a"(acc[7][j]));
#pragma unroll
        for (int i = 0; i < 8; ++i)
          *(v2u*)(st + (j * 16 + l15) * 256 + (((2 * i + (lq >> 1)) ^ l15) << 4) + (lq & 1) * 8) = (v2u){pk2(acc[i][j][0], acc[i][j][1]), pk2(acc[i][j][2], acc[i][j][3])};
      }
      SB0;
    }
  }
  END_TILE;
}
}

struct Hdr {
  unsigned long long epoch[16], token[16];
  unsigned long long flag0[64];
  unsigned long long vote[NPROBER];
  unsigned long long fill[NPROBER];
};
struct Ctl {
  unsigned qhead[8][32];
  unsigned prog[32][32];
  unsigned cntB[16][32];
  unsigned cntAB[16][32];
  unsigned tticket[64][4][16];
  unsigned flagP[8][72][4];
  unsigned pseg[32][16];
  unsigned tmo[32];
  unsigned done[32];
};
constexpr int HBASE = CBASE + (((int)sizeof(Ctl) + 255) & ~255);
struct FParams {
  const u16 *Ax, *Bx, *Wq, *Wk, *Wv, *Wo, *wqkvB, *woB;
  u16 *obuf, *out;
  Ctl* ctl; unsigned long long* prof; Hdr* hdr; const u16* mem0;
  int dq, dq2, dmv, dov, gain, inc, dec, rec, stag, sh1, sh2, sh3, dv, pad, stag2, pad2;
};
#define RLX_AGENT __ATOMIC_RELAXED, __HIP_MEMORY_SCOPE_AGENT
__device__ __forceinline__ unsigned ld_agent(const unsigned* p) { return __hip_atomic_load(p, RLX_AGENT); }
__device__ __forceinline__ unsigned long long ld_agent(const unsigned long long* p) { return __hip_atomic_load(p, RLX_AGENT); }
__device__ __forceinline__ unsigned long long uni64(unsigned long long v) { return (unsigned long long)uni((char*)v); }
__device__ __forceinline__ void wait_token(const FParams& F, unsigned long long tok) {
  for (unsigned spins = 0; uni64(ld_agent(&F.hdr->token[0])) != tok && spins < (1u << 16); ++spins) __builtin_amdgcn_s_sleep(8);
  asm volatile("" ::: "memory");
}
__device__ __forceinline__ void leave_epoch(const FParams& F, int wv) {
  __syncthreads();
  if (opaque_tid() == 0 && __hip_atomic_fetch_add(&F.ctl->done[0], 1u, RLX_AGENT) == 255u) __hip_atomic_fetch_add(&F.hdr->epoch[0], 1ull, RLX_AGENT);
}
__device__ __forceinline__ Plc count_votes(unsigned long long v) {
  const unsigned long long mk = (1ull << (NPAGE / 2)) - 1, e = __ballot(v & 1) & mk, o = __ballot(v & 2) & mk;
  const int n = __builtin_popcountll(e) + __builtin_popcountll(~o & mk);
  const unsigned m = __ballot(v & 1) >> 48, l = (m << 1) | (m & 1), r = (m >> 1) | (m & 0x8000), c = __builtin_popcount(m);
  const unsigned long long sd = (unsigned long long)(c >= 13 ? 0xffffu : (c <= 3 ? 0u : (m & l) | (m & r) | (l & r))) << 48;
  return n >= NPAGE - 12 ? Plc{mk | sd, ~0ull} : (n <= 12 ? Plc{sd, ~0ull} : Plc{e | sd, e ^ o});
}
__device__ __forceinline__ Plc wait_votes(const FParams& F, unsigned long long tok, int lane, bool all) {
  unsigned long long v;
  for (unsigned spins = 0; spins < (1u << 15); ++spins) {
    v = ld_agent(&F.hdr->vote[lane]);
    const unsigned long long f = all ? ld_agent(&F.hdr->fill[lane]) : tok;
    if (__all((v | 3) == 4 * tok + 3 && f == tok)) break;
    __builtin_amdgcn_s_sleep(32);
  }
  asm volatile("" ::: "memory");
  return count_votes(v);
}
__device__ __forceinline__ u16* wsp(const FParams& F, int off) { return (u16*)((char*)F.obuf + off); }
__device__ __forceinline__ void wg_wait(const unsigned* p, unsigned need, Ctl* ctl, bool acq, int wv) {
  asm volatile("" : "+s"(wv));
  if (wv == 0) {
    for (unsigned spins = 0;; ++spins) {
      const bool ok = (p == nullptr) || ld_agent(p) >= need;
      if (__all(ok)) break;
      if (spin_fail(spins, &ctl->tmo[0], 2u)) break;
      __builtin_amdgcn_s_sleep(16);
    }
    if (acq) __builtin_amdgcn_fence(__ATOMIC_ACQUIRE, "agent");
    asm volatile("s_waitcnt vmcnt(0)" ::: "memory");
  }
  __syncthreads();
}
__device__ __forceinline__ void wg_signal(unsigned* p, int wv) { if (opaque_tid() == 0) __hip_atomic_fetch_add(p, 1u, RLX_AGENT); }

template <bool FUSED> __device__ __forceinline__ void attnA(const FParams& F, const Plc pm, int s0, int s1, int st, int h, unsigned lb  , unsigned lo) {
  asm volatile("" : "+s"(h), "+s"(lb), "+s"(lo));
  const int lane = opaque_lane(), l15 = lane & 15, lq = lane >> 4, o0 = QA0 + (3 * h << 20);
  v8u C; v4u M; v16u S;
  C[0] = lane * 16; C[1] = (lane ^ ((lane & 16) >> 1)) * 16; C[2] = (lq & 1) * 256 + ((l15 & 13) + 2 * (lq >> 1)) * 16; C[3] = (l15 >> 3) * 256 + lq * 64 + ((l15 & 4) * 2 + (l15 & 3)) * 4;
  C[4] = 512 * lq + 508; C[5] = lb + (l15 & 3) * 256 + ((4 * lq + (l15 >> 2)) ^ ((l15 & 1) * 8)) * 16; C[6] = FUSED ? lo + lane * 16 : (lane == 0 ? 0 : 0x7ffff000); C[7] = 1;
#pragma unroll
  for (int e = 0; e < 4; ++e) M[e] = 4 * lq + e <= l15 ? 0u : 0xff800000u;
  const unsigned long long pl2 = __ballot((sideo(pm, (lane >> 1) << 21, hside(h) ^ 1) >> (13 + (lane & 1))) & 1);
  S[0] = (unsigned)pl2; S[1] = (unsigned)(pl2 >> 32); S[2] = faro(pm, o0) - o0; S[3] = faro(pm, o0 + (1 << 20)) - o0; S[4] = faro(pm, o0 + (2 << 20)) - o0; S[5] = lb; S[6] = quad(h * 2048);
  S[7] = CBASE + (int)offsetof(Ctl, tmo); S[8] = 0x3e0293eeu  ; S[9] = s0; S[10] = s1; S[11] = st; S[12] = CBASE + (int)offsetof(Ctl, flagP) + (h >> 2) * 1152 + (h & 3) * 4; S[13] = S[14] = S[15] = 0;
#pragma unroll
  for (int k = 0; k < 16; ++k) S[k] = __builtin_amdgcn_readfirstlane(S[k]);
#define ATT_OPS : "+{v[240:247]}"(C), "+{s[44:59]}"(S) \
      : "{v[248:251]}"(M), "{s[36:39]}"(__builtin_amdgcn_make_buffer_rsrc(uni(wsp(F, o0)), 0, 3 << 20, 0x00020000)), "{s[40:43]}"(__builtin_amdgcn_make_buffer_rsrc(F.obuf, 0, HBASE, 0x00020000)) \
      : "v9", R8(v, 1), R8(v, 2), R8(v, 3), R8(v, 4), R8(v, 5), R8(v, 6), R8(v, 7), R8(v, 8), R8(v, 9), R8(v, 10), R8(v, 11), R8(v, 12), R8(v, 13), R8(v, 14), R8(v, 15), \
        "v176", "v177", "v178", "v179", R8(v, 18), R8(v, 19), R8(v, 20), R8(v, 21), R8(v, 22), R8(v, 23), R8(s, 6), "s70", "s71", "s72", "s73", "s74", "vcc", "scc", "m0", "memory"
  if (FUSED) asm volatile(
      "v_mov_b32 v91, 0\n\t"
      "v_mov_b32 v93, 0\n\t"
      "v_mov_b32 v95, 0\n\t"
      "v_mov_b32 v97, 0\n\t"
      "v_mov_b32 v99, 0\n\t"
      "v_mov_b32 v101, 0\n\t"
      "v_mov_b32 v103, 0\n\t"
      "v_mov_b32 v105, 0\n\t"
      "v_mov_b32 v128, 0\n\t"
      "v_mov_b32 v129, 0\n\t"
      "v_mov_b32 v135, 0\n\t"
      "v_mov_b32 v137, 0\n\t"
      "v_mov_b32 v159, v240\n\t"
      "v_mov_b32 v9, v241\n\t"
      "s_lshl_b32 s61, s53, 14\n\t"
      "s_add_u32 s62, s61, s47\n\t"
      "s_add_u32 s63, s61, s48\n\t"
      "s_add_u32 s61, s61, s46\n\t"
      "buffer_load_dwordx4 v[10:13], v159, s[36:39], s61 offen offset:0 sc1\n\t"
      "buffer_load_dwordx4 v[42:45], v159, s[36:39], s62 offen offset:0 sc1\n\t"
      "buffer_load_dwordx4 v[14:17], v159, s[36:39], s61 offen offset:1024 sc1\n\t"
      "buffer_load_dwordx4 v[46:49], v159, s[36:39], s62 offen offset:1024 sc1\n\t"
      "buffer_load_dwordx4 v[18:21], v159, s[36:39], s61 offen offset:2048 sc1\n\t"
      "buffer_load_dwordx4 v[50:53], v159, s[36:39], s62 offen offset:2048 sc1\n\t"
      "buffer_load_dwordx4 v[22:25], v159, s[36:39], s61 offen offset:3072 sc1\n\t"
      "buffer_load_dwordx4 v[54:57], v159, s[36:39], s62 offen offset:3072 sc1\n\t"
      "s_add_u32 s61, s61, 4096\n\t"
      "s_add_u32 s62, s62, 4096\n\t"
      "buffer_load_dwordx4 v[26:29], v159, s[36:39], s61 offen offset:0 sc1\n\t"
      "buffer_load_dwordx4 v[58:61], v159, s[36:39], s62 offen offset:0 sc1\n\t"
      "buffer_load_dwordx4 v[30:33], v159, s[36:39], s61 offen offset:1024 sc1\n\t"
      "buffer_load_dwordx4 v[62:65], v159, s[36:39], s62 offen offset:1024 sc1\n\t"
      "buffer_load_dwordx4 v[34:37], v159, s[36:39], s61 offen offset:2048 sc1\n\t"
      "buffer_load_dwordx4 v[66:69], v159, s[36:39], s62 offen offset:2048 sc1\n\t"
      "buffer_load_dwordx4 v[38:41], v159, s[36:39], s61 offen offset:3072 sc1\n\t"
      "buffer_load_dwordx4 v[70:73], v159, s[36:39], s62 offen offset:3072 sc1\n\t"
      "s_mov_b32 m0, s49\n\t"
      "s_nop 0\n\t"
      "buffer_load_dwordx4 v9, s[36:39], s63 offen offset:0 sc1 lds\n\t"
      "buffer_load_dwordx4 v9, s[36:39], s63 offen offset:1024 sc1 lds\n\t"
      "buffer_load_dwordx4 v9, s[36:39], s63 offen offset:2048 sc1 lds\n\t"
      "buffer_load_dwordx4 v9, s[36:39], s63 offen offset:3072 sc1 lds\n\t"
      "s_add_u32 m0, s49, 4096\n\t"
      "s_add_u32 s63, s63, 4096\n\t"
      "buffer_load_dwordx4 v9, s[36:39], s63 offen offset:0 sc1 lds\n\t"
      "buffer_load_dwordx4 v9, s[36:39], s63 offen offset:1024 sc1 lds\n\t"
      "buffer_load_dwordx4 v9, s[36:39], s63 offen offset:2048 sc1 lds\n\t"
      "buffer_load_dwordx4 v9, s[36:39], s63 offen offset:3072 sc1 lds\n\t"
      "s_mov_b32 s67, 64\n\t"
      "Laf_wait:\n\t"
      "s_mov_b32 s60, s53\n\t"
      "s_lshl_b32 s64, s60, 18\n\t"
      "s_add_u32 s64, s64, 0x1400000\n\t"
      "s_lshr_b32 s70, s64, 20\n\t"
      "s_and_b32 s70, s70, 62\n\t"
      "s_lshr_b64 s[72:73], s[44:45], s70\n\t"
      "s_bfe_u32 s70, s64, 0x10013\n\t"
      "s_lshl1_add_u32 s72, s70, s72\n\t"
      "s_and_b32 s72, s72, 3\n\t"
      "s_lshl_b32 s72, s72, 13\n\t"
      "s_or_b32 s64, s64, s72\n\t"
      "s_add_u32 s64, s64, s50\n\t"
      "s_xor_b32 s65, s64, 0x4000\n\t"
      "s_mov_b32 s69, 0\n\t"
      "s_cmp_eq_u32 s53, 63\n\t"
      "s_cbranch_scc1 Laf_w2\n\t"
      "Laf_w1:\n\t"
      "buffer_load_dword v140, v244, s[40:43], s64 offen sc1\n\t"
      "s_waitcnt vmcnt(0)\n\t"
      "v_cmp_eq_u32 vcc, -1, v140\n\t"
      "s_cbranch_vccz Laf_w2\n\t"
      "s_add_u32 s69, s69, 1\n\t"
      "s_and_b32 s70, s69, 31\n\t"
      "s_cmp_lg_u32 s70, 31\n\t"
      "s_cbranch_scc1 Laf_sp15\n\t"
      "s_cmp_lt_u32 s69, 0x8000\n\t"
      "s_cbranch_scc1 Laf_sp15b\n\t"
      "v_mov_b32 v140, 6\n\t"
      "buffer_store_dword v140, off, s[40:43], s51 sc1\n\t"
      "s_branch Laf_w2\n\t"
      "Laf_sp15b:\n\t"
      "buffer_load_dword v140, off, s[40:43], s51 sc1\n\t"
      "s_waitcnt vmcnt(0)\n\t"
      "v_readfirstlane_b32 s70, v140\n\t"
      "s_cmp_lg_u32 s70, 0\n\t"
      "s_cbranch_scc1 Laf_w2\n\t"
      "Laf_sp15:\n\t"
      "s_sleep 2\n\t"
      "s_branch Laf_w1\n\t"
      "Laf_w2:\n\t"
      "buffer_load_dwordx4 v[74:77], v242, s[40:43], s64 offen offset:0 sc1\n\t"
      "buffer_load_dwordx4 v[78:81], v242, s[40:43], s64 offen offset:512 sc1\n\t"
      "buffer_load_dwordx4 v[82:85], v242, s[40:43], s64 offen offset:1024 sc1\n\t"
      "buffer_load_dwordx4 v[86:89], v242, s[40:43], s64 offen offset:1536 sc1\n\t"
      "buffer_load_dword v90, v243, s[40:43], s65 offen offset:0 sc1\n\t"
      "buffer_load_dword v92, v243, s[40:43], s65 offen offset:16 sc1\n\t"
      "buffer_load_dword v94, v243, s[40:43], s65 offen offset:512 sc1\n\t"
      "buffer_load_dword v96, v243, s[40:43], s65 offen offset:528 sc1\n\t"
      "buffer_load_dword v98, v243, s[40:43], s65 offen offset:1024 sc1\n\t"
      "buffer_load_dword v100, v243, s[40:43], s65 offen offset:1040 sc1\n\t"
      "buffer_load_dword v102, v243, s[40:43], s65 offen offset:1536 sc1\n\t"
      "buffer_load_dword v104, v243, s[40:43], s65 offen offset:1552 sc1\n\t"
      "s_waitcnt vmcnt(0)\n\t"
      ".p2align 5\n\t"
      "Laf_top:\n\t"
      "s_waitcnt vmcnt(17)\n\t"
      "v_max_u32 v140, v74, v75\n\t"
      "v_max3_u32 v140, v140, v76, v77\n\t"
      "v_max3_u32 v140, v140, v78, v79\n\t"
      "v_max3_u32 v140, v140, v80, v81\n\t"
      "v_max3_u32 v140, v140, v82, v83\n\t"
      "v_max3_u32 v140, v140, v84, v85\n\t"
      "v_max3_u32 v140, v140, v86, v87\n\t"
      "v_max3_u32 v140, v140, v88, v89\n\t"
      "ds_read_b64_tr_b16 v[176:177], v245 offset:0\n\t"
      "ds_read_b64_tr_b16 v[178:179], v245 offset:4096\n\t"
      "ds_read_b64_tr_b16 v[180:181], v245 offset:8\n\t"
      "ds_read_b64_tr_b16 v[182:183], v245 offset:4104\n\t"
      "ds_read_b64_tr_b16 v[184:185], v245 offset:1024\n\t"
      "ds_read_b64_tr_b16 v[186:187], v245 offset:5120\n\t"
      "v_cmp_eq_u32 vcc, -1, v140\n\t"
      "ds_read_b64_tr_b16 v[188:189], v245 offset:1032\n\t"
      "ds_read_b64_tr_b16 v[190:191], v245 offset:5128\n\t"
      "ds_read_b64_tr_b16 v[192:193], v245 offset:2048\n\t"
      "ds_read_b64_tr_b16 v[194:195], v245 offset:6144\n\t"
      "ds_read_b64_tr_b16 v[196:197], v245 offset:2056\n\t"
      "ds_read_b64_tr_b16 v[198:199], v245 offset:6152\n\t"
      "ds_read_b64_tr_b16 v[200:201], v245 offset:3072\n\t"
      "ds_read_b64_tr_b16 v[202:203], v245 offset:7168\n\t"
      "ds_read_b64_tr_b16 v[204:205], v245 offset:3080\n\t"
      "ds_read_b64_tr_b16 v[206:207], v245 offset:7176\n\t"
      "s_cbranch_vccnz Laf_again\n\t"
      "Laf_go:\n\t"
      "v_mfma_f32_16x16x32_bf16 v[106:109], v[74:77], v[10:13], 0\n\t"
      "v_mfma_f32_16x16x32_bf16 v[110:113], v[42:45], v[10:13], v[248:251]\n\t"
      "v_mfma_f32_16x16x32_bf16 v[106:109], v[78:81], v[14:17], v[106:109]\n\t"
      "v_mfma_f32_16x16x32_bf16 v[110:113], v[46:49], v[14:17], v[110:113]\n\t"
      "v_mfma_f32_16x16x32_bf16 v[106:109], v[82:85], v[18:21], v[106:109]\n\t"
      "v_mfma_f32_16x16x32_bf16 v[110:113], v[50:53], v[18:21], v[110:113]\n\t"
      "v_mfma_f32_16x16x32_bf16 v[106:109], v[86:89], v[22:25], v[106:109]\n\t"
      "v_mfma_f32_16x16x32_bf16 v[110:113], v[54:57], v[22:25], v[110:113]\n\t"
      "v_mfma_f32_16x16x32_bf16 v[114:117], v[74:77], v[26:29], 0\n\t"
      "v_mfma_f32_16x16x32_bf16 v[118:121], v[42:45], v[26:29], 0\n\t"
      "v_mfma_f32_16x16x32_bf16 v[122:125], v[58:61], v[26:29], v[248:251]\n\t"
      "v_max_f32 v138, v106, v107\n\t"
      "v_max3_f32 v138, v138, v110, v111\n\t"
      "v_max3_f32 v138, v138, v112, v113\n\t"
      "v_mov_b32 v140, v138\n\t"
      "s_nop 1\n\t"
      "v_mfma_f32_16x16x32_bf16 v[114:117], v[78:81], v[30:33], v[114:117]\n\t"
      "v_permlane16_swap_b32 v138, v140\n\t"
      "v_max_f32 v138, v138, v140\n\t"
      "v_mov_b32 v140, v138\n\t"
      "s_nop 1\n\t"
      "v_permlane32_swap_b32 v138, v140\n\t"
      "v_mfma_f32_16x16x32_bf16 v[118:121], v[46:49], v[30:33], v[118:121]\n\t"
      "v_max_f32 v138, v138, v140\n\t"
      "v_sub_f32 v106, v106, v138\n\t"
      "v_sub_f32 v107, v107, v138\n\t"
      "v_sub_f32 v110, v110, v138\n\t"
      "v_sub_f32 v111, v111, v138\n\t"
      "v_sub_f32 v112, v112, v138\n\t"
      "v_mfma_f32_16x16x32_bf16 v[122:125], v[62:65], v[30:33], v[122:125]\n\t"
      "v_sub_f32 v113, v113, v138\n\t"
      "v_mul_f32 v106, s52, v106\n\t"
      "v_mul_f32 v107, s52, v107\n\t"
      "v_mul_f32 v110, s52, v110\n\t"
      "v_mul_f32 v111, s52, v111\n\t"
      "v_mfma_f32_16x16x32_bf16 v[114:117], v[82:85], v[34:37], v[114:117]\n\t"
      "v_mul_f32 v112, s52, v112\n\t"
      "v_mul_f32 v113, s52, v113\n\t"
      "v_exp_f32 v106, v106\n\t"
      "v_exp_f32 v107, v107\n\t"
      "v_exp_f32 v110, v110\n\t"
      "v_exp_f32 v111, v111\n\t"
      "v_mfma_f32_16x16x32_bf16 v[118:121], v[50:53], v[34:37], v[118:121]\n\t"
      "v_exp_f32 v112, v112\n\t"
      "v_exp_f32 v113, v113\n\t"
      "v_add_f32 v139, v106, v107\n\t"
      "v_add_f32 v139, v139, v110\n\t"
      "v_add_f32 v139, v139, v111\n\t"
      "v_mfma_f32_16x16x32_bf16 v[122:125], v[66:69], v[34:37], v[122:125]\n\t"
      "v_add_f32 v139, v139, v112\n\t"
      "v_add_f32 v139, v139, v113\n\t"
      "v_mov_b32 v140, v139\n\t"
      "s_nop 1\n\t"
      "v_permlane16_swap_b32 v139, v140\n\t"
      "v_mfma_f32_16x16x32_bf16 v[114:117], v[86:89], v[38:41], v[114:117]\n\t"
      "v_add_f32 v139, v139, v140\n\t"
      "v_mov_b32 v140, v139\n\t"
      "s_nop 1\n\t"
      "v_permlane32_swap_b32 v139, v140\n\t"
      "v_add_f32 v139, v139, v140\n\t"
      "v_rcp_f32 v139, v139\n\t"
      "v_mfma_f32_16x16x32_bf16 v[118:121], v[54:57], v[38:41], v[118:121]\n\t"
      "s_nop 0\n\t"
      "v_mul_f32 v106, v106, v139\n\t"
      "v_mul_f32 v107, v107, v139\n\t"
      "v_mul_f32 v110, v110, v139\n\t"
      "v_mul_f32 v111, v111, v139\n\t"
      "v_mfma_f32_16x16x32_bf16 v[122:125], v[70:73], v[38:41], v[122:125]\n\t"
      "v_mul_f32 v112, v112, v139\n\t"
      "v_mul_f32 v113, v113, v139\n\t"
      "v_cvt_pk_bf16_f32 v134, v106, v107\n\t"
      "v_cvt_pk_bf16_f32 v126, v110, v111\n\t"
      "v_cvt_pk_bf16_f32 v127, v112, v113\n\t"
      "s_nop 1\n\t"
      "s_waitcnt vmcnt(8) lgkmcnt(0)\n\t"
      "v_max_u32 v158, v90, v92\n\t"
      "v_max3_u32 v158, v158, v94, v96\n\t"
      "v_max3_u32 v158, v158, v98, v100\n\t"
      "v_max3_u32 v158, v158, v102, v104\n\t"
      "v_cmp_eq_u32 vcc, -1, v158\n\t"
      "s_cbranch_vccnz Laf_agv\n\t"
      "Laf_gov:\n\t"
      "v_mfma_f32_16x16x32_bf16 v[208:211], v[176:179], v[126:129], 0\n\t"
      "v_mfma_f32_16x16x32_bf16 v[212:215], v[180:183], v[126:129], 0\n\t"
      "v_max_f32 v138, v114, v115\n\t"
      "v_max3_f32 v138, v138, v118, v119\n\t"
      "v_max3_f32 v138, v138, v120, v121\n\t"
      "v_mfma_f32_16x16x32_bf16 v[216:219], v[184:187], v[126:129], 0\n\t"
      "v_max3_f32 v138, v138, v122, v123\n\t"
      "v_max3_f32 v138, v138, v124, v125\n\t"
      "v_mov_b32 v140, v138\n\t"
      "v_mfma_f32_16x16x32_bf16 v[220:223], v[188:191], v[126:129], 0\n\t"
      "s_nop 1\n\t"
      "v_permlane16_swap_b32 v138, v140\n\t"
      "v_max_f32 v138, v138, v140\n\t"
      "v_mfma_f32_16x16x16_bf16 v[208:211], v[90:91], v[134:135], v[208:211]\n\t"
      "v_mov_b32 v140, v138\n\t"
      "s_nop 1\n\t"
      "v_permlane32_swap_b32 v138, v140\n\t"
      "v_mfma_f32_16x16x16_bf16 v[212:215], v[92:93], v[134:135], v[212:215]\n\t"
      "v_max_f32 v138, v138, v140\n\t"
      "v_sub_f32 v114, v114, v138\n\t"
      "v_sub_f32 v115, v115, v138\n\t"
      "v_sub_f32 v118, v118, v138\n\t"
      "v_mfma_f32_16x16x32_bf16 v[224:227], v[192:195], v[126:129], 0\n\t"
      "v_sub_f32 v119, v119, v138\n\t"
      "v_sub_f32 v120, v120, v138\n\t"
      "v_sub_f32 v121, v121, v138\n\t"
      "v_mfma_f32_16x16x32_bf16 v[228:231], v[196:199], v[126:129], 0\n\t"
      "v_sub_f32 v122, v122, v138\n\t"
      "v_sub_f32 v123, v123, v138\n\t"
      "v_sub_f32 v124, v124, v138\n\t"
      "v_mfma_f32_16x16x16_bf16 v[216:219], v[94:95], v[134:135], v[216:219]\n\t"
      "v_sub_f32 v125, v125, v138\n\t"
      "v_mul_f32 v114, s52, v114\n\t"
      "v_mul_f32 v115, s52, v115\n\t"
      "v_mfma_f32_16x16x16_bf16 v[220:223], v[96:97], v[134:135], v[220:223]\n\t"
      "v_mul_f32 v118, s52, v118\n\t"
      "v_mul_f32 v119, s52, v119\n\t"
      "v_mul_f32 v120, s52, v120\n\t"
      "v_mfma_f32_16x16x32_bf16 v[232:235], v[200:203], v[126:129], 0\n\t"
      "v_mul_f32 v121, s52, v121\n\t"
      "v_mul_f32 v122, s52, v122\n\t"
      "v_mul_f32 v123, s52, v123\n\t"
      "v_mul_f32 v124, s52, v124\n\t"
      "v_mfma_f32_16x16x32_bf16 v[236:239], v[204:207], v[126:129], 0\n\t"
      "v_mul_f32 v125, s52, v125\n\t"
      "v_exp_f32 v114, v114\n\t"
      "v_exp_f32 v115, v115\n\t"
      "v_mfma_f32_16x16x16_bf16 v[224:227], v[98:99], v[134:135], v[224:227]\n\t"
      "v_exp_f32 v118, v118\n\t"
      "v_exp_f32 v119, v119\n\t"
      "v_exp_f32 v120, v120\n\t"
      "v_mfma_f32_16x16x16_bf16 v[228:231], v[100:101], v[134:135], v[228:231]\n\t"
      "v_exp_f32 v121, v121\n\t"
      "v_exp_f32 v122, v122\n\t"
      "v_exp_f32 v123, v123\n\t"
      "v_mfma_f32_16x16x16_bf16 v[232:235], v[102:103], v[134:135], v[232:235]\n\t"
      "v_exp_f32 v124, v124\n\t"
      "v_exp_f32 v125, v125\n\t"
      "v_add_f32 v139, v114, v115\n\t"
      "v_mfma_f32_16x16x16_bf16 v[236:239], v[104:105], v[134:135], v[236:239]\n\t"
      "v_add_f32 v139, v139, v118\n\t"
      "v_add_f32 v139, v139, v119\n\t"
      "v_add_f32 v139, v139, v120\n\t"
      "v_add_f32 v139, v139, v121\n\t"
      "v_add_f32 v139, v139, v122\n\t"
      "v_add_f32 v139, v139, v123\n\t"
      "v_add_f32 v139, v139, v124\n\t"
      "v_add_f32 v139, v139, v125\n\t"
      "v_mov_b32 v140, v139\n\t"
      "s_nop 1\n\t"
      "v_permlane16_swap_b32 v139, v140\n\t"
      "v_add_f32 v139, v139, v140\n\t"
      "v_mov_b32 v140, v139\n\t"
      "s_nop 1\n\t"
      "v_permlane32_swap_b32 v139, v140\n\t"
      "v_add_f32 v139, v139, v140\n\t"
      "v_rcp_f32 v139, v139\n\t"
      "s_nop 0\n\t"
      "v_mul_f32 v114, v114, v139\n\t"
      "v_mul_f32 v115, v115, v139\n\t"
      "v_mul_f32 v118, v118, v139\n\t"
      "v_mul_f32 v119, v119, v139\n\t"
      "v_mul_f32 v120, v120, v139\n\t"
      "v_mul_f32 v121, v121, v139\n\t"
      "v_mul_f32 v122, v122, v139\n\t"
      "v_mul_f32 v123, v123, v139\n\t"
      "v_mul_f32 v124, v124, v139\n\t"
      "v_mul_f32 v125, v125, v139\n\t"
      "v_cvt_pk_bf16_f32 v136, v114, v115\n\t"
      "v_cvt_pk_bf16_f32 v130, v118, v119\n\t"
      "v_cvt_pk_bf16_f32 v131, v120, v121\n\t"
      "v_cvt_pk_bf16_f32 v132, v122, v123\n\t"
      "v_cvt_pk_bf16_f32 v133, v124, v125\n\t"
      "s_nop 1\n\t"
      "v_cvt_pk_bf16_f32 v142, v208, v209\n\t"
      "v_cvt_pk_bf16_f32 v143, v210, v211\n\t"
      "v_cvt_pk_bf16_f32 v144, v212, v213\n\t"
      "v_cvt_pk_bf16_f32 v145, v214, v215\n\t"
      "ds_write_b128 v246, v[142:145] offset:0\n\t"
      "v_mfma_f32_16x16x32_bf16 v[208:211], v[176:179], v[130:133], 0\n\t"
      "v_cvt_pk_bf16_f32 v146, v216, v217\n\t"
      "v_cvt_pk_bf16_f32 v147, v218, v219\n\t"
      "v_mfma_f32_16x16x32_bf16 v[212:215], v[180:183], v[130:133], 0\n\t"
      "v_cvt_pk_bf16_f32 v148, v220, v221\n\t"
      "v_cvt_pk_bf16_f32 v149, v222, v223\n\t"
      "ds_write_b128 v246, v[146:149] offset:2048\n\t"
      "v_mfma_f32_16x16x32_bf16 v[216:219], v[184:187], v[130:133], 0\n\t"
      "v_cvt_pk_bf16_f32 v150, v224, v225\n\t"
      "v_mfma_f32_16x16x32_bf16 v[220:223], v[188:191], v[130:133], 0\n\t"
      "v_cvt_pk_bf16_f32 v151, v226, v227\n\t"
      "v_mfma_f32_16x16x16_bf16 v[208:211], v[90:91], v[136:137], v[208:211]\n\t"
      "v_cvt_pk_bf16_f32 v152, v228, v229\n\t"
      "v_mfma_f32_16x16x16_bf16 v[212:215], v[92:93], v[136:137], v[212:215]\n\t"
      "v_cvt_pk_bf16_f32 v153, v230, v231\n\t"
      "ds_write_b128 v246, v[150:153] offset:4096\n\t"
      "v_mfma_f32_16x16x32_bf16 v[224:227], v[192:195], v[130:133], 0\n\t"
      "v_cvt_pk_bf16_f32 v154, v232, v233\n\t"
      "v_mfma_f32_16x16x32_bf16 v[228:231], v[196:199], v[130:133], 0\n\t"
      "v_cvt_pk_bf16_f32 v155, v234, v235\n\t"
      "v_mfma_f32_16x16x16_bf16 v[216:219], v[94:95], v[136:137], v[216:219]\n\t"
      "v_cvt_pk_bf16_f32 v156, v236, v237\n\t"
      "v_mfma_f32_16x16x16_bf16 v[220:223], v[96:97], v[136:137], v[220:223]\n\t"
      "v_cvt_pk_bf16_f32 v157, v238, v239\n\t"
      "ds_write_b128 v246, v[154:157] offset:6144\n\t"
      "v_mfma_f32_16x16x32_bf16 v[232:235], v[200:203], v[130:133], 0\n\t"
      "v_mfma_f32_16x16x32_bf16 v[236:239], v[204:207], v[130:133], 0\n\t"
      "v_mfma_f32_16x16x16_bf16 v[224:227], v[98:99], v[136:137], v[224:227]\n\t"
      "v_mfma_f32_16x16x16_bf16 v[228:231], v[100:101], v[136:137], v[228:231]\n\t"
      "v_mfma_f32_16x16x16_bf16 v[232:235], v[102:103], v[136:137], v[232:235]\n\t"
      "v_mfma_f32_16x16x16_bf16 v[236:239], v[104:105], v[136:137], v[236:239]\n\t"
      "s_nop 4\n\t"
      "v_cvt_pk_bf16_f32 v142, v208, v209\n\t"
      "v_cvt_pk_bf16_f32 v143, v210, v211\n\t"
      "v_cvt_pk_bf16_f32 v144, v212, v213\n\t"
      "v_cvt_pk_bf16_f32 v145, v214, v215\n\t"
      "ds_write_b128 v246, v[142:145] offset:1024\n\t"
      "v_cvt_pk_bf16_f32 v146, v216, v217\n\t"
      "v_cvt_pk_bf16_f32 v147, v218, v219\n\t"
      "v_cvt_pk_bf16_f32 v148, v220, v221\n\t"
      "v_cvt_pk_bf16_f32 v149, v222, v223\n\t"
      "ds_write_b128 v246, v[146:149] offset:3072\n\t"
      "v_cvt_pk_bf16_f32 v150, v224, v225\n\t"
      "v_cvt_pk_bf16_f32 v151, v226, v227\n\t"
      "v_cvt_pk_bf16_f32 v152, v228, v229\n\t"
      "v_cvt_pk_bf16_f32 v153, v230, v231\n\t"
      "ds_write_b128 v246, v[150:153] offset:5120\n\t"
      "v_cvt_pk_bf16_f32 v154, v232, v233\n\t"
      "v_cvt_pk_bf16_f32 v155, v234, v235\n\t"
      "v_cvt_pk_bf16_f32 v156, v236, v237\n\t"
      "v_cvt_pk_bf16_f32 v157, v238, v239\n\t"
      "ds_write_b128 v246, v[154:157] offset:7168\n\t"
      "s_branch Laf_end\n\t"
      "Laf_again:\n\t"
      "s_waitcnt lgkmcnt(0)\n\t"
      "s_add_u32 s69, s69, 1\n\t"
      "s_and_b32 s70, s69, 31\n\t"
      "s_cmp_lg_u32 s70, 31\n\t"
      "s_cbranch_scc1 Laf_sp16\n\t"
      "s_cmp_lt_u32 s69, 0x8000\n\t"
      "s_cbranch_scc1 Laf_sp16b\n\t"
      "v_mov_b32 v140, 6\n\t"
      "buffer_store_dword v140, off, s[40:43], s51 sc1\n\t"
      "s_branch Laf_go\n\t"
      "Laf_sp16b:\n\t"
      "buffer_load_dword v140, off, s[40:43], s51 sc1\n\t"
      "s_waitcnt vmcnt(0)\n\t"
      "v_readfirstlane_b32 s70, v140\n\t"
      "s_cmp_lg_u32 s70, 0\n\t"
      "s_cbranch_scc1 Laf_go\n\t"
      "Laf_sp16:\n\t"
      "s_branch Laf_w2\n\t"
      "Laf_agv:\n\t"
      "s_add_u32 s69, s69, 1\n\t"
      "s_and_b32 s70, s69, 31\n\t"
      "s_cmp_lg_u32 s70, 31\n\t"
      "s_cbranch_scc1 Laf_sp17\n\t"
      "s_cmp_lt_u32 s69, 0x8000\n\t"
      "s_cbranch_scc1 Laf_sp17b\n\t"
      "v_mov_b32 v158, 6\n\t"
      "buffer_store_dword v158, off, s[40:43], s51 sc1\n\t"
      "s_branch Laf_gov\n\t"
      "Laf_sp17b:\n\t"
      "buffer_load_dword v158, off, s[40:43], s51 sc1\n\t"
      "s_waitcnt vmcnt(0)\n\t"
      "v_readfirstlane_b32 s70, v158\n\t"
      "s_cmp_lg_u32 s70, 0\n\t"
      "s_cbranch_scc1 Laf_gov\n\t"
      "Laf_sp17:\n\t"
      "buffer_load_dword v90, v243, s[40:43], s65 offen offset:0 sc1\n\t"
      "buffer_load_dword v92, v243, s[40:43], s65 offen offset:16 sc1\n\t"
      "buffer_load_dword v94, v243, s[40:43], s65 offen offset:512 sc1\n\t"
      "buffer_load_dword v96, v243, s[40:43], s65 offen offset:528 sc1\n\t"
      "buffer_load_dword v98, v243, s[40:43], s65 offen offset:1024 sc1\n\t"
      "buffer_load_dword v100, v243, s[40:43], s65 offen offset:1040 sc1\n\t"
      "buffer_load_dword v102, v243, s[40:43], s65 offen offset:1536 sc1\n\t"
      "buffer_load_dword v104, v243, s[40:43], s65 offen offset:1552 sc1\n\t"
      "s_waitcnt vmcnt(0)\n\t"
      "v_max_u32 v158, v90, v92\n\t"
      "v_max3_u32 v158, v158, v94, v96\n\t"
      "v_max3_u32 v158, v158, v98, v100\n\t"
      "v_max3_u32 v158, v158, v102, v104\n\t"
      "v_cmp_eq_u32 vcc, -1, v158\n\t"
      "s_cbranch_vccnz Laf_agv\n\t"
      "s_branch Laf_gov\n\t"
      "Laf_end:\n\t"
      "s_waitcnt vmcnt(0) lgkmcnt(0)\n\t"
      ATT_OPS);
  else asm volatile(
      "v_mov_b32 v91, 0\n\t"
      "v_mov_b32 v93, 0\n\t"
      "v_mov_b32 v95, 0\n\t"
      "v_mov_b32 v97, 0\n\t"
      "v_mov_b32 v99, 0\n\t"
      "v_mov_b32 v101, 0\n\t"
      "v_mov_b32 v103, 0\n\t"
      "v_mov_b32 v105, 0\n\t"
      "v_mov_b32 v128, 0\n\t"
      "v_mov_b32 v129, 0\n\t"
      "v_mov_b32 v135, 0\n\t"
      "v_mov_b32 v137, 0\n\t"
      "v_mov_b32 v159, v240\n\t"
      "v_mov_b32 v9, v241\n\t"
      "s_lshl_b32 s61, s53, 14\n\t"
      "s_add_u32 s62, s61, s47\n\t"
      "s_add_u32 s63, s61, s48\n\t"
      "s_add_u32 s61, s61, s46\n\t"
      "buffer_load_dwordx4 v[10:13], v159, s[36:39], s61 offen offset:0 sc1\n\t"
      "buffer_load_dwordx4 v[42:45], v159, s[36:39], s62 offen offset:0 sc1\n\t"
      "buffer_load_dwordx4 v[14:17], v159, s[36:39], s61 offen offset:1024 sc1\n\t"
      "buffer_load_dwordx4 v[46:49], v159, s[36:39], s62 offen offset:1024 sc1\n\t"
      "buffer_load_dwordx4 v[18:21], v159, s[36:39], s61 offen offset:2048 sc1\n\t"
      "buffer_load_dwordx4 v[50:53], v159, s[36:39], s62 offen offset:2048 sc1\n\t"
      "buffer_load_dwordx4 v[22:25], v159, s[36:39], s61 offen offset:3072 sc1\n\t"
      "buffer_load_dwordx4 v[54:57], v159, s[36:39], s62 offen offset:3072 sc1\n\t"
      "s_add_u32 s61, s61, 4096\n\t"
      "s_add_u32 s62, s62, 4096\n\t"
      "buffer_load_dwordx4 v[26:29], v159, s[36:39], s61 offen offset:0 sc1\n\t"
      "buffer_load_dwordx4 v[58:61], v159, s[36:39], s62 offen offset:0 sc1\n\t"
      "buffer_load_dwordx4 v[30:33], v159, s[36:39], s61 offen offset:1024 sc1\n\t"
      "buffer_load_dwordx4 v[62:65], v159, s[36:39], s62 offen offset:1024 sc1\n\t"
      "buffer_load_dwordx4 v[34:37], v159, s[36:39], s61 offen offset:2048 sc1\n\t"
      "buffer_load_dwordx4 v[66:69], v159, s[36:39], s62 offen offset:2048 sc1\n\t"
      "buffer_load_dwordx4 v[38:41], v159, s[36:39], s61 offen offset:3072 sc1\n\t"
      "buffer_load_dwordx4 v[70:73], v159, s[36:39], s62 offen offset:3072 sc1\n\t"
      "s_mov_b32 m0, s49\n\t"
      "s_nop 0\n\t"
      "buffer_load_dwordx4 v9, s[36:39], s63 offen offset:0 sc1 lds\n\t"
      "buffer_load_dwordx4 v9, s[36:39], s63 offen offset:1024 sc1 lds\n\t"
      "buffer_load_dwordx4 v9, s[36:39], s63 offen offset:2048 sc1 lds\n\t"
      "buffer_load_dwordx4 v9, s[36:39], s63 offen offset:3072 sc1 lds\n\t"
      "s_add_u32 m0, s49, 4096\n\t"
      "s_add_u32 s63, s63, 4096\n\t"
      "buffer_load_dwordx4 v9, s[36:39], s63 offen offset:0 sc1 lds\n\t"
      "buffer_load_dwordx4 v9, s[36:39], s63 offen offset:1024 sc1 lds\n\t"
      "buffer_load_dwordx4 v9, s[36:39], s63 offen offset:2048 sc1 lds\n\t"
      "buffer_load_dwordx4 v9, s[36:39], s63 offen offset:3072 sc1 lds\n\t"
      "s_mov_b32 s67, 64\n\t"
      "Lat_wait:\n\t"
      "s_mov_b32 s60, s53\n\t"
      "s_lshl_b32 s64, s60, 18\n\t"
      "s_add_u32 s64, s64, 0x1400000\n\t"
      "s_lshr_b32 s70, s64, 20\n\t"
      "s_and_b32 s70, s70, 62\n\t"
      "s_lshr_b64 s[72:73], s[44:45], s70\n\t"
      "s_bfe_u32 s70, s64, 0x10013\n\t"
      "s_lshl1_add_u32 s72, s70, s72\n\t"
      "s_and_b32 s72, s72, 3\n\t"
      "s_lshl_b32 s72, s72, 13\n\t"
      "s_or_b32 s64, s64, s72\n\t"
      "s_add_u32 s64, s64, s50\n\t"
      "s_xor_b32 s65, s64, 0x4000\n\t"
      "s_add_u32 s70, s60, s55\n\t"
      "s_cmp_lt_u32 s70, s54\n\t"
      "s_cselect_b32 s60, s70, s60\n\t"
      "s_lshl_b32 s74, s60, 18\n\t"
      "s_add_u32 s74, s74, 0x1400000\n\t"
      "s_lshr_b32 s70, s74, 20\n\t"
      "s_and_b32 s70, s70, 62\n\t"
      "s_lshr_b64 s[72:73], s[44:45], s70\n\t"
      "s_bfe_u32 s70, s74, 0x10013\n\t"
      "s_lshl1_add_u32 s72, s70, s72\n\t"
      "s_and_b32 s72, s72, 3\n\t"
      "s_lshl_b32 s72, s72, 13\n\t"
      "s_or_b32 s74, s74, s72\n\t"
      "s_add_u32 s74, s74, s50\n\t"
      "s_mov_b32 s69, 0\n\t"
      "Lat_w1:\n\t"
      "buffer_load_dword v140, v244, s[40:43], s64 offen sc1\n\t"
      "s_waitcnt vmcnt(0)\n\t"
      "s_cmp_eq_u32 s67, 64\n\t"
      "s_cbranch_scc1 Lat_w1b\n\t"
      "s_lshl_b32 s70, s67, 4\n\t"
      "s_add_u32 s70, s70, s56\n\t"
      "buffer_store_dword v247, v246, s[40:43], s70 offen sc1\n\t"
      "s_mov_b32 s67, 64\n\t"
      "Lat_w1b:\n\t"
      "v_cmp_eq_u32 vcc, -1, v140\n\t"
      "s_cbranch_vccz Lat_w2\n\t"
      "s_add_u32 s69, s69, 1\n\t"
      "s_and_b32 s70, s69, 31\n\t"
      "s_cmp_lg_u32 s70, 31\n\t"
      "s_cbranch_scc1 Lat_sp12\n\t"
      "s_cmp_lt_u32 s69, 0x8000\n\t"
      "s_cbranch_scc1 Lat_sp12b\n\t"
      "v_mov_b32 v140, 6\n\t"
      "buffer_store_dword v140, off, s[40:43], s51 sc1\n\t"
      "s_branch Lat_w2\n\t"
      "Lat_sp12b:\n\t"
      "buffer_load_dword v140, off, s[40:43], s51 sc1\n\t"
      "s_waitcnt vmcnt(0)\n\t"
      "v_readfirstlane_b32 s70, v140\n\t"
      "s_cmp_lg_u32 s70, 0\n\t"
      "s_cbranch_scc1 Lat_w2\n\t"
      "Lat_sp12:\n\t"
      "s_sleep 6\n\t"
      "s_branch Lat_w1\n\t"
      "Lat_w2:\n\t"
      "buffer_load_dwordx4 v[74:77], v242, s[40:43], s64 offen offset:0 sc1\n\t"
      "buffer_load_dwordx4 v[78:81], v242, s[40:43], s64 offen offset:512 sc1\n\t"
      "buffer_load_dwordx4 v[82:85], v242, s[40:43], s64 offen offset:1024 sc1\n\t"
      "buffer_load_dwordx4 v[86:89], v242, s[40:43], s64 offen offset:1536 sc1\n\t"
      "buffer_load_dword v90, v243, s[40:43], s65 offen offset:0 sc1\n\t"
      "buffer_load_dword v92, v243, s[40:43], s65 offen offset:16 sc1\n\t"
      "buffer_load_dword v94, v243, s[40:43], s65 offen offset:512 sc1\n\t"
      "buffer_load_dword v96, v243, s[40:43], s65 offen offset:528 sc1\n\t"
      "buffer_load_dword v98, v243, s[40:43], s65 offen offset:1024 sc1\n\t"
      "buffer_load_dword v100, v243, s[40:43], s65 offen offset:1040 sc1\n\t"
      "buffer_load_dword v102, v243, s[40:43], s65 offen offset:1536 sc1\n\t"
      "buffer_load_dword v104, v243, s[40:43], s65 offen offset:1552 sc1\n\t"
      "buffer_load_dword v141, v244, s[40:43], s74 offen sc1\n\t"
      "s_waitcnt vmcnt(0)\n\t"
      ".p2align 5\n\t"
      "Lat_top:\n\t"
      "s_waitcnt vmcnt(17)\n\t"
      "v_max_u32 v140, v74, v75\n\t"
      "v_max3_u32 v140, v140, v76, v77\n\t"
      "v_max3_u32 v140, v140, v78, v79\n\t"
      "v_max3_u32 v140, v140, v80, v81\n\t"
      "v_max3_u32 v140, v140, v82, v83\n\t"
      "v_max3_u32 v140, v140, v84, v85\n\t"
      "v_max3_u32 v140, v140, v86, v87\n\t"
      "v_max3_u32 v140, v140, v88, v89\n\t"
      "ds_read_b64_tr_b16 v[176:177], v245 offset:0\n\t"
      "ds_read_b64_tr_b16 v[178:179], v245 offset:4096\n\t"
      "ds_read_b64_tr_b16 v[180:181], v245 offset:8\n\t"
      "ds_read_b64_tr_b16 v[182:183], v245 offset:4104\n\t"
      "ds_read_b64_tr_b16 v[184:185], v245 offset:1024\n\t"
      "ds_read_b64_tr_b16 v[186:187], v245 offset:5120\n\t"
      "v_cmp_eq_u32 vcc, -1, v140\n\t"
      "ds_read_b64_tr_b16 v[188:189], v245 offset:1032\n\t"
      "ds_read_b64_tr_b16 v[190:191], v245 offset:5128\n\t"
      "ds_read_b64_tr_b16 v[192:193], v245 offset:2048\n\t"
      "ds_read_b64_tr_b16 v[194:195], v245 offset:6144\n\t"
      "ds_read_b64_tr_b16 v[196:197], v245 offset:2056\n\t"
      "ds_read_b64_tr_b16 v[198:199], v245 offset:6152\n\t"
      "ds_read_b64_tr_b16 v[200:201], v245 offset:3072\n\t"
      "ds_read_b64_tr_b16 v[202:203], v245 offset:7168\n\t"
      "ds_read_b64_tr_b16 v[204:205], v245 offset:3080\n\t"
      "ds_read_b64_tr_b16 v[206:207], v245 offset:7176\n\t"
      "s_cbranch_vccnz Lat_again\n\t"
      "Lat_go:\n\t"
      "v_mfma_f32_16x16x32_bf16 v[106:109], v[74:77], v[10:13], 0\n\t"
      "s_add_u32 s68, s53, s55\n\t"
      "v_mfma_f32_16x16x32_bf16 v[110:113], v[42:45], v[10:13], v[248:251]\n\t"
      "s_lshl_b32 s66, s53, 14\n\t"
      "s_add_u32 s66, s66, s46\n\t"
      "v_mfma_f32_16x16x32_bf16 v[106:109], v[78:81], v[14:17], v[106:109]\n\t"
      "s_cmp_lt_u32 s68, s54\n\t"
      "s_cselect_b32 s60, s68, s53\n\t"
      "s_cselect_b32 s70, 0, 0x7ffff000\n\t"
      "v_mfma_f32_16x16x32_bf16 v[110:113], v[46:49], v[14:17], v[110:113]\n\t"
      "v_or_b32 v159, s70, v240\n\t"
      "v_or_b32 v9, s70, v241\n\t"
      "v_mfma_f32_16x16x32_bf16 v[106:109], v[82:85], v[18:21], v[106:109]\n\t"
      "s_lshl_b32 s61, s60, 14\n\t"
      "v_mfma_f32_16x16x32_bf16 v[110:113], v[50:53], v[18:21], v[110:113]\n\t"
      "s_add_u32 s62, s61, s47\n\t"
      "s_add_u32 s63, s61, s48\n\t"
      "v_mfma_f32_16x16x32_bf16 v[106:109], v[86:89], v[22:25], v[106:109]\n\t"
      "s_add_u32 s61, s61, s46\n\t"
      "v_mfma_f32_16x16x32_bf16 v[110:113], v[54:57], v[22:25], v[110:113]\n\t"
      "s_xor_b32 s49, s49, 0x2000\n\t"
      "s_add_u32 s71, s66, 4096\n\t"
      "v_mfma_f32_16x16x32_bf16 v[114:117], v[74:77], v[26:29], 0\n\t"
      "v_mfma_f32_16x16x32_bf16 v[118:121], v[42:45], v[26:29], 0\n\t"
      "v_mfma_f32_16x16x32_bf16 v[122:125], v[58:61], v[26:29], v[248:251]\n\t"
      "v_max_f32 v138, v106, v107\n\t"
      "v_max3_f32 v138, v138, v110, v111\n\t"
      "v_max3_f32 v138, v138, v112, v113\n\t"
      "v_mov_b32 v140, v138\n\t"
      "s_nop 1\n\t"
      "v_permlane16_swap_b32 v138, v140\n\t"
      "v_mfma_f32_16x16x32_bf16 v[114:117], v[78:81], v[30:33], v[114:117]\n\t"
      "s_mov_b32 m0, s49\n\t"
      "s_nop 0\n\t"
      "v_max_f32 v138, v138, v140\n\t"
      "v_mov_b32 v140, v138\n\t"
      "s_nop 1\n\t"
      "v_permlane32_swap_b32 v138, v140\n\t"
      "v_max_f32 v138, v138, v140\n\t"
      "v_mfma_f32_16x16x32_bf16 v[118:121], v[46:49], v[30:33], v[118:121]\n\t"
      "buffer_load_dwordx4 v9, s[36:39], s63 offen offset:0 sc1 lds\n\t"
      "v_sub_f32 v106, v106, v138\n\t"
      "v_sub_f32 v107, v107, v138\n\t"
      "v_sub_f32 v110, v110, v138\n\t"
      "v_sub_f32 v111, v111, v138\n\t"
      "v_sub_f32 v112, v112, v138\n\t"
      "v_sub_f32 v113, v113, v138\n\t"
      "v_mfma_f32_16x16x32_bf16 v[122:125], v[62:65], v[30:33], v[122:125]\n\t"
      "buffer_load_dwordx4 v9, s[36:39], s63 offen offset:1024 sc1 lds\n\t"
      "v_mul_f32 v106, s52, v106\n\t"
      "v_mul_f32 v107, s52, v107\n\t"
      "v_mul_f32 v110, s52, v110\n\t"
      "v_mul_f32 v111, s52, v111\n\t"
      "v_mul_f32 v112, s52, v112\n\t"
      "v_mfma_f32_16x16x32_bf16 v[114:117], v[82:85], v[34:37], v[114:117]\n\t"
      "buffer_load_dwordx4 v9, s[36:39], s63 offen offset:2048 sc1 lds\n\t"
      "v_mul_f32 v113, s52, v113\n\t"
      "v_exp_f32 v106, v106\n\t"
      "v_exp_f32 v107, v107\n\t"
      "v_exp_f32 v110, v110\n\t"
      "v_exp_f32 v111, v111\n\t"
      "buffer_load_dwordx4 v9, s[36:39], s63 offen offset:3072 sc1 lds\n\t"
      "v_mfma_f32_16x16x32_bf16 v[118:121], v[50:53], v[34:37], v[118:121]\n\t"
      "v_exp_f32 v112, v112\n\t"
      "v_exp_f32 v113, v113\n\t"
      "v_add_f32 v139, v106, v107\n\t"
      "v_add_f32 v139, v139, v110\n\t"
      "v_add_f32 v139, v139, v111\n\t"
      "v_add_f32 v139, v139, v112\n\t"
      "v_mfma_f32_16x16x32_bf16 v[122:125], v[66:69], v[34:37], v[122:125]\n\t"
      "s_add_u32 m0, s49, 4096\n\t"
      "s_add_u32 s63, s63, 4096\n\t"
      "v_add_f32 v139, v139, v113\n\t"
      "v_mov_b32 v140, v139\n\t"
      "s_nop 1\n\t"
      "v_permlane16_swap_b32 v139, v140\n\t"
      "v_add_f32 v139, v139, v140\n\t"
      "v_mfma_f32_16x16x32_bf16 v[114:117], v[86:89], v[38:41], v[114:117]\n\t"
      "buffer_load_dwordx4 v9, s[36:39], s63 offen offset:0 sc1 lds\n\t"
      "v_mov_b32 v140, v139\n\t"
      "s_nop 1\n\t"
      "v_permlane32_swap_b32 v139, v140\n\t"
      "v_add_f32 v139, v139, v140\n\t"
      "v_rcp_f32 v139, v139\n\t"
      "s_nop 0\n\t"
      "v_mfma_f32_16x16x32_bf16 v[118:121], v[54:57], v[38:41], v[118:121]\n\t"
      "buffer_load_dwordx4 v9, s[36:39], s63 offen offset:1024 sc1 lds\n\t"
      "v_mul_f32 v106, v106, v139\n\t"
      "v_mul_f32 v107, v107, v139\n\t"
      "v_mul_f32 v110, v110, v139\n\t"
      "v_mul_f32 v111, v111, v139\n\t"
      "v_mul_f32 v112, v112, v139\n\t"
      "v_mfma_f32_16x16x32_bf16 v[122:125], v[70:73], v[38:41], v[122:125]\n\t"
      "buffer_load_dwordx4 v9, s[36:39], s63 offen offset:2048 sc1 lds\n\t"
      "v_mul_f32 v113, v113, v139\n\t"
      "v_cvt_pk_bf16_f32 v134, v106, v107\n\t"
      "v_cvt_pk_bf16_f32 v126, v110, v111\n\t"
      "v_cvt_pk_bf16_f32 v127, v112, v113\n\t"
      "s_nop 1\n\t"
      "buffer_load_dwordx4 v9, s[36:39], s63 offen offset:3072 sc1 lds\n\t"
      "s_waitcnt vmcnt(8) lgkmcnt(0)\n\t"
      "v_max_u32 v158, v90, v92\n\t"
      "v_max3_u32 v158, v158, v94, v96\n\t"
      "v_max3_u32 v158, v158, v98, v100\n\t"
      "v_max3_u32 v158, v158, v102, v104\n\t"
      "v_cmp_eq_u32 vcc, -1, v158\n\t"
      "s_cbranch_vccnz Lat_agv\n\t"
      "Lat_gov:\n\t"
      "v_mfma_f32_16x16x32_bf16 v[208:211], v[176:179], v[126:129], 0\n\t"
      "v_mfma_f32_16x16x32_bf16 v[212:215], v[180:183], v[126:129], 0\n\t"
      "v_max_f32 v138, v114, v115\n\t"
      "v_max3_f32 v138, v138, v118, v119\n\t"
      "v_cmp_eq_u32 vcc, -1, v141\n\t"
      "v_mfma_f32_16x16x32_bf16 v[216:219], v[184:187], v[126:129], 0\n\t"
      "v_max3_f32 v138, v138, v120, v121\n\t"
      "v_max3_f32 v138, v138, v122, v123\n\t"
      "s_lshl_b32 s70, s67, 4\n\t"
      "v_mfma_f32_16x16x32_bf16 v[220:223], v[188:191], v[126:129], 0\n\t"
      "v_max3_f32 v138, v138, v124, v125\n\t"
      "v_mov_b32 v140, v138\n\t"
      "s_add_u32 s70, s70, s56\n\t"
      "v_mfma_f32_16x16x16_bf16 v[208:211], v[90:91], v[134:135], v[208:211]\n\t"
      "s_nop 1\n\t"
      "v_permlane16_swap_b32 v138, v140\n\t"
      "buffer_store_dword v247, v246, s[40:43], s70 offen sc1\n\t"
      "v_mfma_f32_16x16x16_bf16 v[212:215], v[92:93], v[134:135], v[212:215]\n\t"
      "v_max_f32 v138, v138, v140\n\t"
      "v_mov_b32 v140, v138\n\t"
      "s_cmp_eq_u64 vcc, 0\n\t"
      "s_cselect_b32 s64, s74, s64\n\t"
      "s_nop 1\n\t"
      "v_mfma_f32_16x16x32_bf16 v[224:227], v[192:195], v[126:129], 0\n\t"
      "v_permlane32_swap_b32 v138, v140\n\t"
      "s_xor_b32 s65, s64, 0x4000\n\t"
      "v_max_f32 v138, v138, v140\n\t"
      "v_mfma_f32_16x16x32_bf16 v[228:231], v[196:199], v[126:129], 0\n\t"
      "buffer_load_dwordx4 v[74:77], v242, s[40:43], s64 offen offset:0 sc1\n\t"
      "v_sub_f32 v114, v114, v138\n\t"
      "v_sub_f32 v115, v115, v138\n\t"
      "v_mfma_f32_16x16x16_bf16 v[216:219], v[94:95], v[134:135], v[216:219]\n\t"
      "buffer_load_dwordx4 v[78:81], v242, s[40:43], s64 offen offset:512 sc1\n\t"
      "v_sub_f32 v118, v118, v138\n\t"
      "v_sub_f32 v119, v119, v138\n\t"
      "v_mfma_f32_16x16x16_bf16 v[220:223], v[96:97], v[134:135], v[220:223]\n\t"
      "buffer_load_dwordx4 v[82:85], v242, s[40:43], s64 offen offset:1024 sc1\n\t"
      "v_sub_f32 v120, v120, v138\n\t"
      "v_sub_f32 v121, v121, v138\n\t"
      "v_mfma_f32_16x16x32_bf16 v[232:235], v[200:203], v[126:129], 0\n\t"
      "buffer_load_dwordx4 v[86:89], v242, s[40:43], s64 offen offset:1536 sc1\n\t"
      "v_sub_f32 v122, v122, v138\n\t"
      "v_sub_f32 v123, v123, v138\n\t"
      "buffer_load_dwordx4 v[10:13], v159, s[36:39], s61 offen offset:0 sc1\n\t"
      "v_mfma_f32_16x16x32_bf16 v[236:239], v[204:207], v[126:129], 0\n\t"
      "v_sub_f32 v124, v124, v138\n\t"
      "v_sub_f32 v125, v125, v138\n\t"
      "buffer_load_dwordx4 v[42:45], v159, s[36:39], s62 offen offset:0 sc1\n\t"
      "v_mfma_f32_16x16x16_bf16 v[224:227], v[98:99], v[134:135], v[224:227]\n\t"
      "v_mul_f32 v114, s52, v114\n\t"
      "v_mul_f32 v115, s52, v115\n\t"
      "buffer_load_dwordx4 v[14:17], v159, s[36:39], s61 offen offset:1024 sc1\n\t"
      "v_mfma_f32_16x16x16_bf16 v[228:231], v[100:101], v[134:135], v[228:231]\n\t"
      "v_mul_f32 v118, s52, v118\n\t"
      "buffer_load_dwordx4 v[46:49], v159, s[36:39], s62 offen offset:1024 sc1\n\t"
      "v_mul_f32 v119, s52, v119\n\t"
      "v_mfma_f32_16x16x16_bf16 v[232:235], v[102:103], v[134:135], v[232:235]\n\t"
      "v_mul_f32 v120, s52, v120\n\t"
      "buffer_load_dwordx4 v[18:21], v159, s[36:39], s61 offen offset:2048 sc1\n\t"
      "v_mul_f32 v121, s52, v121\n\t"
      "v_mfma_f32_16x16x16_bf16 v[236:239], v[104:105], v[134:135], v[236:239]\n\t"
      "v_mul_f32 v122, s52, v122\n\t"
      "buffer_load_dwordx4 v[50:53], v159, s[36:39], s62 offen offset:2048 sc1\n\t"
      "v_mul_f32 v123, s52, v123\n\t"
      "v_mul_f32 v124, s52, v124\n\t"
      "buffer_load_dwordx4 v[22:25], v159, s[36:39], s61 offen offset:3072 sc1\n\t"
      "v_mul_f32 v125, s52, v125\n\t"
      "v_exp_f32 v114, v114\n\t"
      "buffer_load_dwordx4 v[54:57], v159, s[36:39], s62 offen offset:3072 sc1\n\t"
      "v_exp_f32 v115, v115\n\t"
      "v_exp_f32 v118, v118\n\t"
      "s_add_u32 s61, s61, 4096\n\t"
      "s_add_u32 s62, s62, 4096\n\t"
      "v_exp_f32 v119, v119\n\t"
      "v_exp_f32 v120, v120\n\t"
      "buffer_load_dwordx4 v[26:29], v159, s[36:39], s61 offen offset:0 sc1\n\t"
      "v_exp_f32 v121, v121\n\t"
      "buffer_load_dwordx4 v[58:61], v159, s[36:39], s62 offen offset:0 sc1\n\t"
      "v_exp_f32 v122, v122\n\t"
      "v_exp_f32 v123, v123\n\t"
      "buffer_load_dwordx4 v[30:33], v159, s[36:39], s61 offen offset:1024 sc1\n\t"
      "v_exp_f32 v124, v124\n\t"
      "v_exp_f32 v125, v125\n\t"
      "buffer_load_dwordx4 v[62:65], v159, s[36:39], s62 offen offset:1024 sc1\n\t"
      "v_add_f32 v139, v114, v115\n\t"
      "v_add_f32 v139, v139, v118\n\t"
      "buffer_load_dwordx4 v[34:37], v159, s[36:39], s61 offen offset:2048 sc1\n\t"
      "v_add_f32 v139, v139, v119\n\t"
      "v_add_f32 v139, v139, v120\n\t"
      "buffer_load_dwordx4 v[66:69], v159, s[36:39], s62 offen offset:2048 sc1\n\t"
      "v_add_f32 v139, v139, v121\n\t"
      "v_add_f32 v139, v139, v122\n\t"
      "buffer_load_dwordx4 v[38:41], v159, s[36:39], s61 offen offset:3072 sc1\n\t"
      "v_add_f32 v139, v139, v123\n\t"
      "v_add_f32 v139, v139, v124\n\t"
      "buffer_load_dwordx4 v[70:73], v159, s[36:39], s62 offen offset:3072 sc1\n\t"
      "v_add_f32 v139, v139, v125\n\t"
      "s_add_u32 s70, s60, s55\n\t"
      "v_mov_b32 v140, v139\n\t"
      "s_nop 1\n\t"
      "s_cmp_lt_u32 s70, s54\n\t"
      "v_permlane16_swap_b32 v139, v140\n\t"
      "v_add_f32 v139, v139, v140\n\t"
      "s_cselect_b32 s60, s70, s60\n\t"
      "v_mov_b32 v140, v139\n\t"
      "s_nop 1\n\t"
      "s_lshl_b32 s74, s60, 18\n\t"
      "v_permlane32_swap_b32 v139, v140\n\t"
      "v_add_f32 v139, v139, v140\n\t"
      "s_add_u32 s74, s74, 0x1400000\n\t"
      "v_rcp_f32 v139, v139\n\t"
      "s_nop 0\n\t"
      "s_lshr_b32 s70, s74, 20\n\t"
      "v_mul_f32 v114, v114, v139\n\t"
      "v_mul_f32 v115, v115, v139\n\t"
      "s_and_b32 s70, s70, 62\n\t"
      "v_mul_f32 v118, v118, v139\n\t"
      "s_lshr_b64 s[72:73], s[44:45], s70\n\t"
      "v_mul_f32 v119, v119, v139\n\t"
      "v_mul_f32 v120, v120, v139\n\t"
      "s_bfe_u32 s70, s74, 0x10013\n\t"
      "v_mul_f32 v121, v121, v139\n\t"
      "v_mul_f32 v122, v122, v139\n\t"
      "s_lshl1_add_u32 s72, s70, s72\n\t"
      "v_mul_f32 v123, v123, v139\n\t"
      "v_mul_f32 v124, v124, v139\n\t"
      "s_and_b32 s72, s72, 3\n\t"
      "v_mul_f32 v125, v125, v139\n\t"
      "v_cvt_pk_bf16_f32 v136, v114, v115\n\t"
      "s_lshl_b32 s72, s72, 13\n\t"
      "v_cvt_pk_bf16_f32 v130, v118, v119\n\t"
      "v_cvt_pk_bf16_f32 v131, v120, v121\n\t"
      "s_or_b32 s74, s74, s72\n\t"
      "v_cvt_pk_bf16_f32 v132, v122, v123\n\t"
      "v_cvt_pk_bf16_f32 v133, v124, v125\n\t"
      "s_add_u32 s74, s74, s50\n\t"
      "s_nop 1\n\t"
      "v_xor_b32 v245, 0x2000, v245\n\t"
      "v_cvt_pk_bf16_f32 v142, v208, v209\n\t"
      "v_cvt_pk_bf16_f32 v143, v210, v211\n\t"
      "v_cvt_pk_bf16_f32 v144, v212, v213\n\t"
      "v_cvt_pk_bf16_f32 v145, v214, v215\n\t"
      "buffer_store_dwordx4 v[142:145], v240, s[36:39], s66 offen offset:0 sc1\n\t"
      "v_mfma_f32_16x16x32_bf16 v[208:211], v[176:179], v[130:133], 0\n\t"
      "v_cvt_pk_bf16_f32 v146, v216, v217\n\t"
      "v_cvt_pk_bf16_f32 v147, v218, v219\n\t"
      "v_mfma_f32_16x16x32_bf16 v[212:215], v[180:183], v[130:133], 0\n\t"
      "v_cvt_pk_bf16_f32 v148, v220, v221\n\t"
      "v_cvt_pk_bf16_f32 v149, v222, v223\n\t"
      "buffer_store_dwordx4 v[146:149], v240, s[36:39], s66 offen offset:2048 sc1\n\t"
      "v_mfma_f32_16x16x32_bf16 v[216:219], v[184:187], v[130:133], 0\n\t"
      "v_cvt_pk_bf16_f32 v150, v224, v225\n\t"
      "v_mfma_f32_16x16x32_bf16 v[220:223], v[188:191], v[130:133], 0\n\t"
      "v_cvt_pk_bf16_f32 v151, v226, v227\n\t"
      "v_mfma_f32_16x16x16_bf16 v[208:211], v[90:91], v[136:137], v[208:211]\n\t"
      "v_cvt_pk_bf16_f32 v152, v228, v229\n\t"
      "v_mfma_f32_16x16x16_bf16 v[212:215], v[92:93], v[136:137], v[212:215]\n\t"
      "v_cvt_pk_bf16_f32 v153, v230, v231\n\t"
      "buffer_store_dwordx4 v[150:153], v240, s[36:39], s71 offen offset:0 sc1\n\t"
      "v_mfma_f32_16x16x32_bf16 v[224:227], v[192:195], v[130:133], 0\n\t"
      "v_cvt_pk_bf16_f32 v154, v232, v233\n\t"
      "v_mfma_f32_16x16x32_bf16 v[228:231], v[196:199], v[130:133], 0\n\t"
      "v_cvt_pk_bf16_f32 v155, v234, v235\n\t"
      "v_mfma_f32_16x16x16_bf16 v[216:219], v[94:95], v[136:137], v[216:219]\n\t"
      "v_cvt_pk_bf16_f32 v156, v236, v237\n\t"
      "v_mfma_f32_16x16x16_bf16 v[220:223], v[96:97], v[136:137], v[220:223]\n\t"
      "v_cvt_pk_bf16_f32 v157, v238, v239\n\t"
      "buffer_store_dwordx4 v[154:157], v240, s[36:39], s71 offen offset:2048 sc1\n\t"
      "v_mfma_f32_16x16x32_bf16 v[232:235], v[200:203], v[130:133], 0\n\t"
      "v_mfma_f32_16x16x32_bf16 v[236:239], v[204:207], v[130:133], 0\n\t"
      "v_mfma_f32_16x16x16_bf16 v[224:227], v[98:99], v[136:137], v[224:227]\n\t"
      "v_mfma_f32_16x16x16_bf16 v[228:231], v[100:101], v[136:137], v[228:231]\n\t"
      "v_mfma_f32_16x16x16_bf16 v[232:235], v[102:103], v[136:137], v[232:235]\n\t"
      "v_mfma_f32_16x16x16_bf16 v[236:239], v[104:105], v[136:137], v[236:239]\n\t"
      "buffer_load_dword v90, v243, s[40:43], s65 offen offset:0 sc1\n\t"
      "buffer_load_dword v92, v243, s[40:43], s65 offen offset:16 sc1\n\t"
      "buffer_load_dword v94, v243, s[40:43], s65 offen offset:512 sc1\n\t"
      "buffer_load_dword v96, v243, s[40:43], s65 offen offset:528 sc1\n\t"
      "buffer_load_dword v98, v243, s[40:43], s65 offen offset:1024 sc1\n\t"
      "buffer_load_dword v100, v243, s[40:43], s65 offen offset:1040 sc1\n\t"
      "buffer_load_dword v102, v243, s[40:43], s65 offen offset:1536 sc1\n\t"
      "buffer_load_dword v104, v243, s[40:43], s65 offen offset:1552 sc1\n\t"
      "buffer_load_dword v141, v244, s[40:43], s74 offen sc1\n\t"
      "v_cvt_pk_bf16_f32 v142, v208, v209\n\t"
      "v_cvt_pk_bf16_f32 v143, v210, v211\n\t"
      "v_cvt_pk_bf16_f32 v144, v212, v213\n\t"
      "v_cvt_pk_bf16_f32 v145, v214, v215\n\t"
      "buffer_store_dwordx4 v[142:145], v240, s[36:39], s66 offen offset:1024 sc1\n\t"
      "v_cvt_pk_bf16_f32 v146, v216, v217\n\t"
      "v_cvt_pk_bf16_f32 v147, v218, v219\n\t"
      "v_cvt_pk_bf16_f32 v148, v220, v221\n\t"
      "v_cvt_pk_bf16_f32 v149, v222, v223\n\t"
      "buffer_store_dwordx4 v[146:149], v240, s[36:39], s66 offen offset:3072 sc1\n\t"
      "v_cvt_pk_bf16_f32 v150, v224, v225\n\t"
      "v_cvt_pk_bf16_f32 v151, v226, v227\n\t"
      "v_cvt_pk_bf16_f32 v152, v228, v229\n\t"
      "v_cvt_pk_bf16_f32 v153, v230, v231\n\t"
      "buffer_store_dwordx4 v[150:153], v240, s[36:39], s71 offen offset:1024 sc1\n\t"
      "v_cvt_pk_bf16_f32 v154, v232, v233\n\t"
      "v_cvt_pk_bf16_f32 v155, v234, v235\n\t"
      "v_cvt_pk_bf16_f32 v156, v236, v237\n\t"
      "v_cvt_pk_bf16_f32 v157, v238, v239\n\t"
      "buffer_store_dwordx4 v[154:157], v240, s[36:39], s71 offen offset:3072 sc1\n\t"
      "s_mov_b32 s67, s53\n\t"
      "s_mov_b32 s53, s68\n\t"
      "s_cmp_ge_u32 s53, s54\n\t"
      "s_cbranch_scc1 Lat_fin\n\t"
      "s_cbranch_vccz Lat_top\n\t"
      "s_branch Lat_wait\n\t"
      "Lat_fin:\n\t"
      "s_waitcnt vmcnt(0)\n\t"
      "s_lshl_b32 s70, s67, 4\n\t"
      "s_add_u32 s70, s70, s56\n\t"
      "buffer_store_dword v247, v246, s[40:43], s70 offen sc1\n\t"
      "s_branch Lat_end\n\t"
      "Lat_again:\n\t"
      "s_waitcnt lgkmcnt(0)\n\t"
      "s_add_u32 s69, s69, 1\n\t"
      "s_and_b32 s70, s69, 31\n\t"
      "s_cmp_lg_u32 s70, 31\n\t"
      "s_cbranch_scc1 Lat_sp13\n\t"
      "s_cmp_lt_u32 s69, 0x8000\n\t"
      "s_cbranch_scc1 Lat_sp13b\n\t"
      "v_mov_b32 v140, 6\n\t"
      "buffer_store_dword v140, off, s[40:43], s51 sc1\n\t"
      "s_branch Lat_go\n\t"
      "Lat_sp13b:\n\t"
      "buffer_load_dword v140, off, s[40:43], s51 sc1\n\t"
      "s_waitcnt vmcnt(0)\n\t"
      "v_readfirstlane_b32 s70, v140\n\t"
      "s_cmp_lg_u32 s70, 0\n\t"
      "s_cbranch_scc1 Lat_go\n\t"
      "Lat_sp13:\n\t"
      "s_branch Lat_w2\n\t"
      "Lat_agv:\n\t"
      "s_add_u32 s69, s69, 1\n\t"
      "s_and_b32 s70, s69, 31\n\t"
      "s_cmp_lg_u32 s70, 31\n\t"
      "s_cbranch_scc1 Lat_sp14\n\t"
      "s_cmp_lt_u32 s69, 0x8000\n\t"
      "s_cbranch_scc1 Lat_sp14b\n\t"
      "v_mov_b32 v158, 6\n\t"
      "buffer_store_dword v158, off, s[40:43], s51 sc1\n\t"
      "s_branch Lat_gov\n\t"
      "Lat_sp14b:\n\t"
      "buffer_load_dword v158, off, s[40:43], s51 sc1\n\t"
      "s_waitcnt vmcnt(0)\n\t"
      "v_readfirstlane_b32 s70, v158\n\t"
      "s_cmp_lg_u32 s70, 0\n\t"
      "s_cbranch_scc1 Lat_gov\n\t"
      "Lat_sp14:\n\t"
      "buffer_load_dword v90, v243, s[40:43], s65 offen offset:0 sc1\n\t"
      "buffer_load_dword v92, v243, s[40:43], s65 offen offset:16 sc1\n\t"
      "buffer_load_dword v94, v243, s[40:43], s65 offen offset:512 sc1\n\t"
      "buffer_load_dword v96, v243, s[40:43], s65 offen offset:528 sc1\n\t"
      "buffer_load_dword v98, v243, s[40:43], s65 offen offset:1024 sc1\n\t"
      "buffer_load_dword v100, v243, s[40:43], s65 offen offset:1040 sc1\n\t"
      "buffer_load_dword v102, v243, s[40:43], s65 offen offset:1536 sc1\n\t"
      "buffer_load_dword v104, v243, s[40:43], s65 offen offset:1552 sc1\n\t"
      "s_waitcnt vmcnt(0)\n\t"
      "v_max_u32 v158, v90, v92\n\t"
      "v_max3_u32 v158, v158, v94, v96\n\t"
      "v_max3_u32 v158, v158, v98, v100\n\t"
      "v_max3_u32 v158, v158, v102, v104\n\t"
      "v_cmp_eq_u32 vcc, -1, v158\n\t"
      "s_cbranch_vccnz Lat_agv\n\t"
      "s_branch Lat_gov\n\t"
      "Lat_end:\n\t"
      "s_waitcnt vmcnt(1) lgkmcnt(0)\n\t"
      ATT_OPS);
}

constexpr int LASTP = 62;
__device__ __forceinline__ void proj_task(const FParams& F, const Plc pm, int own  , int hg, int nb, char* lds, int wv) {
  const int tid = opaque_tid(), lane = tid & 63, w = wv;
  const int l15 = lane & 15, lq = lane >> 4, h = 4 * hg + w;
  Ctl* ctl = F.ctl;
  v4u wf[4][16];
#pragma unroll
  for (int ps = 0; ps < 2; ++ps) {
    char* stg = lds + w * 32768;
#pragma unroll 4
    for (int q = 0; q < 32; ++q)
      __builtin_amdgcn_global_load_lds((const unsigned*)(F.Wo + (size_t)(hg * 512 + ps * 256 + 8 * q + (lane >> 3)) * DM + nb * 256 + 64 * w + (lane & 7) * 8), (unsigned*)(stg + q * 1024), 16, 0, 0);
    asm volatile("s_waitcnt vmcnt(0)" ::: "memory");
    const u16* p = (const u16*)(stg + (8 * lq + (l15 >> 2)) * 128 + (l15 & 3) * 16);
#pragma unroll
    for (int i = 0; i < 4; ++i)
#pragma unroll
      for (int u = 0; u < 8; ++u) { v2u lo = trrd(p + (32 * u) * 64 + 32 * (i >> 1) + 4 * (i & 1)), hi = trrd(p + (32 * u + 4) * 64 + 32 * (i >> 1) + 4 * (i & 1)); wf[i][8 * ps + u] = (v4u){lo.x, lo.y, hi.x, hi.y}; asm volatile("" : "+a"(wf[i][8 * ps + u])); }
    asm volatile("s_waitcnt lgkmcnt(0)" ::: "memory");
  }
  lds_barrier();
  const __amdgpu_buffer_rsrc_t rp = __builtin_amdgcn_make_buffer_rsrc(wsp(F, QA0), 0, PT0 - QA0 + (2 << 20), 0x00020000);
  const __amdgpu_buffer_rsrc_t ro = __builtin_amdgcn_make_buffer_rsrc(wsp(F, faro(pm, QA0 + (3 * h << 20))), 0, (1 << 20) - 8192, 0x00020000);
  const __amdgpu_buffer_rsrc_t rc = __builtin_amdgcn_make_buffer_rsrc(ctl, 0, sizeof(Ctl), 0x00020000);
  const __amdgpu_buffer_rsrc_t rout = __builtin_amdgcn_make_buffer_rsrc(uni(F.out + (size_t)(16 * (hg & 1)) * 2 * DM + nb * 256 + 64 * (hg >> 1) + (FROW ? 0 : 16 * w)), 0, (S_ - 17) * 4096 + (FROW ? 128 : 32), 0x00020000);
  constexpr int segl = NSEG - 1, SL0 = 60;
  auto pofs = [&](int hd) { return faro(pm, QA0 + ((3 * hd + 1 + (nb >> 1)) << 20)) - QA0 + (nb & 1) * 4096; };
  const int pl = PT0 - QA0 - (SL0 << 19) + nb * 131072;
  v32u WA[8]; v16u C; v8u PO; v4u Q;
#pragma unroll
  for (int i = 0; i < 256; ++i) WA[i >> 5][i & 31] = wf[i >> 6][(i >> 2) & 15][i & 3];
  auto consts = [&](int par) {
    const int lane = opaque_lane(), l15 = lane & 15, lq = lane >> 4;
    C[0] = 0; C[1] = lane * 16; C[2] = pofs(h) + lane * 16;
    C[3] = ((w & 2) + (hg & 1)) * 1024 + (w & 1) * 512 + lane * 8; C[4] = ((lane >> 1) & 15) * 4096 + (lane >> 5) * 16 + (lane & 1) * 8;

    C[5] = lane + w == 0 ? (int)offsetof(Ctl, pseg) + (hg * 4 + nb) * 64 : 0x7ffff000; C[6] = lane == 0 ? 0 : 0x7ffff000; C[10] = 65536 + 8 + (par >> 10); C[7] = C[10] + w * 4; C[8] = (int)offsetof(Ctl, flagP) + hg * 1152 + lane * 16; C[9] = 1; C[11] = lane;
    const unsigned s1 = (l15 >> 2) == lq ? 0x3f80u << (16 * (l15 & 1)) : 0u; C[12] = C[14] = (l15 & 2) ? 0u : s1; C[13] = C[15] = (l15 & 2) ? s1 : 0u;
  };
#pragma unroll
  for (int k = 0; k < 8; ++k) PO[k] = __builtin_amdgcn_readfirstlane(pofs(4 * k + (hg >> 1)));
  Q = (v4u){(unsigned)offsetof(Ctl, tticket) + nb * 64, w * 8192u, (unsigned)(pl + h * 4096 - pofs(h)), own ? PLIM : 64u};
#pragma unroll
  for (int k = 0; k < 4; ++k) Q[k] = __builtin_amdgcn_readfirstlane(Q[k]);
  auto fin_load = [&](int sg, v2u (&v)[8]) {
#pragma unroll
    for (int k = 0; k < 8; ++k) v[k] = __builtin_amdgcn_raw_buffer_load_b64(rp, C[3], sg >= SL0 ? pl + (sg << 19) + (4 * k + (hg >> 1)) * 4096 : PO[k] + sg * 16384, 16);
  };
  auto fin_store = [&](int sg, const v2u (&v)[8]) {
    f32x4 f = {0.f, 0.f, 0.f, 0.f};
#pragma unroll
    for (int j = 0; j < 4; ++j) asm volatile("s_nop 1\n\tv_mfma_f32_16x16x32_bf16 %0, %1, %2, %0" : "+v"(f) : "v"((v4u){C[12], C[13], C[14], C[15]}), "v"((v4u){v[2 * j].x, v[2 * j].y, v[2 * j + 1].x, v[2 * j + 1].y}));
    asm volatile("s_nop 7" : "+v"(f));
    __builtin_amdgcn_raw_buffer_store_b64((v2u){pk2(f[0], f[1]), pk2(f[2], f[3])}, rout, C[4], sg << 17, OUTAUX);
  };
  auto ticket = [&](int sg) { if (C[11] == 0) __hip_atomic_fetch_add(&ctl->tticket[sg][nb][0], 1u, RLX_AGENT); };
  auto low = [&](unsigned long long m) { return (int)__builtin_ctzll(m | 1ull << segl); };
  constexpr int FX0 = 42, FX1 = 48, FX2 = 54, FX3 = 60;
  constexpr unsigned long long fx = (1ull << FX2) - (1ull << FX1);
  unsigned long long fm = 0, tm = 0;
  unsigned lm = 0;
  auto fins = [&]() {
    const unsigned tkv = __builtin_amdgcn_raw_buffer_load_b32(rc, ((fm >> C[11]) & 1) ? C[11] * 256 : 0x7ffff000, Q[0], 16);
    v2u u[2][8], v[6][8]; int sl[2], sg[6];
    sl[0] = SL0 + __builtin_ctz(lm | 8u); sl[1] = SL0 + 31 - __builtin_clz(lm | 1u);
    if (lm) { fin_load(sl[0], u[0]); fin_load(sl[1], u[1]); }
    SB0;
    unsigned long long c = __ballot(tkv >= 32u);
    asm volatile("" ::: "memory");
    const int nc = __builtin_popcountll(c);
#pragma unroll
    for (int j = 0; j < 6; ++j) { sg[j] = low(c); c &= c - 1; fm &= ~(1ull << sg[j]); if (j < nc) fin_load(sg[j], v[j]); }
    SB0;
    if (lm)
#pragma unroll
      for (int j = 0; j < 2; ++j) {
        unsigned m = 0;
#pragma unroll
        for (int k = 0; k < 8; ++k) m = umax3(m, u[j][k].x, u[j][k].y);
        if (__all(m != 0xffffffffu) && ((lm >> (sl[j] - SL0)) & 1)) { fin_store(sl[j], u[j]); lm &= ~(1u << (sl[j] - SL0)); }
      }
#pragma unroll
    for (int j = 0; j < 6; ++j) if (j < nc) fin_store(sg[j], v[j]);
    asm volatile("" ::: "memory");
    return nc != 0;
  };
  unsigned* sh = (unsigned*)(lds + 65536);
  if (tid < 2) sh[tid] = own && ld_agent(&ctl->pseg[hg * 4 + nb][0]) >= PLIM ? NSEG : __hip_atomic_fetch_add(&ctl->pseg[hg * 4 + nb][0], 1u, RLX_AGENT);
  lds_barrier();
  int seg = __builtin_amdgcn_readfirstlane(sh[0] < sh[1] ? sh[0] : sh[1]), sn = __builtin_amdgcn_readfirstlane(sh[0] < sh[1] ? sh[1] : sh[0]), sprev = 0, par = 0;
  if (seg >= LASTP) seg = NSEG;
  if (sn >= LASTP) sn = NSEG;
#pragma unroll 1
  while (seg < NSEG || own) {
    const int lane = opaque_lane(), lq = lane >> 4;
    consts(par);
    if (seg == NSEG) {
      asm volatile("s_waitcnt vmcnt(0)" ::: "memory");
      for (; tm; tm &= tm - 1) ticket(low(tm));
      fm |= own == segl ? fx : (1ull << FX3) - (1ull << FX0) - fx;
      wg_wait(lane < 12 ? &ctl->prog[4 * hg + (lane & 3)][(lane >> 2) * 8] : nullptr, NSEG, ctl, false, wv);
      for (unsigned spins = 0;; ++spins) {
        const unsigned z = __builtin_amdgcn_raw_buffer_load_b32(__builtin_amdgcn_make_buffer_rsrc((char*)F.obuf + sideo(pm, VB0 + (own - 1) * OREG, hside(h) ^ 1) + quad(h * 2048), 0, 2048, 0x00020000), 512 * lq + 508, 0, 16);
        const unsigned zp = __builtin_amdgcn_raw_buffer_load_b32(__builtin_amdgcn_make_buffer_rsrc((char*)F.obuf + sideo(pm, VB0 + (own - 2) * OREG, hside(h) ^ 1) + quad(h * 2048), 0, 2048, 0x00020000), 512 * lq + 508, 0, 16);
        if (__all(z != 0xffffffffu) || spin_fail(spins, &ctl->tmo[0], 4u)) break;
        if (__all(zp != 0xffffffffu) || !fins()) __builtin_amdgcn_s_sleep(PSL2);
      }
      attnA<true>(F, pm, own, own + 1, 1, h, (unsigned)(size_t)lds + 81920 + w * 16384, (unsigned)(size_t)lds + par + w * 8192);
      consts(par);
      seg = own; own = 0;
    } else {
      for (unsigned spins = 0;; ++spins) {
        const unsigned z = ld_agent(&ctl->flagP[hg][seg][w]);
        asm volatile("s_waitcnt vmcnt(0)" ::: "memory");
        for (; tm; tm &= tm - 1) ticket(low(tm));
        if (__builtin_amdgcn_readfirstlane(z) != 0u || spin_fail(spins, &ctl->tmo[0], 4u)) break;
        if (!((spins == 0 || seg < FBS) && fins())) __builtin_amdgcn_s_sleep(PSL2);
      }
#pragma unroll
      for (int k = 0; k < 8; ++k) __builtin_amdgcn_raw_ptr_buffer_load_lds(ro, (__attribute__((address_space(3))) void*)(lds + par + w * 8192 + k * 1024), 16, lane * 16, seg * 16384 + k * 1024, 0, 16);
    }
    v2u allm;
    v8u T = {(unsigned)seg, (unsigned)sn, 0u, (unsigned)par, (unsigned)fm, (unsigned)(fm >> 32), 0u, 0u};
#pragma unroll
    for (int k = 0; k < 8; ++k) T[k] = __builtin_amdgcn_readfirstlane(T[k]);
    asm volatile(
      "s_mov_b64 s[92:93], 0\n\t"
      "v_mov_b32 v146, 0\n\t"
      "v_mov_b32 v150, 0\n\t"
      "s_cmp_ge_u32 s64, 62\n\t"
      "s_cbranch_scc1 Lpj_e2\n\t"
      "buffer_load_dwordx4 v[146:149], v168, s[44:47], 0 offen sc1\n\t"
      "s_ff1_i32_b64 s83, s[68:69]\n\t"
      "s_and_b32 s83, s83, 63\n\t"
      "s_lshl_b32 s83, s83, 8\n\t"
      "s_add_u32 s83, s83, s60\n\t"
      "buffer_load_dword v150, off, s[44:47], s83 sc1\n\t"
      "s_waitcnt vmcnt(0)\n\t"
      "Lpj_e2:\n\t"
      "v_add_u32 v160, s67, v161\n\t"
      "s_mov_b32 s73, 0\n\t"
      "s_mov_b32 s78, 1032192\n\t"
      "v_and_b32 v146, v146, v147\n\t"
      "v_and_b32 v148, v148, v149\n\t"
      "v_and_b32 v146, v146, v148\n\t"
      "v_cmp_ne_u32 vcc, 0, v146\n\t"
      "s_min_u32 s79, s65, 63\n\t"
      "s_bitcmp1_b64 vcc, s79\n\t"
      "s_cselect_b32 s79, 256, 0\n\t"
      "s_or_b32 s79, s79, s65\n\t"
      "v_mov_b32 v152, s79\n\t"
      "ds_write_b32 v167, v152 offset:64\n\t"
      "s_waitcnt lgkmcnt(0)\n\t"
      "s_barrier\n\t"
      "ds_read_b32 v152, v170 offset:64\n\t"
      "ds_read_b128 v[90:93], v160 offset:0\n\t"
      "ds_read_b128 v[94:97], v160 offset:1024\n\t"
      "ds_read_b128 v[98:101], v160 offset:2048\n\t"
      "ds_read_b128 v[102:105], v160 offset:3072\n\t"
      "ds_read_b128 v[106:109], v160 offset:4096\n\t"
      "ds_read_b128 v[110:113], v160 offset:5120\n\t"
      "s_waitcnt lgkmcnt(6)\n\t"
      "v_readfirstlane_b32 s72, v152\n\t"
      "s_lshr_b32 s72, s72, 8\n\t"
      "s_lshl_b32 s85, s78, 3\n\t"
      "s_cmp_lg_u32 s78, 1032192\n\t"
      "s_cselect_b32 s79, 0, 0x7ffff000\n\t"
      "v_or_b32 v158, s79, v164\n\t"
      "v_readfirstlane_b32 s79, v150\n\t"
      "s_ff1_i32_b64 s75, s[68:69]\n\t"
      "s_cmp_ge_u32 s79, 32\n\t"
      "s_cselect_b32 s79, 0, 0x7ffff000\n\t"
      "s_cselect_b32 s80, s75, 63\n\t"
      "s_bitset0_b64 s[68:69], s80\n\t"
      "s_lshl_b32 s78, s80, 14\n\t"
      "s_or_b32 s79, s79, s78\n\t"
      "v_or_b32 v155, s79, v163\n\t"
      "s_cmp_lg_u32 s72, 0\n\t"
      "s_cselect_b32 s79, 0, 0x7ffff000\n\t"
      "v_or_b32 v154, s79, v161\n\t"
      "s_lshl_b32 s77, s65, 14\n\t"
      "s_cmp_lg_u32 s73, 0\n\t"
      "s_cselect_b32 s79, 0, 0x7ffff000\n\t"
      "v_or_b32 v153, s79, v162\n\t"
      "s_xor_b32 s84, s67, 0x8000\n\t"
      "s_add_u32 s84, s84, s61\n\t"
      "s_ff1_i32_b64 s83, s[68:69]\n\t"
      "s_and_b32 s83, s83, 63\n\t"
      "s_lshl_b32 s83, s83, 8\n\t"
      "s_add_u32 s83, s83, s60\n\t"
      "s_cmp_lt_u32 s65, s63\n\t"
      "s_cselect_b32 s74, 0, 0x7ffff000\n\t"
      "v_or_b32 v156, s74, v165\n\t"
      "v_mov_b32 v151, 1\n\t"
      "s_mov_b32 m0, s84\n\t"
      ".p2align 6\n\t"
      "Lpj_loop:\n\t"
      "s_waitcnt lgkmcnt(4)\n\t"
      "v_mfma_f32_16x16x32_bf16 v[10:13], a[0:3], v[90:93], 0\n\t"
      "buffer_atomic_add v151, v156, s[44:47], 0 offen sc0\n\t"
      "v_mfma_f32_16x16x32_bf16 v[14:17], a[0:3], v[94:97], 0\n\t"
      "buffer_load_dwordx4 v[146:149], v168, s[44:47], 0 offen sc1\n\t"
      "v_mfma_f32_16x16x32_bf16 v[18:21], a[64:67], v[90:93], 0\n\t"
      "buffer_load_dword v150, off, s[44:47], s83 sc1\n\t"
      "v_mfma_f32_16x16x32_bf16 v[22:25], a[64:67], v[94:97], 0\n\t"
      "buffer_load_dwordx4 v154, s[40:43], s77 offen offset:0 sc1 lds\n\t"
      "v_mfma_f32_16x16x32_bf16 v[26:29], a[128:131], v[90:93], 0\n\t"
      "v_cvt_pk_bf16_f32 v74, v42, v43\n\t"
      "v_mfma_f32_16x16x32_bf16 v[30:33], a[128:131], v[94:97], 0\n\t"
      "ds_read_b128 v[114:117], v160 offset:6144\n\t"
      "v_mfma_f32_16x16x32_bf16 v[34:37], a[192:195], v[90:93], 0\n\t"
      "ds_read_b128 v[118:121], v160 offset:7168\n\t"
      "v_mfma_f32_16x16x32_bf16 v[38:41], a[192:195], v[94:97], 0\n\t"
      "buffer_load_dwordx4 v154, s[40:43], s77 offen offset:1024 sc1 lds\n\t"
      "s_waitcnt lgkmcnt(4)\n\t"
      "v_mfma_f32_16x16x32_bf16 v[10:13], a[4:7], v[98:101], v[10:13]\n\t"
      "v_cvt_pk_bf16_f32 v75, v44, v45\n\t"
      "v_mfma_f32_16x16x32_bf16 v[14:17], a[4:7], v[102:105], v[14:17]\n\t"
      "v_cvt_pk_bf16_f32 v76, v50, v51\n\t"
      "v_mfma_f32_16x16x32_bf16 v[18:21], a[68:71], v[98:101], v[18:21]\n\t"
      "v_cvt_pk_bf16_f32 v77, v52, v53\n\t"
      "v_mfma_f32_16x16x32_bf16 v[22:25], a[68:71], v[102:105], v[22:25]\n\t"
      "buffer_load_dwordx4 v154, s[40:43], s77 offen offset:2048 sc1 lds\n\t"
      "v_mfma_f32_16x16x32_bf16 v[26:29], a[132:135], v[98:101], v[26:29]\n\t"
      "v_cvt_pk_bf16_f32 v78, v46, v47\n\t"
      "v_mfma_f32_16x16x32_bf16 v[30:33], a[132:135], v[102:105], v[30:33]\n\t"
      "ds_read_b128 v[90:93], v160 offset:8192\n\t"
      "v_mfma_f32_16x16x32_bf16 v[34:37], a[196:199], v[98:101], v[34:37]\n\t"
      "ds_read_b128 v[94:97], v160 offset:9216\n\t"
      "v_mfma_f32_16x16x32_bf16 v[38:41], a[196:199], v[102:105], v[38:41]\n\t"
      "buffer_load_dwordx4 v154, s[40:43], s77 offen offset:3072 sc1 lds\n\t"
      "s_waitcnt lgkmcnt(4)\n\t"
      "v_mfma_f32_16x16x32_bf16 v[10:13], a[8:11], v[106:109], v[10:13]\n\t"
      "s_add_u32 m0, s84, 4096\n\t"
      "v_mfma_f32_16x16x32_bf16 v[14:17], a[8:11], v[110:113], v[14:17]\n\t"
      "s_add_u32 s77, s77, 4096\n\t"
      "v_mfma_f32_16x16x32_bf16 v[18:21], a[72:75], v[106:109], v[18:21]\n\t"
      "v_cvt_pk_bf16_f32 v79, v48, v49\n\t"
      "v_mfma_f32_16x16x32_bf16 v[22:25], a[72:75], v[110:113], v[22:25]\n\t"
      "buffer_load_dwordx4 v154, s[40:43], s77 offen offset:0 sc1 lds\n\t"
      "v_mfma_f32_16x16x32_bf16 v[26:29], a[136:139], v[106:109], v[26:29]\n\t"
      "v_cvt_pk_bf16_f32 v80, v54, v55\n\t"
      "v_mfma_f32_16x16x32_bf16 v[30:33], a[136:139], v[110:113], v[30:33]\n\t"
      "ds_read_b128 v[98:101], v160 offset:10240\n\t"
      "v_mfma_f32_16x16x32_bf16 v[34:37], a[200:203], v[106:109], v[34:37]\n\t"
      "ds_read_b128 v[102:105], v160 offset:11264\n\t"
      "v_mfma_f32_16x16x32_bf16 v[38:41], a[200:203], v[110:113], v[38:41]\n\t"
      "buffer_load_dwordx4 v154, s[40:43], s77 offen offset:1024 sc1 lds\n\t"
      "s_waitcnt lgkmcnt(4)\n\t"
      "v_mfma_f32_16x16x32_bf16 v[10:13], a[12:15], v[114:117], v[10:13]\n\t"
      "v_cvt_pk_bf16_f32 v81, v56, v57\n\t"
      "v_mfma_f32_16x16x32_bf16 v[14:17], a[12:15], v[118:121], v[14:17]\n\t"
      "v_cvt_pk_bf16_f32 v82, v58, v59\n\t"
      "v_mfma_f32_16x16x32_bf16 v[18:21], a[76:79], v[114:117], v[18:21]\n\t"
      "v_cvt_pk_bf16_f32 v83, v60, v61\n\t"
      "v_mfma_f32_16x16x32_bf16 v[22:25], a[76:79], v[118:121], v[22:25]\n\t"
      "buffer_load_dwordx4 v154, s[40:43], s77 offen offset:2048 sc1 lds\n\t"
      "v_mfma_f32_16x16x32_bf16 v[26:29], a[140:143], v[114:117], v[26:29]\n\t"
      "v_cvt_pk_bf16_f32 v84, v66, v67\n\t"
      "v_mfma_f32_16x16x32_bf16 v[30:33], a[140:143], v[118:121], v[30:33]\n\t"
      "ds_read_b128 v[106:109], v160 offset:12288\n\t"
      "v_mfma_f32_16x16x32_bf16 v[34:37], a[204:207], v[114:117], v[34:37]\n\t"
      "ds_read_b128 v[110:113], v160 offset:13312\n\t"
      "v_mfma_f32_16x16x32_bf16 v[38:41], a[204:207], v[118:121], v[38:41]\n\t"
      "buffer_load_dwordx4 v154, s[40:43], s77 offen offset:3072 sc1 lds\n\t"
      "s_waitcnt lgkmcnt(4)\n\t"
      "v_mfma_f32_16x16x32_bf16 v[10:13], a[16:19], v[90:93], v[10:13]\n\t"
      "v_cvt_pk_bf16_f32 v85, v68, v69\n\t"
      "v_mfma_f32_16x16x32_bf16 v[14:17], a[16:19], v[94:97], v[14:17]\n\t"
      "s_waitcnt vmcnt(22)\n\t"
      "v_mfma_f32_16x16x32_bf16 v[138:141], v[172:175], v[122:125], 0\n\t"
      "v_mfma_f32_16x16x32_bf16 v[18:21], a[80:83], v[90:93], v[18:21]\n\t"
      "v_cvt_pk_bf16_f32 v86, v62, v63\n\t"
      "v_mfma_f32_16x16x32_bf16 v[22:25], a[80:83], v[94:97], v[22:25]\n\t"
      "s_waitcnt vmcnt(20)\n\t"
      "v_mfma_f32_16x16x32_bf16 v[138:141], v[172:175], v[126:129], v[138:141]\n\t"
      "v_mfma_f32_16x16x32_bf16 v[26:29], a[144:147], v[90:93], v[26:29]\n\t"
      "v_cvt_pk_bf16_f32 v87, v64, v65\n\t"
      "v_mfma_f32_16x16x32_bf16 v[30:33], a[144:147], v[94:97], v[30:33]\n\t"
      "ds_read_b128 v[114:117], v160 offset:14336\n\t"
      "s_waitcnt vmcnt(18)\n\t"
      "v_mfma_f32_16x16x32_bf16 v[138:141], v[172:175], v[130:133], v[138:141]\n\t"
      "v_mfma_f32_16x16x32_bf16 v[34:37], a[208:211], v[90:93], v[34:37]\n\t"
      "ds_read_b128 v[118:121], v160 offset:15360\n\t"
      "v_mfma_f32_16x16x32_bf16 v[38:41], a[208:211], v[94:97], v[38:41]\n\t"
      "s_waitcnt vmcnt(16)\n\t"
      "v_mfma_f32_16x16x32_bf16 v[138:141], v[172:175], v[134:137], v[138:141]\n\t"
      "s_waitcnt lgkmcnt(4)\n\t"
      "v_mfma_f32_16x16x32_bf16 v[10:13], a[20:23], v[98:101], v[10:13]\n\t"
      "v_cvt_pk_bf16_f32 v88, v70, v71\n\t"
      "v_mfma_f32_16x16x32_bf16 v[14:17], a[20:23], v[102:105], v[14:17]\n\t"
      "v_cvt_pk_bf16_f32 v89, v72, v73\n\t"
      "v_mfma_f32_16x16x32_bf16 v[18:21], a[84:87], v[98:101], v[18:21]\n\t"
      "v_cvt_pk_bf16_f32 v138, v138, v139\n\t"
      "v_mfma_f32_16x16x32_bf16 v[22:25], a[84:87], v[102:105], v[22:25]\n\t"
      "v_cvt_pk_bf16_f32 v139, v140, v141\n\t"
      "v_mfma_f32_16x16x32_bf16 v[26:29], a[148:151], v[98:101], v[26:29]\n\t"
      "buffer_store_dwordx2 v[138:139], v158, s[48:51], s85 offen\n\t"
      "v_mfma_f32_16x16x32_bf16 v[30:33], a[148:151], v[102:105], v[30:33]\n\t"
      "ds_read_b128 v[90:93], v160 offset:16384\n\t"
      "v_mfma_f32_16x16x32_bf16 v[34:37], a[212:215], v[98:101], v[34:37]\n\t"
      "ds_read_b128 v[94:97], v160 offset:17408\n\t"
      "v_mfma_f32_16x16x32_bf16 v[38:41], a[212:215], v[102:105], v[38:41]\n\t"
      "buffer_load_dwordx2 v[122:123], v155, s[36:39], s52 offen sc1\n\t"
      "s_waitcnt lgkmcnt(4)\n\t"
      "v_mfma_f32_16x16x32_bf16 v[10:13], a[24:27], v[106:109], v[10:13]\n\t"
      "s_lshl_b32 s76, s66, 14\n\t"
      "v_mfma_f32_16x16x32_bf16 v[14:17], a[24:27], v[110:113], v[14:17]\n\t"
      "buffer_load_dwordx2 v[124:125], v155, s[36:39], s53 offen sc1\n\t"
      "v_mfma_f32_16x16x32_bf16 v[18:21], a[88:91], v[106:109], v[18:21]\n\t"
      "s_lshl_b32 s79, s66, 19\n\t"
      "s_add_u32 s79, s79, s62\n\t"
      "v_mfma_f32_16x16x32_bf16 v[22:25], a[88:91], v[110:113], v[22:25]\n\t"
      "buffer_load_dwordx2 v[126:127], v155, s[36:39], s54 offen sc1\n\t"
      "v_mfma_f32_16x16x32_bf16 v[26:29], a[152:155], v[106:109], v[26:29]\n\t"
      "s_cmp_ge_u32 s66, 60\n\t"
      "s_cselect_b32 s76, s79, s76\n\t"
      "v_mfma_f32_16x16x32_bf16 v[30:33], a[152:155], v[110:113], v[30:33]\n\t"
      "ds_read_b128 v[98:101], v160 offset:18432\n\t"
      "v_mfma_f32_16x16x32_bf16 v[34:37], a[216:219], v[106:109], v[34:37]\n\t"
      "ds_read_b128 v[102:105], v160 offset:19456\n\t"
      "v_mfma_f32_16x16x32_bf16 v[38:41], a[216:219], v[110:113], v[38:41]\n\t"
      "buffer_load_dwordx2 v[128:129], v155, s[36:39], s55 offen sc1\n\t"
      "s_waitcnt lgkmcnt(4)\n\t"
      "v_mfma_f32_16x16x32_bf16 v[10:13], a[28:31], v[114:117], v[10:13]\n\t"
      "s_bitset1_b64 s[92:93], s64\n\t"
      "v_mfma_f32_16x16x32_bf16 v[14:17], a[28:31], v[118:121], v[14:17]\n\t"
      "buffer_load_dwordx2 v[130:131], v155, s[36:39], s56 offen sc1\n\t"
      "v_mfma_f32_16x16x32_bf16 v[18:21], a[92:95], v[114:117], v[18:21]\n\t"
      "v_mfma_f32_16x16x32_bf16 v[22:25], a[92:95], v[118:121], v[22:25]\n\t"
      "buffer_load_dwordx2 v[132:133], v155, s[36:39], s57 offen sc1\n\t"
      "v_mfma_f32_16x16x32_bf16 v[26:29], a[156:159], v[114:117], v[26:29]\n\t"
      "v_mfma_f32_16x16x32_bf16 v[30:33], a[156:159], v[118:121], v[30:33]\n\t"
      "ds_read_b128 v[106:109], v160 offset:20480\n\t"
      "v_mfma_f32_16x16x32_bf16 v[34:37], a[220:223], v[114:117], v[34:37]\n\t"
      "ds_read_b128 v[110:113], v160 offset:21504\n\t"
      "v_mfma_f32_16x16x32_bf16 v[38:41], a[220:223], v[118:121], v[38:41]\n\t"
      "buffer_load_dwordx2 v[134:135], v155, s[36:39], s58 offen sc1\n\t"
      "s_waitcnt lgkmcnt(4)\n\t"
      "v_mfma_f32_16x16x32_bf16 v[10:13], a[32:35], v[90:93], v[10:13]\n\t"
      "v_mfma_f32_16x16x32_bf16 v[14:17], a[32:35], v[94:97], v[14:17]\n\t"
      "buffer_load_dwordx2 v[136:137], v155, s[36:39], s59 offen sc1\n\t"
      "v_mfma_f32_16x16x32_bf16 v[18:21], a[96:99], v[90:93], v[18:21]\n\t"
      "s_sub_u32 s79, s70, 1\n\t"
      "v_mfma_f32_16x16x32_bf16 v[22:25], a[96:99], v[94:97], v[22:25]\n\t"
      "s_subb_u32 s80, s71, 0\n\t"
      "v_mfma_f32_16x16x32_bf16 v[26:29], a[160:163], v[90:93], v[26:29]\n\t"
      "s_ff1_i32_b64 s75, s[70:71]\n\t"
      "v_mfma_f32_16x16x32_bf16 v[30:33], a[160:163], v[94:97], v[30:33]\n\t"
      "ds_read_b128 v[114:117], v160 offset:22528\n\t"
      "v_mfma_f32_16x16x32_bf16 v[34:37], a[224:227], v[90:93], v[34:37]\n\t"
      "ds_read_b128 v[118:121], v160 offset:23552\n\t"
      "v_mfma_f32_16x16x32_bf16 v[38:41], a[224:227], v[94:97], v[38:41]\n\t"
      "s_and_b32 s79, s79, s70\n\t"
      "s_waitcnt lgkmcnt(4)\n\t"
      "v_mfma_f32_16x16x32_bf16 v[10:13], a[36:39], v[98:101], v[10:13]\n\t"
      "s_and_b32 s80, s80, s71\n\t"
      "v_mfma_f32_16x16x32_bf16 v[14:17], a[36:39], v[102:105], v[14:17]\n\t"
      "s_or_b32 s79, s79, s80\n\t"
      "v_mfma_f32_16x16x32_bf16 v[18:21], a[100:103], v[98:101], v[18:21]\n\t"
      "s_cselect_b32 s79, 0, 0x7ffff000\n\t"
      "v_mfma_f32_16x16x32_bf16 v[22:25], a[100:103], v[102:105], v[22:25]\n\t"
      "buffer_store_dwordx4 v[74:77], v153, s[36:39], s76 offen offset:0 sc1\n\t"
      "v_mfma_f32_16x16x32_bf16 v[26:29], a[164:167], v[98:101], v[26:29]\n\t"
      "s_cselect_b32 s75, s75, 63\n\t"
      "v_mfma_f32_16x16x32_bf16 v[30:33], a[164:167], v[102:105], v[30:33]\n\t"
      "ds_read_b128 v[90:93], v160 offset:24576\n\t"
      "v_mfma_f32_16x16x32_bf16 v[34:37], a[228:231], v[98:101], v[34:37]\n\t"
      "ds_read_b128 v[94:97], v160 offset:25600\n\t"
      "v_mfma_f32_16x16x32_bf16 v[38:41], a[228:231], v[102:105], v[38:41]\n\t"
      "buffer_store_dwordx4 v[78:81], v153, s[36:39], s76 offen offset:1024 sc1\n\t"
      "s_waitcnt lgkmcnt(4)\n\t"
      "v_mfma_f32_16x16x32_bf16 v[10:13], a[40:43], v[106:109], v[10:13]\n\t"
      "v_or_b32 v157, s79, v166\n\t"
      "v_mfma_f32_16x16x32_bf16 v[14:17], a[40:43], v[110:113], v[14:17]\n\t"
      "s_bitset0_b64 s[70:71], s75\n\t"
      "v_mfma_f32_16x16x32_bf16 v[18:21], a[104:107], v[106:109], v[18:21]\n\t"
      "s_lshl_b32 s75, s75, 8\n\t"
      "v_mfma_f32_16x16x32_bf16 v[22:25], a[104:107], v[110:113], v[22:25]\n\t"
      "buffer_store_dwordx4 v[82:85], v153, s[36:39], s76 offen offset:2048 sc1\n\t"
      "v_mfma_f32_16x16x32_bf16 v[26:29], a[168:171], v[106:109], v[26:29]\n\t"
      "s_add_u32 s75, s75, s60\n\t"
      "v_mfma_f32_16x16x32_bf16 v[30:33], a[168:171], v[110:113], v[30:33]\n\t"
      "ds_read_b128 v[98:101], v160 offset:26624\n\t"
      "v_mfma_f32_16x16x32_bf16 v[34:37], a[232:235], v[106:109], v[34:37]\n\t"
      "ds_read_b128 v[102:105], v160 offset:27648\n\t"
      "v_mfma_f32_16x16x32_bf16 v[38:41], a[232:235], v[110:113], v[38:41]\n\t"
      "buffer_store_dwordx4 v[86:89], v153, s[36:39], s76 offen offset:3072 sc1\n\t"
      "s_waitcnt lgkmcnt(4)\n\t"
      "v_mfma_f32_16x16x32_bf16 v[10:13], a[44:47], v[114:117], v[10:13]\n\t"
      "s_waitcnt vmcnt(22)\n\t"
      "v_and_b32 v146, v146, v147\n\t"
      "v_mfma_f32_16x16x32_bf16 v[14:17], a[44:47], v[118:121], v[14:17]\n\t"
      "v_readfirstlane_b32 s81, v151\n\t"
      "v_mfma_f32_16x16x32_bf16 v[18:21], a[108:111], v[114:117], v[18:21]\n\t"
      "s_or_b32 s81, s81, s74\n\t"
      "v_mfma_f32_16x16x32_bf16 v[22:25], a[108:111], v[118:121], v[22:25]\n\t"
      "s_cmp_ge_u32 s81, 62\n\t"
      "s_cselect_b32 s81, 64, s81\n\t"
      "v_mfma_f32_16x16x32_bf16 v[26:29], a[172:175], v[114:117], v[26:29]\n\t"
      "v_and_b32 v148, v148, v149\n\t"
      "v_mfma_f32_16x16x32_bf16 v[30:33], a[172:175], v[118:121], v[30:33]\n\t"
      "ds_read_b128 v[106:109], v160 offset:28672\n\t"
      "v_and_b32 v146, v146, v148\n\t"
      "v_mfma_f32_16x16x32_bf16 v[34:37], a[236:239], v[114:117], v[34:37]\n\t"
      "ds_read_b128 v[110:113], v160 offset:29696\n\t"
      "v_cmp_ne_u32 vcc, 0, v146\n\t"
      "v_mfma_f32_16x16x32_bf16 v[38:41], a[236:239], v[118:121], v[38:41]\n\t"
      "s_min_u32 s79, s81, 63\n\t"
      "s_waitcnt lgkmcnt(4)\n\t"
      "v_mfma_f32_16x16x32_bf16 v[10:13], a[48:51], v[90:93], v[10:13]\n\t"
      "s_bitcmp1_b64 vcc, s79\n\t"
      "v_mfma_f32_16x16x32_bf16 v[14:17], a[48:51], v[94:97], v[14:17]\n\t"
      "s_cselect_b32 s79, 256, 0\n\t"
      "v_mfma_f32_16x16x32_bf16 v[18:21], a[112:115], v[90:93], v[18:21]\n\t"
      "s_or_b32 s79, s79, s81\n\t"
      "v_mfma_f32_16x16x32_bf16 v[22:25], a[112:115], v[94:97], v[22:25]\n\t"
      "v_mov_b32 v152, s79\n\t"
      "v_mfma_f32_16x16x32_bf16 v[26:29], a[176:179], v[90:93], v[26:29]\n\t"
      "ds_write_b32 v167, v152\n\t"
      "v_mfma_f32_16x16x32_bf16 v[30:33], a[176:179], v[94:97], v[30:33]\n\t"
      "ds_read_b128 v[114:117], v160 offset:30720\n\t"
      "v_mfma_f32_16x16x32_bf16 v[34:37], a[240:243], v[90:93], v[34:37]\n\t"
      "ds_read_b128 v[118:121], v160 offset:31744\n\t"
      "v_mfma_f32_16x16x32_bf16 v[38:41], a[240:243], v[94:97], v[38:41]\n\t"
      "s_waitcnt vmcnt(13) lgkmcnt(0)\n\t"
      "s_barrier\n\t"
      "v_mfma_f32_16x16x32_bf16 v[10:13], a[52:55], v[98:101], v[10:13]\n\t"
      "v_xor_b32 v160, 0x8000, v160\n\t"
      "ds_read_b32 v152, v170\n\t"
      "v_mfma_f32_16x16x32_bf16 v[14:17], a[52:55], v[102:105], v[14:17]\n\t"
      "buffer_atomic_add v169, v157, s[44:47], s75 offen\n\t"
      "v_xor_b32 v170, 32, v170\n\t"
      "v_mfma_f32_16x16x32_bf16 v[18:21], a[116:119], v[98:101], v[18:21]\n\t"
      "v_xor_b32 v167, 32, v167\n\t"
      "s_bitset1_b64 s[68:69], s64\n\t"
      "s_bitset1_b64 s[70:71], s64\n\t"
      "v_mfma_f32_16x16x32_bf16 v[22:25], a[116:119], v[102:105], v[22:25]\n\t"
      "s_and_b32 s69, s69, 0x3ff\n\t"
      "s_and_b32 s71, s71, 0xfffffff\n\t"
      "s_mov_b32 s66, s64\n\t"
      "v_mfma_f32_16x16x32_bf16 v[26:29], a[180:183], v[98:101], v[26:29]\n\t"
      "s_mov_b32 s64, s65\n\t"
      "s_mov_b32 s73, s72\n\t"
      "v_mfma_f32_16x16x32_bf16 v[30:33], a[180:183], v[102:105], v[30:33]\n\t"
      "ds_read_b128 v[90:93], v160 offset:0\n\t"
      "s_xor_b32 s67, s67, 0x8000\n\t"
      "v_mfma_f32_16x16x32_bf16 v[34:37], a[244:247], v[98:101], v[34:37]\n\t"
      "ds_read_b128 v[94:97], v160 offset:1024\n\t"
      "s_waitcnt lgkmcnt(2)\n\t"
      "v_readfirstlane_b32 s65, v152\n\t"
      "v_mfma_f32_16x16x32_bf16 v[38:41], a[244:247], v[102:105], v[38:41]\n\t"
      "s_lshr_b32 s72, s65, 8\n\t"
      "s_and_b32 s65, s65, 255\n\t"
      "v_mfma_f32_16x16x32_bf16 v[10:13], a[56:59], v[106:109], v[10:13]\n\t"
      "s_lshl_b32 s85, s78, 3\n\t"
      "s_cmp_lg_u32 s78, 1032192\n\t"
      "s_cselect_b32 s79, 0, 0x7ffff000\n\t"
      "v_mfma_f32_16x16x32_bf16 v[14:17], a[56:59], v[110:113], v[14:17]\n\t"
      "v_or_b32 v158, s79, v164\n\t"
      "v_readfirstlane_b32 s79, v150\n\t"
      "s_ff1_i32_b64 s75, s[68:69]\n\t"
      "v_mfma_f32_16x16x32_bf16 v[18:21], a[120:123], v[106:109], v[18:21]\n\t"
      "s_cmp_ge_u32 s79, 32\n\t"
      "s_cselect_b32 s79, 0, 0x7ffff000\n\t"
      "s_cselect_b32 s80, s75, 63\n\t"
      "v_mfma_f32_16x16x32_bf16 v[22:25], a[120:123], v[110:113], v[22:25]\n\t"
      "s_bitset0_b64 s[68:69], s80\n\t"
      "s_lshl_b32 s78, s80, 14\n\t"
      "s_or_b32 s79, s79, s78\n\t"
      "v_mfma_f32_16x16x32_bf16 v[26:29], a[184:187], v[106:109], v[26:29]\n\t"
      "v_or_b32 v155, s79, v163\n\t"
      "s_cmp_lg_u32 s72, 0\n\t"
      "v_mfma_f32_16x16x32_bf16 v[30:33], a[184:187], v[110:113], v[30:33]\n\t"
      "ds_read_b128 v[98:101], v160 offset:2048\n\t"
      "s_cselect_b32 s79, 0, 0x7ffff000\n\t"
      "v_mfma_f32_16x16x32_bf16 v[34:37], a[248:251], v[106:109], v[34:37]\n\t"
      "ds_read_b128 v[102:105], v160 offset:3072\n\t"
      "v_or_b32 v154, s79, v161\n\t"
      "v_mfma_f32_16x16x32_bf16 v[38:41], a[248:251], v[110:113], v[38:41]\n\t"
      "s_lshl_b32 s77, s65, 14\n\t"
      "s_cmp_lg_u32 s73, 0\n\t"
      "v_mfma_f32_16x16x32_bf16 v[42:45], a[60:63], v[114:117], v[10:13]\n\t"
      "s_cselect_b32 s79, 0, 0x7ffff000\n\t"
      "v_or_b32 v153, s79, v162\n\t"
      "s_xor_b32 s84, s67, 0x8000\n\t"
      "v_mfma_f32_16x16x32_bf16 v[46:49], a[60:63], v[118:121], v[14:17]\n\t"
      "s_add_u32 s84, s84, s61\n\t"
      "s_ff1_i32_b64 s83, s[68:69]\n\t"
      "s_and_b32 s83, s83, 63\n\t"
      "v_mfma_f32_16x16x32_bf16 v[50:53], a[124:127], v[114:117], v[18:21]\n\t"
      "s_lshl_b32 s83, s83, 8\n\t"
      "s_add_u32 s83, s83, s60\n\t"
      "s_cmp_lt_u32 s65, s63\n\t"
      "v_mfma_f32_16x16x32_bf16 v[54:57], a[124:127], v[118:121], v[22:25]\n\t"
      "s_cselect_b32 s74, 0, 0x7ffff000\n\t"
      "v_or_b32 v156, s74, v165\n\t"
      "v_mov_b32 v151, 1\n\t"
      "v_mfma_f32_16x16x32_bf16 v[58:61], a[188:191], v[114:117], v[26:29]\n\t"
      "s_mov_b32 m0, s84\n\t"
      "v_mfma_f32_16x16x32_bf16 v[62:65], a[188:191], v[118:121], v[30:33]\n\t"
      "ds_read_b128 v[106:109], v160 offset:4096\n\t"
      "v_mfma_f32_16x16x32_bf16 v[66:69], a[252:255], v[114:117], v[34:37]\n\t"
      "ds_read_b128 v[110:113], v160 offset:5120\n\t"
      "v_mfma_f32_16x16x32_bf16 v[70:73], a[252:255], v[118:121], v[38:41]\n\t"
      "s_cmp_lg_u32 s73, 0\n\t"
      "s_cbranch_scc1 Lpj_loop\n\t"
      "s_lshl_b32 s76, s66, 14\n\t"
      "s_lshl_b32 s79, s66, 19\n\t"
      "s_add_u32 s79, s79, s62\n\t"
      "s_cmp_ge_u32 s66, 60\n\t"
      "s_cselect_b32 s76, s79, s76\n\t"
      "s_lshr_b32 s79, s78, 14\n\t"
      "s_bitset1_b64 s[68:69], s79\n\t"
      "s_bitset0_b64 s[68:69], 63\n\t"
      "v_cvt_pk_bf16_f32 v74, v42, v43\n\t"
      "v_cvt_pk_bf16_f32 v75, v44, v45\n\t"
      "v_cvt_pk_bf16_f32 v76, v50, v51\n\t"
      "v_cvt_pk_bf16_f32 v77, v52, v53\n\t"
      "buffer_store_dwordx4 v[74:77], v162, s[36:39], s76 offen offset:0 sc1\n\t"
      "v_cvt_pk_bf16_f32 v78, v46, v47\n\t"
      "v_cvt_pk_bf16_f32 v79, v48, v49\n\t"
      "v_cvt_pk_bf16_f32 v80, v54, v55\n\t"
      "v_cvt_pk_bf16_f32 v81, v56, v57\n\t"
      "buffer_store_dwordx4 v[78:81], v162, s[36:39], s76 offen offset:1024 sc1\n\t"
      "v_cvt_pk_bf16_f32 v82, v58, v59\n\t"
      "v_cvt_pk_bf16_f32 v83, v60, v61\n\t"
      "v_cvt_pk_bf16_f32 v84, v66, v67\n\t"
      "v_cvt_pk_bf16_f32 v85, v68, v69\n\t"
      "buffer_store_dwordx4 v[82:85], v162, s[36:39], s76 offen offset:2048 sc1\n\t"
      "v_cvt_pk_bf16_f32 v86, v62, v63\n\t"
      "v_cvt_pk_bf16_f32 v87, v64, v65\n\t"
      "v_cvt_pk_bf16_f32 v88, v70, v71\n\t"
      "v_cvt_pk_bf16_f32 v89, v72, v73\n\t"
      "buffer_store_dwordx4 v[86:89], v162, s[36:39], s76 offen offset:3072 sc1\n\t"
      "s_waitcnt vmcnt(9) lgkmcnt(0)\n\t"
      "v_mfma_f32_16x16x32_bf16 v[138:141], v[172:175], v[122:125], 0\n\t"
      "v_mfma_f32_16x16x32_bf16 v[138:141], v[172:175], v[126:129], v[138:141]\n\t"
      "v_mfma_f32_16x16x32_bf16 v[138:141], v[172:175], v[130:133], v[138:141]\n\t"
      "v_mfma_f32_16x16x32_bf16 v[138:141], v[172:175], v[134:137], v[138:141]\n\t"
      "s_nop 7\n\t"
      "v_cvt_pk_bf16_f32 v138, v138, v139\n\t"
      "v_cvt_pk_bf16_f32 v139, v140, v141\n\t"
      "buffer_store_dwordx2 v[138:139], v158, s[48:51], s85 offen \n\t"
      : "+{s[64:71]}"(T), "+{v[160:175]}"(C), "={s[92:93]}"(allm)
      : "{a[0:31]}"(WA[0]), "{a[32:63]}"(WA[1]), "{a[64:95]}"(WA[2]), "{a[96:127]}"(WA[3]), "{a[128:159]}"(WA[4]), "{a[160:191]}"(WA[5]), "{a[192:223]}"(WA[6]), "{a[224:255]}"(WA[7]),
        "{s[36:39]}"(rp), "{s[40:43]}"(ro), "{s[44:47]}"(rc), "{s[48:51]}"(rout), "{s[52:59]}"(PO), "{s[60:63]}"(Q)
      : R8(v, 1), R8(v, 2), R8(v, 3), R8(v, 4), R8(v, 5), R8(v, 6), R8(v, 7), R8(v, 8), R8(v, 9), R8(v, 10), R8(v, 11), R8(v, 12), R8(v, 13), R8(v, 14), R8(v, 15),
        "s72", "s73", "s74", "s75", "s76", "s77", "s78", "s79", "s80", "s81", "s82", "s83", "s84", "s85", "s86", "s87", "vcc", "scc", "m0", "memory");
#pragma unroll
    for (int k = 0; k < 8; ++k) T[k] = __builtin_amdgcn_readfirstlane(T[k]);
    seg = T[0]; sn = T[1]; sprev = T[2]; par = T[3]; if (sprev < LASTP) fm = T[4] | (unsigned long long)T[5] << 32; tm = T[6] | (unsigned long long)T[7] << 32;
    lm |= (unsigned)__builtin_amdgcn_readfirstlane(allm.y) >> (SL0 - 32);
  }
  consts(0);
  if (tm) asm volatile("s_waitcnt vmcnt(0)" ::: "memory");
  for (; tm; tm &= tm - 1) ticket(low(tm));
#pragma unroll 1
  for (unsigned spins = 0; (fm || lm) && !spin_fail(spins, &ctl->tmo[0], 5u); ++spins) {
    fins();
  }
}

__device__ __forceinline__ Plc qkv_task(const FParams& F, unsigned long long ep, int mat  , int h, char* lds, int wv) {
  const int tid = opaque_tid(), lane = tid & 63, w = wv;
  const int l15 = lane & 15, lq = lane >> 4;
  const u16* W = mat == 0 ? F.Wk : (mat == 1 ? F.Wq : F.Wv);
  v32u WA[8];
#pragma unroll
  for (int ps = 0; ps < 2; ++ps) {
    char* stg = lds + w * 32768;
#pragma unroll 4
    for (int q = 0; q < 32; ++q)
      __builtin_amdgcn_global_load_lds((const unsigned*)(W + (size_t)(ps * 512 + 16 * q + (lane >> 2)) * HDA + h * DHA + 32 * w + (lane & 3) * 8), (unsigned*)(stg + q * 1024), 16, 0, 0);
    asm volatile("s_waitcnt vmcnt(0)" ::: "memory");
    const u16* p = (const u16*)(stg + (128 * lq + (l15 >> 2)) * 64 + (l15 & 3) * 16);
#pragma unroll
    for (int i = 0; i < 2; ++i)
#pragma unroll
      for (int u = 0; u < 16; ++u) {
        const v2u lo = trrd(p + 8 * u * 32 + 4 * i), hi = trrd(p + (8 * u + 4) * 32 + 4 * i);
        v4u z = {lo.x, lo.y, hi.x, hi.y};
        asm volatile("" : "+a"(z));
#pragma unroll
        for (int e = 0; e < 4; ++e) WA[4 * i + 2 * ps + (u >> 3)][(u & 7) * 4 + e] = z[e];
      }
    asm volatile("s_waitcnt lgkmcnt(0)" ::: "memory");
  }
  const unsigned long long tok = uni64(ep) + 1;
  wait_token(F, tok);
  const Plc pm = wait_votes(F, tok, lane, false);
  lds_barrier();
  v16u C; v8u S;
  C[0] = (unsigned)(size_t)lds + l15 * 1040 + lq * 256;
#pragma unroll
  for (int k = 0; k < 8; ++k) C[1 + k] = (8 * w + k) * 2048 + lane * 16;
  C[9] = w * 1024 + lane * 16; C[10] = mat == 1 ? 0x7ffff000 : quad(h * 2048) + (w & 1) * 1024 + lane * 16; C[11] = mat == 1 || tid >= 32 ? 0x7ffff000 : quad(h * 512) + lane * 16;
  C[12] = tid == 0 ? CBASE + (int)offsetof(Ctl, prog) + h * 128 + mat * 32 : 0x7ffff000; C[13] = tid == 0 && mat != 1 ? HBASE + 256 + (mat * 16 + h) * 8 : 0x7ffff000; C[14] = C[15] = 0;
  { const unsigned long long pl2 = __ballot((sideo(pm, (lane >> 1) << 21, hside(h) ^ (mat == 0)) >> (13 + (lane & 1))) & 1);
    S = (v8u){(unsigned)pl2, (unsigned)(pl2 >> 32), (unsigned)(w < 2 ? VB0 : OB0), (unsigned)(size_t)lds + w * 8320, (unsigned)tok, (unsigned)(tok >> 32), 0u, 0u}; }
#pragma unroll
  for (int k = 0; k < 8; ++k) S[k] = __builtin_amdgcn_readfirstlane(S[k]);
  asm volatile(
      "s_mov_b32 s56, 0\n\t"
      "s_mov_b32 s58, 0\n\t"
      "s_mov_b32 s59, s51\n\t"
      "s_mov_b32 s60, 66560\n\t"
      "v_mov_b32 v116, -1\n\t"
      "v_mov_b32 v117, -1\n\t"
      "v_mov_b32 v118, -1\n\t"
      "v_mov_b32 v119, -1\n\t"
      "s_add_u32 m0, s59, 0\n\t"
      "s_nop 0\n\t"
      "buffer_load_dwordx4 v101, s[36:39], s58 offen lds\n\t"
      "s_add_u32 m0, s59, 1040\n\t"
      "s_nop 0\n\t"
      "buffer_load_dwordx4 v102, s[36:39], s58 offen lds\n\t"
      "s_add_u32 m0, s59, 2080\n\t"
      "s_nop 0\n\t"
      "buffer_load_dwordx4 v103, s[36:39], s58 offen lds\n\t"
      "s_add_u32 m0, s59, 3120\n\t"
      "s_nop 0\n\t"
      "buffer_load_dwordx4 v104, s[36:39], s58 offen lds\n\t"
      "s_add_u32 m0, s59, 4160\n\t"
      "s_nop 0\n\t"
      "buffer_load_dwordx4 v105, s[36:39], s58 offen lds\n\t"
      "s_add_u32 m0, s59, 5200\n\t"
      "s_nop 0\n\t"
      "buffer_load_dwordx4 v106, s[36:39], s58 offen lds\n\t"
      "s_add_u32 m0, s59, 6240\n\t"
      "s_nop 0\n\t"
      "buffer_load_dwordx4 v107, s[36:39], s58 offen lds\n\t"
      "s_add_u32 m0, s59, 7280\n\t"
      "s_nop 0\n\t"
      "buffer_load_dwordx4 v108, s[36:39], s58 offen lds\n\t"
      "s_add_u32 s58, s58, 1024\n\t"
      "s_add_u32 m0, s59, 33280\n\t"
      "s_nop 0\n\t"
      "buffer_load_dwordx4 v101, s[36:39], s58 offen lds\n\t"
      "s_add_u32 m0, s59, 34320\n\t"
      "s_nop 0\n\t"
      "buffer_load_dwordx4 v102, s[36:39], s58 offen lds\n\t"
      "s_add_u32 m0, s59, 35360\n\t"
      "s_nop 0\n\t"
      "buffer_load_dwordx4 v103, s[36:39], s58 offen lds\n\t"
      "s_add_u32 m0, s59, 36400\n\t"
      "s_nop 0\n\t"
      "buffer_load_dwordx4 v104, s[36:39], s58 offen lds\n\t"
      "s_add_u32 m0, s59, 37440\n\t"
      "s_nop 0\n\t"
      "buffer_load_dwordx4 v105, s[36:39], s58 offen lds\n\t"
      "s_add_u32 m0, s59, 38480\n\t"
      "s_nop 0\n\t"
      "buffer_load_dwordx4 v106, s[36:39], s58 offen lds\n\t"
      "s_add_u32 m0, s59, 39520\n\t"
      "s_nop 0\n\t"
      "buffer_load_dwordx4 v107, s[36:39], s58 offen lds\n\t"
      "s_add_u32 m0, s59, 40560\n\t"
      "s_nop 0\n\t"
      "buffer_load_dwordx4 v108, s[36:39], s58 offen lds\n\t"
      "s_add_u32 s58, s58, 64512\n\t"
      "s_add_u32 m0, s59, 66560\n\t"
      "s_nop 0\n\t"
      "buffer_load_dwordx4 v101, s[36:39], s58 offen lds\n\t"
      "s_add_u32 m0, s59, 67600\n\t"
      "s_nop 0\n\t"
      "buffer_load_dwordx4 v102, s[36:39], s58 offen lds\n\t"
      "s_add_u32 m0, s59, 68640\n\t"
      "s_nop 0\n\t"
      "buffer_load_dwordx4 v103, s[36:39], s58 offen lds\n\t"
      "s_add_u32 m0, s59, 69680\n\t"
      "s_nop 0\n\t"
      "buffer_load_dwordx4 v104, s[36:39], s58 offen lds\n\t"
      "s_add_u32 m0, s59, 70720\n\t"
      "s_nop 0\n\t"
      "buffer_load_dwordx4 v105, s[36:39], s58 offen lds\n\t"
      "s_add_u32 m0, s59, 71760\n\t"
      "s_nop 0\n\t"
      "buffer_load_dwordx4 v106, s[36:39], s58 offen lds\n\t"
      "s_add_u32 m0, s59, 72800\n\t"
      "s_nop 0\n\t"
      "buffer_load_dwordx4 v107, s[36:39], s58 offen lds\n\t"
      "s_add_u32 m0, s59, 73840\n\t"
      "s_nop 0\n\t"
      "buffer_load_dwordx4 v108, s[36:39], s58 offen lds\n\t"
      "s_add_u32 s58, s58, 1024\n\t"
      "s_add_u32 s59, s59, 99840\n\t"
      "v_mov_b32 v120, 0x7ffff000\n\t"
      "s_waitcnt vmcnt(8)\n\t"
      "s_barrier\n\t"
      "ds_read_b128 v[50:53], v100 offset:0\n\t"
      "ds_read_b128 v[54:57], v100 offset:16640\n\t"
      "ds_read_b128 v[58:61], v100 offset:16\n\t"
      "ds_read_b128 v[62:65], v100 offset:16656\n\t"
      "ds_read_b128 v[66:69], v100 offset:32\n\t"
      "ds_read_b128 v[70:73], v100 offset:16672\n\t"
      ".p2align 6\n\t"
      "Lst_loop:\n\t"
      "s_waitcnt lgkmcnt(4)\n\t"
      "v_mfma_f32_16x16x32_bf16 v[10:13], a[0:3], v[50:53], 0\n\t"
      "s_add_u32 m0, s59, 0\n\t"
      "v_mfma_f32_16x16x32_bf16 v[18:21], a[128:131], v[50:53], 0\n\t"
      "ds_read_b128 v[74:77], v100 offset:48\n\t"
      "v_mfma_f32_16x16x32_bf16 v[14:17], a[0:3], v[54:57], 0\n\t"
      "ds_read_b128 v[78:81], v100 offset:16688\n\t"
      "v_mfma_f32_16x16x32_bf16 v[22:25], a[128:131], v[54:57], 0\n\t"
      "buffer_load_dwordx4 v101, s[36:39], s58 offen lds\n\t"
      "s_waitcnt lgkmcnt(4)\n\t"
      "v_mfma_f32_16x16x32_bf16 v[10:13], a[4:7], v[58:61], v[10:13]\n\t"
      "v_mfma_f32_16x16x32_bf16 v[18:21], a[132:135], v[58:61], v[18:21]\n\t"
      "ds_read_b128 v[50:53], v100 offset:64\n\t"
      "v_cvt_pk_bf16_f32 v42, v26, v27\n\t"
      "v_mfma_f32_16x16x32_bf16 v[14:17], a[4:7], v[62:65], v[14:17]\n\t"
      "ds_read_b128 v[54:57], v100 offset:16704\n\t"
      "v_cvt_pk_bf16_f32 v43, v28, v29\n\t"
      "v_mfma_f32_16x16x32_bf16 v[22:25], a[132:135], v[62:65], v[22:25]\n\t"
      "s_add_u32 m0, s59, 1040\n\t"
      "s_waitcnt lgkmcnt(4)\n\t"
      "v_mfma_f32_16x16x32_bf16 v[10:13], a[8:11], v[66:69], v[10:13]\n\t"
      "v_mfma_f32_16x16x32_bf16 v[18:21], a[136:139], v[66:69], v[18:21]\n\t"
      "ds_read_b128 v[58:61], v100 offset:80\n\t"
      "v_cvt_pk_bf16_f32 v44, v34, v35\n\t"
      "v_mfma_f32_16x16x32_bf16 v[14:17], a[8:11], v[70:73], v[14:17]\n\t"
      "ds_read_b128 v[62:65], v100 offset:16720\n\t"
      "buffer_load_dwordx4 v102, s[36:39], s58 offen lds\n\t"
      "v_mfma_f32_16x16x32_bf16 v[22:25], a[136:139], v[70:73], v[22:25]\n\t"
      "s_waitcnt lgkmcnt(4)\n\t"
      "v_mfma_f32_16x16x32_bf16 v[10:13], a[12:15], v[74:77], v[10:13]\n\t"
      "s_add_u32 m0, s59, 2080\n\t"
      "v_mfma_f32_16x16x32_bf16 v[18:21], a[140:143], v[74:77], v[18:21]\n\t"
      "ds_read_b128 v[66:69], v100 offset:96\n\t"
      "v_cvt_pk_bf16_f32 v45, v36, v37\n\t"
      "v_mfma_f32_16x16x32_bf16 v[14:17], a[12:15], v[78:81], v[14:17]\n\t"
      "ds_read_b128 v[70:73], v100 offset:16736\n\t"
      "v_cvt_pk_bf16_f32 v46, v30, v31\n\t"
      "v_mfma_f32_16x16x32_bf16 v[22:25], a[140:143], v[78:81], v[22:25]\n\t"
      "buffer_load_dwordx4 v103, s[36:39], s58 offen lds\n\t"
      "s_waitcnt lgkmcnt(4)\n\t"
      "v_mfma_f32_16x16x32_bf16 v[10:13], a[16:19], v[50:53], v[10:13]\n\t"
      "v_mfma_f32_16x16x32_bf16 v[18:21], a[144:147], v[50:53], v[18:21]\n\t"
      "ds_read_b128 v[74:77], v100 offset:112\n\t"
      "v_cvt_pk_bf16_f32 v47, v32, v33\n\t"
      "v_mfma_f32_16x16x32_bf16 v[14:17], a[16:19], v[54:57], v[14:17]\n\t"
      "ds_read_b128 v[78:81], v100 offset:16752\n\t"
      "v_cvt_pk_bf16_f32 v48, v38, v39\n\t"
      "v_mfma_f32_16x16x32_bf16 v[22:25], a[144:147], v[54:57], v[22:25]\n\t"
      "s_add_u32 m0, s59, 3120\n\t"
      "s_waitcnt lgkmcnt(4)\n\t"
      "v_mfma_f32_16x16x32_bf16 v[10:13], a[20:23], v[58:61], v[10:13]\n\t"
      "v_mfma_f32_16x16x32_bf16 v[18:21], a[148:151], v[58:61], v[18:21]\n\t"
      "ds_read_b128 v[50:53], v100 offset:128\n\t"
      "v_cvt_pk_bf16_f32 v49, v40, v41\n\t"
      "v_mfma_f32_16x16x32_bf16 v[14:17], a[20:23], v[62:65], v[14:17]\n\t"
      "ds_read_b128 v[54:57], v100 offset:16768\n\t"
      "buffer_load_dwordx4 v104, s[36:39], s58 offen lds\n\t"
      "v_mfma_f32_16x16x32_bf16 v[22:25], a[148:151], v[62:65], v[22:25]\n\t"
      "buffer_store_dwordx4 v[42:45], v120, s[40:43], s61 offen sc1\n\t"
      "s_waitcnt lgkmcnt(4)\n\t"
      "v_mfma_f32_16x16x32_bf16 v[10:13], a[24:27], v[66:69], v[10:13]\n\t"
      "s_add_u32 m0, s59, 4160\n\t"
      "v_mfma_f32_16x16x32_bf16 v[18:21], a[152:155], v[66:69], v[18:21]\n\t"
      "ds_read_b128 v[58:61], v100 offset:144\n\t"
      "v_mfma_f32_16x16x32_bf16 v[14:17], a[24:27], v[70:73], v[14:17]\n\t"
      "ds_read_b128 v[62:65], v100 offset:16784\n\t"
      "v_mfma_f32_16x16x32_bf16 v[22:25], a[152:155], v[70:73], v[22:25]\n\t"
      "buffer_load_dwordx4 v105, s[36:39], s58 offen lds\n\t"
      "s_waitcnt lgkmcnt(4)\n\t"
      "v_mfma_f32_16x16x32_bf16 v[10:13], a[28:31], v[74:77], v[10:13]\n\t"
      "s_add_u32 s61, s61, 4096\n\t"
      "v_mfma_f32_16x16x32_bf16 v[18:21], a[156:159], v[74:77], v[18:21]\n\t"
      "ds_read_b128 v[66:69], v100 offset:160\n\t"
      "v_mfma_f32_16x16x32_bf16 v[14:17], a[28:31], v[78:81], v[14:17]\n\t"
      "ds_read_b128 v[70:73], v100 offset:16800\n\t"
      "v_mfma_f32_16x16x32_bf16 v[22:25], a[156:159], v[78:81], v[22:25]\n\t"
      "s_add_u32 m0, s59, 5200\n\t"
      "s_waitcnt lgkmcnt(4)\n\t"
      "v_mfma_f32_16x16x32_bf16 v[10:13], a[32:35], v[50:53], v[10:13]\n\t"
      "buffer_store_dwordx4 v[46:49], v120, s[40:43], s61 offen sc1\n\t"
      "v_mfma_f32_16x16x32_bf16 v[18:21], a[160:163], v[50:53], v[18:21]\n\t"
      "ds_read_b128 v[74:77], v100 offset:176\n\t"
      "v_mfma_f32_16x16x32_bf16 v[14:17], a[32:35], v[54:57], v[14:17]\n\t"
      "ds_read_b128 v[78:81], v100 offset:16816\n\t"
      "buffer_load_dwordx4 v106, s[36:39], s58 offen lds\n\t"
      "v_mfma_f32_16x16x32_bf16 v[22:25], a[160:163], v[54:57], v[22:25]\n\t"
      "s_waitcnt lgkmcnt(4)\n\t"
      "v_mfma_f32_16x16x32_bf16 v[10:13], a[36:39], v[58:61], v[10:13]\n\t"
      "s_add_u32 m0, s59, 6240\n\t"
      "v_mfma_f32_16x16x32_bf16 v[18:21], a[164:167], v[58:61], v[18:21]\n\t"
      "ds_read_b128 v[50:53], v100 offset:192\n\t"
      "v_mfma_f32_16x16x32_bf16 v[14:17], a[36:39], v[62:65], v[14:17]\n\t"
      "ds_read_b128 v[54:57], v100 offset:16832\n\t"
      "v_mfma_f32_16x16x32_bf16 v[22:25], a[164:167], v[62:65], v[22:25]\n\t"
      "buffer_load_dwordx4 v107, s[36:39], s58 offen lds\n\t"
      "s_waitcnt lgkmcnt(4)\n\t"
      "v_mfma_f32_16x16x32_bf16 v[10:13], a[40:43], v[66:69], v[10:13]\n\t"
      "v_mfma_f32_16x16x32_bf16 v[18:21], a[168:171], v[66:69], v[18:21]\n\t"
      "ds_read_b128 v[58:61], v100 offset:208\n\t"
      "v_mfma_f32_16x16x32_bf16 v[14:17], a[40:43], v[70:73], v[14:17]\n\t"
      "ds_read_b128 v[62:65], v100 offset:16848\n\t"
      "v_mfma_f32_16x16x32_bf16 v[22:25], a[168:171], v[70:73], v[22:25]\n\t"
      "s_add_u32 m0, s59, 7280\n\t"
      "s_waitcnt lgkmcnt(4)\n\t"
      "v_mfma_f32_16x16x32_bf16 v[10:13], a[44:47], v[74:77], v[10:13]\n\t"
      "v_mfma_f32_16x16x32_bf16 v[18:21], a[172:175], v[74:77], v[18:21]\n\t"
      "ds_read_b128 v[66:69], v100 offset:224\n\t"
      "v_mfma_f32_16x16x32_bf16 v[14:17], a[44:47], v[78:81], v[14:17]\n\t"
      "ds_read_b128 v[70:73], v100 offset:16864\n\t"
      "buffer_load_dwordx4 v108, s[36:39], s58 offen lds\n\t"
      "v_mfma_f32_16x16x32_bf16 v[22:25], a[172:175], v[78:81], v[22:25]\n\t"
      "s_waitcnt lgkmcnt(4)\n\t"
      "v_mfma_f32_16x16x32_bf16 v[10:13], a[48:51], v[50:53], v[10:13]\n\t"
      "s_add_u32 s58, s58, 64512\n\t"
      "s_min_u32 s58, s58, 4128768\n\t"
      "v_mfma_f32_16x16x32_bf16 v[18:21], a[176:179], v[50:53], v[18:21]\n\t"
      "ds_read_b128 v[74:77], v100 offset:240\n\t"
      "v_mfma_f32_16x16x32_bf16 v[14:17], a[48:51], v[54:57], v[14:17]\n\t"
      "ds_read_b128 v[78:81], v100 offset:16880\n\t"
      "s_cmp_gt_u32 s56, 2\n\t"
      "v_mfma_f32_16x16x32_bf16 v[22:25], a[176:179], v[54:57], v[22:25]\n\t"
      "s_cbranch_scc0 Lst_w0\n\t"
      "Lst_w0r:\n\t"
      "s_waitcnt vmcnt(21) lgkmcnt(0)\n\t"
      "s_barrier\n\t"
      "v_mfma_f32_16x16x32_bf16 v[10:13], a[52:55], v[58:61], v[10:13]\n\t"
      "s_sub_u32 s59, s59, s60\n\t"
      "s_sub_u32 s59, s59, 33280\n\t"
      "v_mfma_f32_16x16x32_bf16 v[18:21], a[180:183], v[58:61], v[18:21]\n\t"
      "ds_read_b128 v[50:53], v100 offset:33280\n\t"
      "v_mfma_f32_16x16x32_bf16 v[14:17], a[52:55], v[62:65], v[14:17]\n\t"
      "ds_read_b128 v[54:57], v100 offset:49920\n\t"
      "v_mfma_f32_16x16x32_bf16 v[22:25], a[180:183], v[62:65], v[22:25]\n\t"
      "s_cmp_gt_u32 s56, 2\n\t"
      "s_subb_u32 s65, s56, 0\n\t"
      "v_mfma_f32_16x16x32_bf16 v[10:13], a[56:59], v[66:69], v[10:13]\n\t"
      "v_mov_b32 v124, s65\n\t"
      "buffer_store_dword v124, v112, s[44:47], 0 offen sc1\n\t"
      "v_mfma_f32_16x16x32_bf16 v[18:21], a[184:187], v[66:69], v[18:21]\n\t"
      "ds_read_b128 v[58:61], v100 offset:33296\n\t"
      "v_mfma_f32_16x16x32_bf16 v[14:17], a[56:59], v[70:73], v[14:17]\n\t"
      "ds_read_b128 v[62:65], v100 offset:49936\n\t"
      "v_mfma_f32_16x16x32_bf16 v[22:25], a[184:187], v[70:73], v[22:25]\n\t"
      "s_cmp_lg_u32 s56, 1\n\t"
      "s_cbranch_scc0 Lst_f0\n\t"
      "Lst_f0r:\n\t"
      "v_mfma_f32_16x16x32_bf16 v[10:13], a[60:63], v[74:77], v[10:13]\n\t"
      "s_cmp_lt_u32 s56, 60\n\t"
      "s_cselect_b32 s64, 0, 0x7ffff000\n\t"
      "v_mfma_f32_16x16x32_bf16 v[18:21], a[188:191], v[74:77], v[18:21]\n\t"
      "ds_read_b128 v[66:69], v100 offset:33312\n\t"
      "s_add_u32 s57, s56, 4\n\t"
      "v_mfma_f32_16x16x32_bf16 v[14:17], a[60:63], v[78:81], v[14:17]\n\t"
      "ds_read_b128 v[70:73], v100 offset:49952\n\t"
      "s_lshl_b32 s62, s57, 18\n\t"
      "v_mfma_f32_16x16x32_bf16 v[22:25], a[188:191], v[78:81], v[22:25]\n\t"
      "s_waitcnt lgkmcnt(4)\n\t"
      "v_mfma_f32_16x16x32_bf16 v[10:13], a[64:67], v[50:53], v[10:13]\n\t"
      "s_add_u32 m0, s59, 0\n\t"
      "v_mfma_f32_16x16x32_bf16 v[18:21], a[192:195], v[50:53], v[18:21]\n\t"
      "ds_read_b128 v[74:77], v100 offset:33328\n\t"
      "s_add_u32 s62, s62, s50\n\t"
      "v_mfma_f32_16x16x32_bf16 v[14:17], a[64:67], v[54:57], v[14:17]\n\t"
      "ds_read_b128 v[78:81], v100 offset:49968\n\t"
      "s_lshr_b32 s65, s62, 20\n\t"
      "v_mfma_f32_16x16x32_bf16 v[22:25], a[192:195], v[54:57], v[22:25]\n\t"
      "buffer_load_dwordx4 v101, s[36:39], s58 offen lds\n\t"
      "s_waitcnt lgkmcnt(4)\n\t"
      "v_mfma_f32_16x16x32_bf16 v[10:13], a[68:71], v[58:61], v[10:13]\n\t"
      "v_mfma_f32_16x16x32_bf16 v[18:21], a[196:199], v[58:61], v[18:21]\n\t"
      "ds_read_b128 v[50:53], v100 offset:33344\n\t"
      "s_and_b32 s65, s65, 62\n\t"
      "v_mfma_f32_16x16x32_bf16 v[14:17], a[68:71], v[62:65], v[14:17]\n\t"
      "ds_read_b128 v[54:57], v100 offset:49984\n\t"
      "s_lshr_b64 s[66:67], s[48:49], s65\n\t"
      "v_mfma_f32_16x16x32_bf16 v[22:25], a[196:199], v[62:65], v[22:25]\n\t"
      "s_add_u32 m0, s59, 1040\n\t"
      "s_waitcnt lgkmcnt(4)\n\t"
      "v_mfma_f32_16x16x32_bf16 v[10:13], a[72:75], v[66:69], v[10:13]\n\t"
      "v_mfma_f32_16x16x32_bf16 v[18:21], a[200:203], v[66:69], v[18:21]\n\t"
      "ds_read_b128 v[58:61], v100 offset:33360\n\t"
      "s_bfe_u32 s65, s62, 0x10013\n\t"
      "v_mfma_f32_16x16x32_bf16 v[14:17], a[72:75], v[70:73], v[14:17]\n\t"
      "ds_read_b128 v[62:65], v100 offset:50000\n\t"
      "buffer_load_dwordx4 v102, s[36:39], s58 offen lds\n\t"
      "v_mfma_f32_16x16x32_bf16 v[22:25], a[200:203], v[70:73], v[22:25]\n\t"
      "s_waitcnt lgkmcnt(4)\n\t"
      "v_mfma_f32_16x16x32_bf16 v[10:13], a[76:79], v[74:77], v[10:13]\n\t"
      "s_add_u32 m0, s59, 2080\n\t"
      "v_mfma_f32_16x16x32_bf16 v[18:21], a[204:207], v[74:77], v[18:21]\n\t"
      "ds_read_b128 v[66:69], v100 offset:33376\n\t"
      "s_lshl1_add_u32 s66, s65, s66\n\t"
      "v_mfma_f32_16x16x32_bf16 v[14:17], a[76:79], v[78:81], v[14:17]\n\t"
      "ds_read_b128 v[70:73], v100 offset:50016\n\t"
      "s_and_b32 s66, s66, 3\n\t"
      "v_mfma_f32_16x16x32_bf16 v[22:25], a[204:207], v[78:81], v[22:25]\n\t"
      "buffer_load_dwordx4 v103, s[36:39], s58 offen lds\n\t"
      "s_waitcnt lgkmcnt(4)\n\t"
      "v_mfma_f32_16x16x32_bf16 v[10:13], a[80:83], v[50:53], v[10:13]\n\t"
      "v_or_b32 v121, s64, v110\n\t"
      "v_mfma_f32_16x16x32_bf16 v[18:21], a[208:211], v[50:53], v[18:21]\n\t"
      "ds_read_b128 v[74:77], v100 offset:33392\n\t"
      "s_lshl_b32 s66, s66, 13\n\t"
      "v_mfma_f32_16x16x32_bf16 v[14:17], a[80:83], v[54:57], v[14:17]\n\t"
      "ds_read_b128 v[78:81], v100 offset:50032\n\t"
      "s_or_b32 s62, s62, s66\n\t"
      "v_mfma_f32_16x16x32_bf16 v[22:25], a[208:211], v[54:57], v[22:25]\n\t"
      "s_add_u32 m0, s59, 3120\n\t"
      "s_waitcnt lgkmcnt(4)\n\t"
      "v_mfma_f32_16x16x32_bf16 v[10:13], a[84:87], v[58:61], v[10:13]\n\t"
      "v_or_b32 v122, s64, v111\n\t"
      "v_mfma_f32_16x16x32_bf16 v[18:21], a[212:215], v[58:61], v[18:21]\n\t"
      "ds_read_b128 v[50:53], v100 offset:33408\n\t"
      "s_lshl_b32 s63, s57, 16\n\t"
      "v_mfma_f32_16x16x32_bf16 v[14:17], a[84:87], v[62:65], v[14:17]\n\t"
      "ds_read_b128 v[54:57], v100 offset:50048\n\t"
      "buffer_load_dwordx4 v104, s[36:39], s58 offen lds\n\t"
      "v_mfma_f32_16x16x32_bf16 v[22:25], a[212:215], v[62:65], v[22:25]\n\t"
      "s_waitcnt lgkmcnt(4)\n\t"
      "v_mfma_f32_16x16x32_bf16 v[10:13], a[88:91], v[66:69], v[10:13]\n\t"
      "s_add_u32 m0, s59, 4160\n\t"
      "v_mfma_f32_16x16x32_bf16 v[18:21], a[216:219], v[66:69], v[18:21]\n\t"
      "ds_read_b128 v[58:61], v100 offset:33424\n\t"
      "s_lshr_b32 s65, s63, 20\n\t"
      "v_mfma_f32_16x16x32_bf16 v[14:17], a[88:91], v[70:73], v[14:17]\n\t"
      "ds_read_b128 v[62:65], v100 offset:50064\n\t"
      "s_and_b32 s65, s65, 62\n\t"
      "v_mfma_f32_16x16x32_bf16 v[22:25], a[216:219], v[70:73], v[22:25]\n\t"
      "buffer_load_dwordx4 v105, s[36:39], s58 offen lds\n\t"
      "s_waitcnt lgkmcnt(4)\n\t"
      "v_mfma_f32_16x16x32_bf16 v[10:13], a[92:95], v[74:77], v[10:13]\n\t"
      "v_mfma_f32_16x16x32_bf16 v[18:21], a[220:223], v[74:77], v[18:21]\n\t"
      "ds_read_b128 v[66:69], v100 offset:33440\n\t"
      "s_lshr_b64 s[66:67], s[48:49], s65\n\t"
      "v_mfma_f32_16x16x32_bf16 v[14:17], a[92:95], v[78:81], v[14:17]\n\t"
      "ds_read_b128 v[70:73], v100 offset:50080\n\t"
      "s_bfe_u32 s65, s63, 0x10013\n\t"
      "v_mfma_f32_16x16x32_bf16 v[22:25], a[220:223], v[78:81], v[22:25]\n\t"
      "s_add_u32 m0, s59, 5200\n\t"
      "s_waitcnt lgkmcnt(4)\n\t"
      "v_mfma_f32_16x16x32_bf16 v[10:13], a[96:99], v[50:53], v[10:13]\n\t"
      "v_mfma_f32_16x16x32_bf16 v[18:21], a[224:227], v[50:53], v[18:21]\n\t"
      "ds_read_b128 v[74:77], v100 offset:33456\n\t"
      "s_lshl1_add_u32 s66, s65, s66\n\t"
      "v_mfma_f32_16x16x32_bf16 v[14:17], a[96:99], v[54:57], v[14:17]\n\t"
      "ds_read_b128 v[78:81], v100 offset:50096\n\t"
      "buffer_load_dwordx4 v106, s[36:39], s58 offen lds\n\t"
      "v_mfma_f32_16x16x32_bf16 v[22:25], a[224:227], v[54:57], v[22:25]\n\t"
      "s_waitcnt lgkmcnt(4)\n\t"
      "v_mfma_f32_16x16x32_bf16 v[10:13], a[100:103], v[58:61], v[10:13]\n\t"
      "s_add_u32 m0, s59, 6240\n\t"
      "v_mfma_f32_16x16x32_bf16 v[18:21], a[228:231], v[58:61], v[18:21]\n\t"
      "ds_read_b128 v[50:53], v100 offset:33472\n\t"
      "s_and_b32 s66, s66, 3\n\t"
      "v_mfma_f32_16x16x32_bf16 v[14:17], a[100:103], v[62:65], v[14:17]\n\t"
      "ds_read_b128 v[54:57], v100 offset:50112\n\t"
      "s_lshl_b32 s66, s66, 13\n\t"
      "v_mfma_f32_16x16x32_bf16 v[22:25], a[228:231], v[62:65], v[22:25]\n\t"
      "buffer_load_dwordx4 v107, s[36:39], s58 offen lds\n\t"
      "s_waitcnt lgkmcnt(4)\n\t"
      "v_mfma_f32_16x16x32_bf16 v[10:13], a[104:107], v[66:69], v[10:13]\n\t"
      "v_mfma_f32_16x16x32_bf16 v[18:21], a[232:235], v[66:69], v[18:21]\n\t"
      "ds_read_b128 v[58:61], v100 offset:33488\n\t"
      "s_or_b32 s63, s63, s66\n\t"
      "v_mfma_f32_16x16x32_bf16 v[14:17], a[104:107], v[70:73], v[14:17]\n\t"
      "ds_read_b128 v[62:65], v100 offset:50128\n\t"
      "v_mfma_f32_16x16x32_bf16 v[22:25], a[232:235], v[70:73], v[22:25]\n\t"
      "s_add_u32 m0, s59, 7280\n\t"
      "s_waitcnt lgkmcnt(4)\n\t"
      "v_mfma_f32_16x16x32_bf16 v[10:13], a[108:111], v[74:77], v[10:13]\n\t"
      "v_mfma_f32_16x16x32_bf16 v[18:21], a[236:239], v[74:77], v[18:21]\n\t"
      "ds_read_b128 v[66:69], v100 offset:33504\n\t"
      "v_mfma_f32_16x16x32_bf16 v[14:17], a[108:111], v[78:81], v[14:17]\n\t"
      "ds_read_b128 v[70:73], v100 offset:50144\n\t"
      "buffer_load_dwordx4 v108, s[36:39], s58 offen lds\n\t"
      "v_mfma_f32_16x16x32_bf16 v[22:25], a[236:239], v[78:81], v[22:25]\n\t"
      "buffer_store_dwordx4 v[116:119], v121, s[44:47], s62 offen sc1\n\t"
      "s_waitcnt lgkmcnt(4)\n\t"
      "v_mfma_f32_16x16x32_bf16 v[10:13], a[112:115], v[50:53], v[10:13]\n\t"
      "s_add_u32 s58, s58, 1024\n\t"
      "buffer_store_dwordx4 v[116:119], v122, s[44:47], s63 offen sc1\n\t"
      "v_mfma_f32_16x16x32_bf16 v[18:21], a[240:243], v[50:53], v[18:21]\n\t"
      "ds_read_b128 v[74:77], v100 offset:33520\n\t"
      "v_mfma_f32_16x16x32_bf16 v[14:17], a[112:115], v[54:57], v[14:17]\n\t"
      "ds_read_b128 v[78:81], v100 offset:50160\n\t"
      "v_mfma_f32_16x16x32_bf16 v[22:25], a[240:243], v[54:57], v[22:25]\n\t"
      "s_waitcnt vmcnt(21) lgkmcnt(0)\n\t"
      "s_barrier\n\t"
      "v_mfma_f32_16x16x32_bf16 v[10:13], a[116:119], v[58:61], v[10:13]\n\t"
      "v_add_u32 v100, s60, v100\n\t"
      "s_add_u32 s59, s59, 33280\n\t"
      "v_mfma_f32_16x16x32_bf16 v[18:21], a[244:247], v[58:61], v[18:21]\n\t"
      "ds_read_b128 v[50:53], v100 offset:0\n\t"
      "v_mfma_f32_16x16x32_bf16 v[14:17], a[116:119], v[62:65], v[14:17]\n\t"
      "ds_read_b128 v[54:57], v100 offset:16640\n\t"
      "v_mfma_f32_16x16x32_bf16 v[22:25], a[244:247], v[62:65], v[22:25]\n\t"
      "s_sub_u32 s60, 0, s60\n\t"
      "v_mfma_f32_16x16x32_bf16 v[10:13], a[120:123], v[66:69], v[10:13]\n\t"
      "s_lshl_b32 s61, s56, 14\n\t"
      "v_mov_b32 v120, v109\n\t"
      "v_mfma_f32_16x16x32_bf16 v[18:21], a[248:251], v[66:69], v[18:21]\n\t"
      "ds_read_b128 v[58:61], v100 offset:16\n\t"
      "v_mfma_f32_16x16x32_bf16 v[14:17], a[120:123], v[70:73], v[14:17]\n\t"
      "ds_read_b128 v[62:65], v100 offset:16656\n\t"
      "v_mfma_f32_16x16x32_bf16 v[22:25], a[248:251], v[70:73], v[22:25]\n\t"
      "s_add_u32 s56, s56, 1\n\t"
      "v_mfma_f32_16x16x32_bf16 v[26:29], a[124:127], v[74:77], v[10:13]\n\t"
      "s_cmp_lt_u32 s56, 64\n\t"
      "v_mfma_f32_16x16x32_bf16 v[34:37], a[252:255], v[74:77], v[18:21]\n\t"
      "ds_read_b128 v[66:69], v100 offset:32\n\t"
      "v_mfma_f32_16x16x32_bf16 v[30:33], a[124:127], v[78:81], v[14:17]\n\t"
      "ds_read_b128 v[70:73], v100 offset:16672\n\t"
      "v_mfma_f32_16x16x32_bf16 v[38:41], a[252:255], v[78:81], v[22:25]\n\t"
      "s_cbranch_scc1 Lst_loop\n\t"
      "s_nop 7\n\t"
      "v_cvt_pk_bf16_f32 v42, v26, v27\n\t"
      "v_cvt_pk_bf16_f32 v43, v28, v29\n\t"
      "v_cvt_pk_bf16_f32 v44, v34, v35\n\t"
      "v_cvt_pk_bf16_f32 v45, v36, v37\n\t"
      "v_cvt_pk_bf16_f32 v46, v30, v31\n\t"
      "v_cvt_pk_bf16_f32 v47, v32, v33\n\t"
      "v_cvt_pk_bf16_f32 v48, v38, v39\n\t"
      "v_cvt_pk_bf16_f32 v49, v40, v41\n\t"
      "buffer_store_dwordx4 v[42:45], v109, s[40:43], s61 offen sc1\n\t"
      "s_add_u32 s61, s61, 4096\n\t"
      "buffer_store_dwordx4 v[46:49], v109, s[40:43], s61 offen sc1\n\t"
      "v_mov_b32 v124, 64\n\t"
      "s_waitcnt vmcnt(0) lgkmcnt(0)\n\t"
      "s_barrier\n\t"
      "buffer_store_dword v124, v112, s[44:47], 0 offen sc1\n\t"
      "s_branch Lst_end\n\t"
      "Lst_w0:\n\t"
      "s_waitcnt vmcnt(0)\n\t"
      "s_branch Lst_w0r\n\t"
      "Lst_f0:\n\t"
      "v_mov_b32 v126, s52\n\t"
      "v_mov_b32 v127, s53\n\t"
      "buffer_store_dwordx2 v[126:127], v113, s[44:47], 0 offen sc1\n\t"
      "s_branch Lst_f0r\n\t"
      "Lst_end:\n\t"
      : "+{v[100:115]}"(C)
      : "{a[0:31]}"(WA[0]), "{a[32:63]}"(WA[1]), "{a[64:95]}"(WA[2]), "{a[96:127]}"(WA[3]), "{a[128:159]}"(WA[4]), "{a[160:191]}"(WA[5]), "{a[192:223]}"(WA[6]), "{a[224:255]}"(WA[7]),
        "{s[36:39]}"(__builtin_amdgcn_make_buffer_rsrc((void*)F.Ax, 0, S_ * DM * 2, 0x00020000)),
        "{s[40:43]}"(__builtin_amdgcn_make_buffer_rsrc(uni(wsp(F, faro(pm, QA0 + ((3 * h + (mat ^ (mat < 2))) << 20)))), 0, (1 << 20) - 8192, 0x00020000)),
        "{s[44:47]}"(__builtin_amdgcn_make_buffer_rsrc(F.obuf, 0, HBASE + (int)sizeof(Hdr), 0x00020000)), "{s[48:55]}"(S)
      : R8(v, 1), R8(v, 2), R8(v, 3), R8(v, 4), R8(v, 5), R8(v, 6), R8(v, 7), R8(v, 8), R8(v, 9), "v116", "v117", "v118", "v119", R8(v, 12),
        "s56", "s57", "s58", "s59", R8(s, 6), "s86", "s87", "scc", "m0", "memory");
  return pm;
}

__device__ __forceinline__ float dot8(v4u a, v4u b) {
  float d = 0.f;
#pragma unroll
  for (int e = 0; e < 4; ++e) { const unsigned x = a[e], y = b[e]; d = __builtin_amdgcn_fdot2_f32_bf16(__builtin_bit_cast(bfx2, x), __builtin_bit_cast(bfx2, y), d, false); }
  return d;
}
template <int CTRL> __device__ __forceinline__ float qperm(float v) { return __int_as_float(__builtin_amdgcn_update_dpp(0, __float_as_int(v), CTRL, 0xf, 0xf, true)); }
__device__ __forceinline__ unsigned attnB_32(const FParams& F, const Plc pm, int pos0, unsigned* qh, int wv) {
  unsigned tn = 0;
  const int tid = opaque_tid(), cc = tid & 127, l3 = tid & 3;
  int p0 = pos0 + (tid >> 7);
  const __amdgpu_buffer_rsrc_t rs = __builtin_amdgcn_make_buffer_rsrc(wsp(F, RB0), 0, S_ * 16384, 0x00020000);
  const int f1 = faro(pm, RB0 + pos0 * 16384) & 8192, f0 = faro(pm, RB0 + (pos0 < 16 ? 0 : pos0 - 16) * 16384) & 8192;
  v4u k[8], qn[8], kn[8], vn[8], h0[7]; f32x2 v[8][4];
#pragma unroll
  for (int i = 0; i < 7; ++i) {
    const int kp = p0 - 14 + 2 * i, o = (kp < 0 ? 0 : kp) * 16384 + f0 + cc * 16;
    k[i] = __builtin_amdgcn_raw_buffer_load_b128(rs, o, DB * 2, 16); h0[i] = __builtin_amdgcn_raw_buffer_load_b128(rs, o, 2 * DB * 2, 16);
  }
  int vo = p0 * 16384 + f1 + cc * 16;
#define LDROW(i) do { kn[i] = __builtin_amdgcn_raw_buffer_load_b128(rs, vo + DB * 2, (i) * 32768, 16); qn[i] = __builtin_amdgcn_raw_buffer_load_b128(rs, vo, (i) * 32768, 16); \
    vn[i] = __builtin_amdgcn_raw_buffer_load_b128(rs, vo, (i) * 32768 + 2 * DB * 2, 16); } while (0)
#define UNPK(d, x) do { _Pragma("unroll") for (int e = 0; e < 4; ++e) d[e] = (f32x2){__uint_as_float((x)[e] << 16), __uint_as_float((x)[e] & 0xffff0000u)}; } while (0)
#pragma unroll
  for (int i = 0; i < 8; ++i) LDROW(i);
#pragma unroll
  for (int i = 0; i < 7; ++i) UNPK(v[i], h0[i]);
  const float scale = 0.17677669529663687f;
#pragma unroll 1
  for (int it = 0; it < 2; ++it) {
    vo = it == 0 ? vo + 16 * 16384 : 0x7ffff000;
    if (it == 1 && tid == 0) tn = __hip_atomic_fetch_add(qh, 1u, RLX_AGENT);
    SB0;
#pragma unroll
    for (int i = 0; i < 8; ++i) {
      const v4u q = qn[i];
      k[(i + 7) & 7] = kn[i]; UNPK(v[(i + 7) & 7], vn[i]);
      const int pos = p0 + 2 * i;
      float d[8];
#pragma unroll
      for (int w = 0; w < 8; ++w) d[w] = dot8(q, k[(i + 7 - w) & 7]);
      SB0;
      LDROW(i);
      SB0;
      float a[4], g[2];
#pragma unroll
      for (int j = 0; j < 4; ++j) a[j] = ((l3 & 1) ? d[2 * j + 1] : d[2 * j]) + qperm<0xB1>((l3 & 1) ? d[2 * j] : d[2 * j + 1]);
#pragma unroll
      for (int j = 0; j < 2; ++j) g[j] = ((l3 & 2) ? a[2 * j + 1] : a[2 * j]) + qperm<0x4E>((l3 & 2) ? a[2 * j] : a[2 * j + 1]);
      unsigned p = pk2(g[0], g[1]);
      p = pk2(__uint_as_float(p << 16) * scale, __uint_as_float(p & 0xffff0000u) * scale);
      g[0] = pos - 2 * l3 >= 0 ? __uint_as_float(p << 16) : -INFINITY; g[1] = pos - 2 * l3 - 8 >= 0 ? __uint_as_float(p & 0xffff0000u) : -INFINITY;
      float mx = fmaxf(g[0], g[1]);
      mx = fmaxf(mx, qperm<0xB1>(mx)); mx = fmaxf(mx, qperm<0x4E>(mx));
      g[0] = __expf(g[0] - mx); g[1] = __expf(g[1] - mx);
      float sum = g[0] + g[1];
      sum += qperm<0xB1>(sum); sum += qperm<0x4E>(sum);
      const float inv = __builtin_amdgcn_rcpf(sum);
      p = pk2(g[0] * inv, g[1] * inv);
      g[0] = __uint_as_float(p << 16); g[1] = __uint_as_float(p & 0xffff0000u);
      f32x2 o[4] = {{0.f, 0.f}, {0.f, 0.f}, {0.f, 0.f}, {0.f, 0.f}};
#pragma unroll
      for (int w = 0; w < 8; ++w) {
        const float gw = g[w >> 2], pw = (w & 3) == 0 ? qperm<0x00>(gw) : ((w & 3) == 1 ? qperm<0x55>(gw) : ((w & 3) == 2 ? qperm<0xAA>(gw) : qperm<0xFF>(gw)));
#pragma unroll
        for (int e = 0; e < 4; ++e) o[e] = __builtin_elementwise_fma((f32x2){pw, pw}, v[(i + 7 - w) & 7][e], o[e]);
      }
      __builtin_amdgcn_raw_buffer_store_b128((v4u){pk2(o[0].x, o[0].y), pk2(o[1].x, o[1].y), pk2(o[2].x, o[2].y), pk2(o[3].x, o[3].y)}, rs, pos * 16384 + f1 + cc * 16, 3 * DB * 2, 16);
      SB0;
    }
    p0 += 16;
  }
#undef LDROW
#undef UNPK
  return tn;
}

constexpr int NTASK = 111;
__constant__ unsigned TASKS[5][NTASK] = {
  {
  2051,2179,2307,2435,2056,2184,2312,2440,2059,2187,2315,2443,5120,5121,5122,5123,5124,5125,5126,5127,5184,5185,5186,5187,3074,3075,3136,3137,3138,3139,3200,3201,
  3202,3203,3264,3265,3520,3521,3522,3523,5380,5381,5382,5383,4096,4097,4098,4099,3840,3841,3842,3843,4480,4481,4482,4483,5696,5697,5698,5699,5824,5825,5826,5827,
  5828,5829,5830,5831,3906,3907,3968,3969,3970,3971,4032,4033,4034,4035,7236,7237,7238,7239,7240,7241,7242,7243,8128,8129,8130,8131,8156,8157,8158,8159,6920,6921,
  6922,6923,6924,6925,6926,6927,4864,4865,4866,4867,4992,4993,4994,4995,65535,
  },
  {
  2048,2176,2304,2432,2052,2180,2308,2436,2057,2185,2313,2441,2563,2691,2819,2947,5188,5189,5190,5191,2568,2696,2824,2952,5312,5313,5314,5315,3266,3267,3394,3395,
  5440,5441,5442,5443,5508,5509,5510,5511,5632,5633,5634,5635,3712,3713,7168,7169,7170,7171,7180,7181,7182,7183,5700,5701,5702,5703,4608,4609,4610,4611,7244,7245,
  7246,7247,7248,7249,7250,7251,7252,7253,7254,7255,8136,8137,8138,8139,8148,8149,8150,8151,65535,65535,65535,65535,65535,65535,65535,65535,65535,65535,65535,65535,65535,65535,
  65535,65535,65535,65535,65535,65535,65535,65535,65535,65535,65535,65535,65535,65535,65535,
  },
  {
  2049,2177,2305,2433,2053,2181,2309,2437,2058,2186,2314,2442,2564,2692,2820,2948,5248,5249,5250,5251,2569,2697,2825,2953,5316,5317,5318,5319,3328,3329,3456,3457,
  5444,5445,5446,5447,5568,5569,5570,5571,5636,5637,5638,5639,3714,3715,7172,7173,7174,7175,7184,7185,7186,7187,5760,5761,5762,5763,5764,5765,5766,5767,7256,7257,
  7258,7259,7260,7261,7262,7263,8064,8065,8066,8067,8140,8141,8142,8143,8152,8153,8154,8155,6916,6917,6918,6919,65535,65535,65535,65535,65535,65535,65535,65535,65535,65535,
  65535,65535,65535,65535,65535,65535,65535,65535,65535,65535,65535,65535,65535,65535,65535,
  },
  {
  2560,2688,2816,2944,2054,2182,2310,2438,2561,2689,2817,2945,2565,2693,2821,2949,5252,5253,5254,5255,2570,2698,2826,2954,5376,5377,5378,5379,3330,3331,3458,3459,
  5504,5505,5506,5507,4352,4353,4354,4355,5572,5573,5574,5575,3776,3777,7176,7177,7178,7179,7188,7189,7190,7191,3904,3905,8068,8069,8070,8071,8072,8073,8074,8075,
  8076,8077,8078,8079,8080,8081,8082,8083,8144,8145,8146,8147,4736,4737,4738,4739,65535,65535,65535,65535,65535,65535,65535,65535,65535,65535,65535,65535,65535,65535,65535,65535,
  65535,65535,65535,65535,65535,65535,65535,65535,65535,65535,65535,65535,65535,65535,65535,
  },
  {
  2050,2178,2306,2434,2055,2183,2311,2439,2562,2690,2818,2946,2566,2694,2822,2950,2567,2695,2823,2951,2571,2699,2827,2955,3072,3073,3392,3393,4224,4225,4226,4227,
  3584,3585,3586,3587,3648,3649,3650,3651,3778,3779,7192,7193,7194,7195,7196,7197,7198,7199,7232,7233,7234,7235,8084,8085,8086,8087,8088,8089,8090,8091,8092,8093,
  8094,8095,8132,8133,8134,8135,6912,6913,6914,6915,65535,65535,65535,65535,65535,65535,65535,65535,65535,65535,65535,65535,65535,65535,65535,65535,65535,65535,65535,65535,65535,65535,
  65535,65535,65535,65535,65535,65535,65535,65535,65535,65535,65535,65535,65535,65535,65535,
  },
};
__device__ __forceinline__ void worker(const FParams& F, char* lds, int wv) {
  unsigned long long tok;
  const unsigned long long ep = ld_agent(&F.hdr->epoch[0]);
  const int w = wv;
#define tid opaque_tid()
  const int x = (blockIdx.x & 7) - 3;
  Ctl* ctl = F.ctl;
  int* sh = (int*)(lds + 3 * wg::STAGE_B);
#define TOUCH(ptr, bytes, off) __builtin_amdgcn_raw_ptr_buffer_load_lds(__builtin_amdgcn_make_buffer_rsrc((void*)(ptr), 0, bytes, 0x00020000), \
        (__attribute__((address_space(3))) void*)(lds + 3 * wg::STAGE_B + w * 4096 + 1024), 4, off, 0, 0, 0)
  bool picked = false;
  unsigned* psig = nullptr; int pn = 0;
#define FLUSH_SIG do { if (pn) { asm volatile("s_waitcnt vmcnt(0)" ::: "memory"); __syncthreads(); LATE_SIGNAL; pn = 0; } } while (0)
  Plc pm = {0, 0};
  const v4u ff = {0xffffffffu, 0xffffffffu, 0xffffffffu, 0xffffffffu};
  const int wid = (blockIdx.x >> 3) * 5 + x;
  const unsigned t00 = (unsigned)__builtin_amdgcn_s_memrealtime();
  if (wid < 96) {
    const int tz = opaque_tid();
    if (wid < 16) TOUCH(F.Ax, S_ * DM * 2, (wid * 256 + tz) * 128);
    if (wid >= 64) {
      const int n1 = wid - 64;
      if (F.sh3 & 8)
#pragma unroll
        for (int k = 0; k < 3; ++k) { const u16* wp = k == 0 ? F.Wq : (k == 2 ? F.Wo : F.Wk); TOUCH(wp, DM * HDA * 2, (n1 * 256 + tz) * 1024 + ((384 - (int)(size_t)wp) & 1023)); }
      if (F.sh3 & 2)
#pragma unroll 4
        for (int k = 0; k < 8; ++k) TOUCH(F.Wo, DM * HDA * 2, ((k * 32 + n1) * 256 + tz) * 128);
    }
    if (wid >= 64) while ((int)(t00 + ((F.sh3 >> 16) & 255) * 100 - (unsigned)__builtin_amdgcn_s_memrealtime()) > 0) __builtin_amdgcn_s_sleep(32);
    pm = qkv_task(F, ep, wid < 32 ? 0 : (wid < 64 ? 2 : 1), wid & 31, lds, wv);
    wait_votes(F, uni64(ep) + 1, opaque_lane(), true);
  } else {
    const int tz = opaque_tid();
    {
      const int u = wid - 96;
      const __amdgpu_buffer_rsrc_t rh = __builtin_amdgcn_make_buffer_rsrc(F.obuf, 0, CBASE + (int)sizeof(Ctl), 0x00020000);
      if (u == NPROBER - 1) {
        for (int i = tz; i < (int)sizeof(Ctl) / 16; i += 256) __builtin_amdgcn_raw_buffer_store_b128((v4u){0u, 0u, 0u, 0u}, rh, CBASE + i * 16, 0, 16);
        asm volatile("s_waitcnt vmcnt(0)" ::: "memory");
        __syncthreads();
        if (tid == 0) __hip_atomic_store(&F.hdr->token[0], ep + 1, RLX_AGENT);
      }
      if (w == 0) {
        const __amdgpu_buffer_rsrc_t ra = __builtin_amdgcn_make_buffer_rsrc(F.obuf, 0, ARENA, 0x00020000);
        unsigned xcc, sp = 0; asm volatile("s_getreg_b32 %0, hwreg(HW_REG_XCC_ID)" : "=s"(xcc));
#pragma unroll 1
        for (int pg = 0; pg < (u < NPAGE / 2 ? 2 : 0); ++pg) {
          unsigned best0 = ~0u, best1 = ~0u;
#pragma unroll 1
          for (int k = 0; k < PROBES; ++k) {
            const unsigned long long t0 = __builtin_amdgcn_s_memtime();
            __builtin_amdgcn_raw_buffer_store_b128(ff, ra, tz < 8 ? ((2 * u + pg) << 21) + (1 << 20) + 49152 + (k & 1) * 8192 + tz * 16 : 0x7ffffff0, 0, 16);
            asm volatile("s_waitcnt vmcnt(0)" ::: "memory");
            const unsigned dt = (unsigned)(__builtin_amdgcn_s_memtime() - t0);
            if (k & 1) best1 = dt < best1 ? dt : best1; else best0 = dt < best0 ? dt : best0;
            if (k >= 3 && (k & 1) && (best0 < PNEAR) != (best1 < PNEAR)) break;
          }
          sp |= ((best1 < best0) ^ (((xcc ^ (xcc - (blockIdx.x & 7) + 1)) >> 2) & 1)) << pg;
        }
        if (u >= 48) {
          unsigned bs[4] = {~0u, ~0u, ~0u, ~0u}; int sc = 0;
#pragma unroll 1
          for (int k = 0; k < SPROBES; ++k) {
#pragma unroll
            for (int q = 0; q < 4; ++q) {
              const unsigned long long t0 = __builtin_amdgcn_s_memtime();
              __builtin_amdgcn_raw_buffer_store_b128(ff, ra, tz < 8 ? ((u - 48) << 21) + (1 << 20) + 32768 + q * 8192 + 1024 + tz * 16 : 0x7ffffff0, 0, 16);
              asm volatile("s_waitcnt vmcnt(0)" ::: "memory");
              const unsigned dt = (unsigned)(__builtin_amdgcn_s_memtime() - t0);
              bs[q] = dt < bs[q] ? dt : bs[q];
            }
            const bool f1 = bs[1] + bs[3] > bs[0] + bs[2];
            const int d0 = (int)(bs[0] - bs[2]), d1 = (int)(bs[1] - bs[3]), df = f1 ? d1 : d0, dn = (xcc & 1) ? (f1 ? d0 : d1) : -(f1 ? d0 : d1);
            sc = df + dn;
            if (k >= 2 && ((df > 24 && df < 80 && dn > 24 && dn < 80) || (df < -24 && df > -80 && dn < -24 && dn > -80))) break;
          }
          sp = (sc > 0) ^ ((xcc - (blockIdx.x & 7)) & 1);
        }
        if (tz == 0) __hip_atomic_store(&F.hdr->vote[u], 4 * (ep + 1) + sp, RLX_AGENT);
      }
      __syncthreads();
#pragma unroll
      for (int k = 0; k < 8; ++k) __builtin_amdgcn_raw_buffer_store_b128(ff, rh, u * 16384 + (k & 3) * 4096 + tz * 16, k < 4 ? OB0 : VB0, 16);
      { v4u v = ff;
        if (u < 16) {
          const int i = (u >> 3) * 512 + (u & 1) * 256 + tz, c = tr4(i >> 6) * 4 + ((i >> 4) & 3), sl = i & 15, r = (sl >> 2) * 2 + (sl & 1), hh = (sl >> 1) & 1;
#pragma unroll
          for (int p = 0; p < 4; ++p) v[p] = nosent(*(const unsigned*)(F.mem0 + r * DM + 16 * c + 4 * p + 2 * hh));
        }
        __builtin_amdgcn_raw_buffer_store_b128(v, rh, u * 4096 + tz * 16, MB0, 16); }
      if ((u >> 3) == 2) __builtin_amdgcn_raw_buffer_store_b128((u & 5) == 0 && tz == 0 ? (v4u){0u, 0u, 0u, 0u} : ff, rh, (u & 7) * 4096 + tz * 16, RC0, 16);
      asm volatile("s_waitcnt vmcnt(0)" ::: "memory");
      __syncthreads();
      if (tid == 0) __hip_atomic_store(&F.hdr->fill[u], ep + 1, RLX_AGENT);
    }
    if (F.sh3 & 64)
#pragma unroll
      for (int k = 0; k < 4; ++k) TOUCH(F.Wo, DM * HDA * 2, ((k * 64 + wid - 96) * 256 + tz) * 128);
    const int vo = ((wid - 96) * 256 + tz) * 128;
#pragma unroll
    for (int k = 0; k < 8; ++k) {
      while ((int)(t00 + ((F.sh3 >> 8) & 255) * 100 + k * ((F.sh3 >> 24) & 127) * 10 - (unsigned)__builtin_amdgcn_s_memrealtime()) > 0) __builtin_amdgcn_s_sleep(16);
      if (k < 2 || !(F.sh3 & 128)) TOUCH(k < 2 ? F.Ax : (k < 4 ? F.Bx : (k < 7 ? F.wqkvB : F.woB)), (k < 4 ? S_ * DB : (k < 7 ? DB * NQKVB : DB * DB)) * 2, vo + (k < 4 ? k & 1 : (k < 7 ? k - 4 : 0)) * 64 * 256 * 128);
    }
#pragma unroll
    for (int k = 0; k < 8; ++k) __builtin_amdgcn_raw_buffer_store_b128(ff, __builtin_amdgcn_make_buffer_rsrc(wsp(F, PT0), 0, 2 << 20, 0x00020000), tz * 16, (wid - 96) * 32768 + k * 4096, 16);
    tok = uni64(ep) + 1;
    wait_token(F, tok);
    pm = wait_votes(F, tok, tz & 63, true);
    asm volatile("s_waitcnt vmcnt(0)" ::: "memory");
    __syncthreads();
    wg_signal(&ctl->done[16], wv);
  }
  leave_epoch(F, wv);
  int last;
  for (;;) {
    const int lane = opaque_lane();
    pn = __builtin_amdgcn_readfirstlane(pn);
    if (!picked) {
      if (tid == 0) sh[0] = (int)__hip_atomic_fetch_add(&ctl->qhead[x][0], 1u, RLX_AGENT);
      __syncthreads();
    }
    const int t = __builtin_amdgcn_readfirstlane(sh[0]);
    lds_barrier();
    picked = true;
    const int code = (int)TASKS[x][t < NTASK ? t : NTASK - 1];
    if ((code >> 10) > 2) FLUSH_SIG;
    if (code == 0xFFFF) return;
    const int tk = (code >> 10) & 7, r = (code >> 6) & 15, n = code & 63;
    const int rb0 = faro(pm, RB0 + (r << 21)), rbd = faro(pm, RB0 + ((r | 1) << 21)) - rb0 - (1 << 21);
    if (tk == 2 || tk == 4) {
      if (tk == 4) wg_wait(lane < 2 ? &ctl->cntAB[r + lane][0] : nullptr, 4, ctl, true, wv);
      wg::gemm_big(tk == 2 ? F.Bx + (size_t)r * 128 * DB : wsp(F, rb0 + NQKVB * 2), tk == 2 ? DB : LDO, (tk == 2 ? F.wqkvB : F.woB) + n * 256, tk == 2 ? NQKVB : DB,
                   tk == 2 ? wsp(F, rb0 + n * 512) : F.out + (size_t)r * 128 * 2 * DM + DM + n * 256, tk == 2 ? LDO : 2 * DM, 128, tk == 2 ? 0 : rbd, tk == 2 ? rbd : 0, false, lds, psig, pn, &ctl->qhead[x][0], wv);
      psig = &ctl->cntB[r][0]; pn = tk == 2;
    } else if (tk == 3) {
      wg_wait(lane < 2 && r - lane >= 0 ? &ctl->cntB[(r - lane) & 14][0] : nullptr, 12, ctl, false, wv);
      const unsigned tn = attnB_32(F, pm, r * 128 + n * 32, &ctl->qhead[x][0], wv);
      if (tid == 0) sh[0] = tn;
      lds_barrier();
      psig = &ctl->cntAB[r][0]; pn = 1;
    } else if (tk < 7) {
      wg_wait(lane < 12 ? &ctl->prog[4 * (n & 7) + (lane & 3)][(lane >> 2) * 8] : nullptr, tk == 5 ? 4 * r + 4 : NSEG, ctl, false, wv);
      attnA<false>(F, pm, 4 * r + (n >> 3), tk == 5 ? 4 * r + 4 : LASTP, tk - 4, 4 * (n & 7) + w, (unsigned)(size_t)lds + w * 16384, 0);
      picked = false;
    } else { last = code; break; }
  }
  wg_wait(opaque_lane() == 0 ? &ctl->done[16] : nullptr, 64, ctl, false, wv);
  proj_task(F, pm, (last & 512) ? 48 + ((last >> 6) & 15) : 0, (last >> 2) & 7, last & 3, lds, wv);
}

#undef tid
__device__ __forceinline__ unsigned rtnow() { return (unsigned)__builtin_amdgcn_s_memrealtime(); }
__device__ __forceinline__ void wait_rt(unsigned t) {
  int rem = (int)(t - rtnow());
  if (rem > 20000) return;
  while (rem > 12) { __builtin_amdgcn_s_sleep(2); rem = (int)(t - rtnow()); }
  while (rem > 0) rem = (int)(t - rtnow());
}
struct Clk { unsigned f, c, t, p; };
template <int AL> __device__ __forceinline__ void wait_fine(unsigned t, Clk& k, int lat) {
  int rem = (int)(t - rtnow());
  if (rem > 20000) return;
  { const int e = (int)(k.c - k.p); if (e > -200 && e < 200) k.f += e >> 1; }
  k.p = k.c + (((t - k.t) * k.f) >> 8); k.t = t; k.c = k.p + 10;
  if (rem < 16) k.p += 1000;
  while (rem > 56) { __builtin_amdgcn_s_sleep(2); rem = (int)(t - rtnow()); }
  asm volatile(".p2align 6\n\t.rept %5\n\ts_nop 0\n\t.endr\n\t"
               "1:\n\t"
               "s_memrealtime vcc\n\t"
               "s_memtime s[98:99]\n\t"
               "s_waitcnt lgkmcnt(0)\n\t"
               "s_sub_i32 vcc_lo, %1, vcc_lo\n\t"
               "s_mul_i32 vcc_lo, vcc_lo, %2\n\t"
               "s_ashr_i32 vcc_lo, vcc_lo, 8\n\t"
               "s_add_i32 vcc_lo, vcc_lo, s98\n\t"
               "s_sub_i32 vcc_hi, vcc_lo, %0\n\t"
               "s_cmp_gt_u32 vcc_hi, %3\n\t"
               "s_cselect_b32 %0, vcc_lo, %0\n\t"
               "s_sub_i32 s98, %0, s98\n\t"
               "s_cmpk_gt_i32 s98, 230\n\t"
               "s_cbranch_scc1 1b\n\t"
               "s_sub_i32 s98, s98, %4\n\t"
               "s_max_i32 s98, s98, 0\n\t"
               "s_min_i32 s98, s98, 192\n\t"
               "s_and_b32 s98, s98, -4\n\t"
               "s_sub_i32 s98, 204, s98\n\t"
               "s_getpc_b64 vcc\n\t"
               "s_add_u32 vcc_lo, vcc_lo, s98\n\t"
               "s_addc_u32 vcc_hi, vcc_hi, 0\n\t"
               "s_setpc_b64 vcc\n\t"
               ".rept 48\n\ts_nop 0\n\t.endr" : "+s"(k.c) : "s"(t), "s"(k.f), "s"(k.f >> 6), "s"(lat), "i"(AL) : "scc", "vcc", "s98", "s99");
}
__device__ __forceinline__ bool bad4(v4u v) { return __builtin_amdgcn_ballot_w64(chk4(0u, v) == 0xffffffffu) != 0ull; }
__device__ __forceinline__ v4u repoll(__amdgpu_buffer_rsrc_t rs, int off, int soff, unsigned* tmo) {
  v4u v;
#pragma unroll 1
  for (unsigned fails = 0;; ++fails) {
    if (fails > 8) __builtin_amdgcn_s_sleep(4);
    v = __builtin_amdgcn_raw_buffer_load_b128(rs, off, soff, 16);
    if (!bad4(v) || spin_fail(fails, tmo, 1u)) break;
  }
  return v;
}
__device__ __forceinline__ int nearh(unsigned plh, int off) { return off + (int)((plh >> (off >> 21)) & 1u) * 8192; }
__device__ __forceinline__ int ownh(unsigned plh, unsigned psd, int off) { return nearh(plh, off) + (int)(((psd >> (off >> 21)) ^ (off >> 19)) & 1u) * 16384; }
#define SET_PLH do { const int o_ = sideo(plc, (lane & 31) << 21, xq != 1); plh = (unsigned)__ballot(o_ & 8192); psd = (unsigned)__ballot(o_ & 16384); asm volatile("" : "+s"(plh), "+s"(psd)); } while (0)
#define MBASE(s) ownh(plh, psd, MB0 + (s) * MREG)
#define VBASE(s) ownh(plh, psd, VB0 + (s) * OREG)
#define RBASE(s) (nearh(plh, RC0) + dbl((s) * 256))
constexpr int RSO_END = HBASE + (int)sizeof(Hdr);
constexpr int FBASE = CBASE + (int)offsetof(Ctl, prog);
#define OBASE(s) ownh(plh, psd, OB0 + (s) * OREG)

__global__ void __launch_bounds__(256, 1) k_fused(FParams P) {
  __shared__ __attribute__((aligned(16))) char Lraw[163840];
  static_assert(SW0 + NLW * 8192 <= 163840 - 256, "");
  unsigned long long pc0;
  asm volatile("s_getpc_b64 %0\nLfused_pc0:" : "=s"(pc0));
  { int clen; asm volatile("s_mov_b32 %0, Lfused_end-Lfused_pc0" : "=s"(clen));
    __builtin_amdgcn_raw_ptr_buffer_load_lds(__builtin_amdgcn_make_buffer_rsrc((void*)pc0, 0, clen, 0x00020000), (__attribute__((address_space(3))) void*)(Lraw + 163840 - 256), 4,
                                             (((blockIdx.x >> 3) & 7) * 256 + threadIdx.x) * 64, 0, 0, 0); }
  unsigned long long tok;
  const int wvn = __builtin_amdgcn_readfirstlane(threadIdx.x >> 6);
  if ((blockIdx.x & 7) >= 3) {
    worker(P, Lraw, wvn);
    return;
  }
  u16 *obuf = P.obuf; unsigned* tmo = &P.ctl->tmo[0]; unsigned long long* prof = P.prof; (void)prof;
  const int tid = threadIdx.x, lane = tid & 63, w = __builtin_amdgcn_readfirstlane(tid >> 6);
  const int xq = blockIdx.x & 7, slot = blockIdx.x >> 3, nq = xq < 2 ? 12 : 8;
  const int b = slot < nq ? 12 * xq + slot : NQK + 20 * xq + (slot - nq);
  const int l15 = lane & 15, lq = lane >> 4;
  const int idx = (lane & 2) ? 0xEEEE : 0x4444;
  asm volatile("" : "+s"(obuf));
  const __amdgpu_buffer_rsrc_t rs_o = __builtin_amdgcn_make_buffer_rsrc(obuf, 0, RSO_END, 0x00020000);
#define rs_m rs_o
#define rs_v rs_o
#define rs_k rs_o
  Plc plc; unsigned plh, psd;
  const int mem_lane_off = lane * 16;
  int mso[4];
#pragma unroll
  for (int k = 0; k < 4; ++k) mso[k] = quad(mpos(xq, 4 * k + w) * 1024);
  unsigned tA = 0;
  Clk clk = {23u * 256u, 0u, 0u, 1000u};
  if (b < NQK) {
    const int h = b;
    const unsigned lds0 = (unsigned)(size_t)Lraw;
    v32u A0, A1, A2, A3, A4, A5, A6, A7, V0, V1, V2, V3, V4;
    unsigned long long tk1, ep, hv, hf, h0;
    {
      const int wr_r = lane >> 2, wr_h2 = ((wr_r >> 1) & 1) | (((wr_r >> 3) & 1) << 1), rd_h2 = ((l15 >> 3) & 1) | ((lq & 1) << 1);
      v16u C, R;
#pragma unroll
      for (int u = 0; u < 4; ++u) { C[u] = ((16 * u + (lane >> 2)) * HDA + h * DHA + 32 * w + (lane & 3) * 8) * 2; C[6 + u] = lds0 + w * 8192 + (8 * lq + (l15 >> 2)) * 128 + (l15 & 3) * 8 + ((u ^ rd_h2) << 5); }
      C[4] = lds0 + w * 8192 + wr_r * 128 + (((2 * (w & 1) + ((lane & 3) >> 1)) ^ wr_h2) << 5) + (lane & 1) * 16; C[5] = C[4] ^ 64;
      C[10] = lds0 + SW0 + w * (NLW * 2048) + lane * 16; C[11] = lane * 8; C[12] = ((lane & 1) * 32 + h) * 8; C[13] = C[14] = C[15] = 0;
#pragma unroll
      for (int j = 0; j < 16; ++j) R[j] = mchunk(xq, 4 * ((j >> 1) & 3) + ((w ^ ((j >> 3) << 1)) & 2) + (j & 1)) * (64 * HDA * 2);
      asm volatile(
      "buffer_load_dwordx4 v[162:165], v226, s[40:43], s48 offen\n\t"
      "buffer_load_dwordx4 v[166:169], v227, s[40:43], s48 offen\n\t"
      "buffer_load_dwordx4 v[170:173], v228, s[40:43], s48 offen\n\t"
      "buffer_load_dwordx4 v[174:177], v229, s[40:43], s48 offen\n\t"
      "buffer_load_dwordx4 v[178:181], v226, s[40:43], s49 offen\n\t"
      "buffer_load_dwordx4 v[182:185], v227, s[40:43], s49 offen\n\t"
      "buffer_load_dwordx4 v[186:189], v228, s[40:43], s49 offen\n\t"
      "buffer_load_dwordx4 v[190:193], v229, s[40:43], s49 offen\n\t"
      "buffer_load_dwordx4 v[130:133], v226, s[44:47], s48 offen\n\t"
      "buffer_load_dwordx4 v[134:137], v227, s[44:47], s48 offen\n\t"
      "buffer_load_dwordx4 v[138:141], v228, s[44:47], s48 offen\n\t"
      "buffer_load_dwordx4 v[142:145], v229, s[44:47], s48 offen\n\t"
      "buffer_load_dwordx4 v[146:149], v226, s[44:47], s49 offen\n\t"
      "buffer_load_dwordx4 v[150:153], v227, s[44:47], s49 offen\n\t"
      "buffer_load_dwordx4 v[154:157], v228, s[44:47], s49 offen\n\t"
      "buffer_load_dwordx4 v[158:161], v229, s[44:47], s49 offen\n\t"
      "buffer_load_dwordx4 v[194:197], v226, s[40:43], s50 offen\n\t"
      "buffer_load_dwordx4 v[198:201], v227, s[40:43], s50 offen\n\t"
      "buffer_load_dwordx4 v[202:205], v228, s[40:43], s50 offen\n\t"
      "buffer_load_dwordx4 v[206:209], v229, s[40:43], s50 offen\n\t"
      "buffer_load_dwordx4 v[210:213], v226, s[40:43], s51 offen\n\t"
      "buffer_load_dwordx4 v[214:217], v227, s[40:43], s51 offen\n\t"
      "buffer_load_dwordx4 v[218:221], v228, s[40:43], s51 offen\n\t"
      "buffer_load_dwordx4 v[222:225], v229, s[40:43], s51 offen\n\t"
      "s_waitcnt vmcnt(23)\n\t"
      "ds_write_b128 v230, v[162:165] offset:0\n\t"
      "s_waitcnt vmcnt(22)\n\t"
      "ds_write_b128 v230, v[166:169] offset:2048\n\t"
      "s_waitcnt vmcnt(21)\n\t"
      "ds_write_b128 v230, v[170:173] offset:4096\n\t"
      "s_waitcnt vmcnt(20)\n\t"
      "ds_write_b128 v230, v[174:177] offset:6144\n\t"
      "s_waitcnt vmcnt(19)\n\t"
      "ds_write_b128 v231, v[178:181] offset:0\n\t"
      "s_waitcnt vmcnt(18)\n\t"
      "ds_write_b128 v231, v[182:185] offset:2048\n\t"
      "s_waitcnt vmcnt(17)\n\t"
      "ds_write_b128 v231, v[186:189] offset:4096\n\t"
      "s_waitcnt vmcnt(16)\n\t"
      "ds_write_b128 v231, v[190:193] offset:6144\n\t"
      "ds_read_b64_tr_b16 a[0:1], v232 offset:0\n\t"
      "ds_read_b64_tr_b16 a[2:3], v232 offset:512\n\t"
      "ds_read_b64_tr_b16 a[4:5], v232 offset:4096\n\t"
      "ds_read_b64_tr_b16 a[6:7], v232 offset:4608\n\t"
      "ds_read_b64_tr_b16 a[8:9], v233 offset:0\n\t"
      "ds_read_b64_tr_b16 a[10:11], v233 offset:512\n\t"
      "ds_read_b64_tr_b16 a[12:13], v233 offset:4096\n\t"
      "ds_read_b64_tr_b16 a[14:15], v233 offset:4608\n\t"
      "ds_read_b64_tr_b16 v[34:35], v234 offset:0\n\t"
      "ds_read_b64_tr_b16 v[36:37], v234 offset:512\n\t"
      "ds_read_b64_tr_b16 v[38:39], v234 offset:4096\n\t"
      "ds_read_b64_tr_b16 v[40:41], v234 offset:4608\n\t"
      "ds_read_b64_tr_b16 a[80:81], v235 offset:0\n\t"
      "ds_read_b64_tr_b16 a[82:83], v235 offset:512\n\t"
      "ds_read_b64_tr_b16 a[84:85], v235 offset:4096\n\t"
      "ds_read_b64_tr_b16 a[86:87], v235 offset:4608\n\t"
      "s_waitcnt lgkmcnt(0)\n\t"
      "buffer_load_dwordx4 v[162:165], v226, s[44:47], s50 offen\n\t"
      "buffer_load_dwordx4 v[166:169], v227, s[44:47], s50 offen\n\t"
      "buffer_load_dwordx4 v[170:173], v228, s[44:47], s50 offen\n\t"
      "buffer_load_dwordx4 v[174:177], v229, s[44:47], s50 offen\n\t"
      "buffer_load_dwordx4 v[178:181], v226, s[44:47], s51 offen\n\t"
      "buffer_load_dwordx4 v[182:185], v227, s[44:47], s51 offen\n\t"
      "buffer_load_dwordx4 v[186:189], v228, s[44:47], s51 offen\n\t"
      "buffer_load_dwordx4 v[190:193], v229, s[44:47], s51 offen\n\t"
      "s_waitcnt vmcnt(23)\n\t"
      "ds_write_b128 v230, v[130:133] offset:0\n\t"
      "s_waitcnt vmcnt(22)\n\t"
      "ds_write_b128 v230, v[134:137] offset:2048\n\t"
      "s_waitcnt vmcnt(21)\n\t"
      "ds_write_b128 v230, v[138:141] offset:4096\n\t"
      "s_waitcnt vmcnt(20)\n\t"
      "ds_write_b128 v230, v[142:145] offset:6144\n\t"
      "s_waitcnt vmcnt(19)\n\t"
      "ds_write_b128 v231, v[146:149] offset:0\n\t"
      "s_waitcnt vmcnt(18)\n\t"
      "ds_write_b128 v231, v[150:153] offset:2048\n\t"
      "s_waitcnt vmcnt(17)\n\t"
      "ds_write_b128 v231, v[154:157] offset:4096\n\t"
      "s_waitcnt vmcnt(16)\n\t"
      "ds_write_b128 v231, v[158:161] offset:6144\n\t"
      "ds_read_b64_tr_b16 v[2:3], v232 offset:0\n\t"
      "ds_read_b64_tr_b16 v[4:5], v232 offset:512\n\t"
      "ds_read_b64_tr_b16 v[6:7], v232 offset:4096\n\t"
      "ds_read_b64_tr_b16 v[8:9], v232 offset:4608\n\t"
      "ds_read_b64_tr_b16 a[16:17], v233 offset:0\n\t"
      "ds_read_b64_tr_b16 a[18:19], v233 offset:512\n\t"
      "ds_read_b64_tr_b16 a[20:21], v233 offset:4096\n\t"
      "ds_read_b64_tr_b16 a[22:23], v233 offset:4608\n\t"
      "ds_read_b64_tr_b16 a[88:89], v234 offset:0\n\t"
      "ds_read_b64_tr_b16 a[90:91], v234 offset:512\n\t"
      "ds_read_b64_tr_b16 a[92:93], v234 offset:4096\n\t"
      "ds_read_b64_tr_b16 a[94:95], v234 offset:4608\n\t"
      "ds_read_b64_tr_b16 v[154:155], v235 offset:0\n\t"
      "ds_read_b64_tr_b16 v[156:157], v235 offset:512\n\t"
      "ds_read_b64_tr_b16 v[158:159], v235 offset:4096\n\t"
      "ds_read_b64_tr_b16 v[160:161], v235 offset:4608\n\t"
      "s_waitcnt lgkmcnt(0)\n\t"
      "ds_write_b128 v236, v[154:157] offset:4096\n\t"
      "ds_write_b128 v236, v[158:161] offset:5120\n\t"
      "s_waitcnt lgkmcnt(0)\n\t"
      "buffer_load_dwordx4 v[130:133], v226, s[40:43], s52 offen\n\t"
      "buffer_load_dwordx4 v[134:137], v227, s[40:43], s52 offen\n\t"
      "buffer_load_dwordx4 v[138:141], v228, s[40:43], s52 offen\n\t"
      "buffer_load_dwordx4 v[142:145], v229, s[40:43], s52 offen\n\t"
      "buffer_load_dwordx4 v[146:149], v226, s[40:43], s53 offen\n\t"
      "buffer_load_dwordx4 v[150:153], v227, s[40:43], s53 offen\n\t"
      "buffer_load_dwordx4 v[154:157], v228, s[40:43], s53 offen\n\t"
      "buffer_load_dwordx4 v[158:161], v229, s[40:43], s53 offen\n\t"
      "s_waitcnt vmcnt(23)\n\t"
      "ds_write_b128 v230, v[194:197] offset:0\n\t"
      "s_waitcnt vmcnt(22)\n\t"
      "ds_write_b128 v230, v[198:201] offset:2048\n\t"
      "s_waitcnt vmcnt(21)\n\t"
      "ds_write_b128 v230, v[202:205] offset:4096\n\t"
      "s_waitcnt vmcnt(20)\n\t"
      "ds_write_b128 v230, v[206:209] offset:6144\n\t"
      "s_waitcnt vmcnt(19)\n\t"
      "ds_write_b128 v231, v[210:213] offset:0\n\t"
      "s_waitcnt vmcnt(18)\n\t"
      "ds_write_b128 v231, v[214:217] offset:2048\n\t"
      "s_waitcnt vmcnt(17)\n\t"
      "ds_write_b128 v231, v[218:221] offset:4096\n\t"
      "s_waitcnt vmcnt(16)\n\t"
      "ds_write_b128 v231, v[222:225] offset:6144\n\t"
      "ds_read_b64_tr_b16 a[24:25], v232 offset:0\n\t"
      "ds_read_b64_tr_b16 a[26:27], v232 offset:512\n\t"
      "ds_read_b64_tr_b16 a[28:29], v232 offset:4096\n\t"
      "ds_read_b64_tr_b16 a[30:31], v232 offset:4608\n\t"
      "ds_read_b64_tr_b16 v[10:11], v233 offset:0\n\t"
      "ds_read_b64_tr_b16 v[12:13], v233 offset:512\n\t"
      "ds_read_b64_tr_b16 v[14:15], v233 offset:4096\n\t"
      "ds_read_b64_tr_b16 v[16:17], v233 offset:4608\n\t"
      "ds_read_b64_tr_b16 v[210:211], v234 offset:0\n\t"
      "ds_read_b64_tr_b16 v[212:213], v234 offset:512\n\t"
      "ds_read_b64_tr_b16 v[214:215], v234 offset:4096\n\t"
      "ds_read_b64_tr_b16 v[216:217], v234 offset:4608\n\t"
      "ds_read_b64_tr_b16 a[128:129], v235 offset:0\n\t"
      "ds_read_b64_tr_b16 a[130:131], v235 offset:512\n\t"
      "ds_read_b64_tr_b16 a[132:133], v235 offset:4096\n\t"
      "ds_read_b64_tr_b16 a[134:135], v235 offset:4608\n\t"
      "s_waitcnt lgkmcnt(0)\n\t"
      "ds_write_b128 v236, v[210:213] offset:8192\n\t"
      "ds_write_b128 v236, v[214:217] offset:9216\n\t"
      "s_waitcnt lgkmcnt(0)\n\t"
      "buffer_load_dwordx4 v[194:197], v226, s[44:47], s52 offen\n\t"
      "buffer_load_dwordx4 v[198:201], v227, s[44:47], s52 offen\n\t"
      "buffer_load_dwordx4 v[202:205], v228, s[44:47], s52 offen\n\t"
      "buffer_load_dwordx4 v[206:209], v229, s[44:47], s52 offen\n\t"
      "buffer_load_dwordx4 v[210:213], v226, s[44:47], s53 offen\n\t"
      "buffer_load_dwordx4 v[214:217], v227, s[44:47], s53 offen\n\t"
      "buffer_load_dwordx4 v[218:221], v228, s[44:47], s53 offen\n\t"
      "buffer_load_dwordx4 v[222:225], v229, s[44:47], s53 offen\n\t"
      "s_waitcnt vmcnt(23)\n\t"
      "ds_write_b128 v230, v[162:165] offset:0\n\t"
      "s_waitcnt vmcnt(22)\n\t"
      "ds_write_b128 v230, v[166:169] offset:2048\n\t"
      "s_waitcnt vmcnt(21)\n\t"
      "ds_write_b128 v230, v[170:173] offset:4096\n\t"
      "s_waitcnt vmcnt(20)\n\t"
      "ds_write_b128 v230, v[174:177] offset:6144\n\t"
      "s_waitcnt vmcnt(19)\n\t"
      "ds_write_b128 v231, v[178:181] offset:0\n\t"
      "s_waitcnt vmcnt(18)\n\t"
      "ds_write_b128 v231, v[182:185] offset:2048\n\t"
      "s_waitcnt vmcnt(17)\n\t"
      "ds_write_b128 v231, v[186:189] offset:4096\n\t"
      "s_waitcnt vmcnt(16)\n\t"
      "ds_write_b128 v231, v[190:193] offset:6144\n\t"
      "ds_read_b64_tr_b16 a[32:33], v232 offset:0\n\t"
      "ds_read_b64_tr_b16 a[34:35], v232 offset:512\n\t"
      "ds_read_b64_tr_b16 a[36:37], v232 offset:4096\n\t"
      "ds_read_b64_tr_b16 a[38:39], v232 offset:4608\n\t"
      "ds_read_b64_tr_b16 a[40:41], v233 offset:0\n\t"
      "ds_read_b64_tr_b16 a[42:43], v233 offset:512\n\t"
      "ds_read_b64_tr_b16 a[44:45], v233 offset:4096\n\t"
      "ds_read_b64_tr_b16 a[46:47], v233 offset:4608\n\t"
      "ds_read_b64_tr_b16 a[136:137], v234 offset:0\n\t"
      "ds_read_b64_tr_b16 a[138:139], v234 offset:512\n\t"
      "ds_read_b64_tr_b16 a[140:141], v234 offset:4096\n\t"
      "ds_read_b64_tr_b16 a[142:143], v234 offset:4608\n\t"
      "ds_read_b64_tr_b16 v[66:67], v235 offset:0\n\t"
      "ds_read_b64_tr_b16 v[68:69], v235 offset:512\n\t"
      "ds_read_b64_tr_b16 v[70:71], v235 offset:4096\n\t"
      "ds_read_b64_tr_b16 v[72:73], v235 offset:4608\n\t"
      "s_waitcnt lgkmcnt(0)\n\t"
      "buffer_load_dwordx4 v[162:165], v226, s[40:43], s54 offen\n\t"
      "buffer_load_dwordx4 v[166:169], v227, s[40:43], s54 offen\n\t"
      "buffer_load_dwordx4 v[170:173], v228, s[40:43], s54 offen\n\t"
      "buffer_load_dwordx4 v[174:177], v229, s[40:43], s54 offen\n\t"
      "buffer_load_dwordx4 v[178:181], v226, s[40:43], s55 offen\n\t"
      "buffer_load_dwordx4 v[182:185], v227, s[40:43], s55 offen\n\t"
      "buffer_load_dwordx4 v[186:189], v228, s[40:43], s55 offen\n\t"
      "buffer_load_dwordx4 v[190:193], v229, s[40:43], s55 offen\n\t"
      "s_waitcnt vmcnt(23)\n\t"
      "ds_write_b128 v230, v[130:133] offset:0\n\t"
      "s_waitcnt vmcnt(22)\n\t"
      "ds_write_b128 v230, v[134:137] offset:2048\n\t"
      "s_waitcnt vmcnt(21)\n\t"
      "ds_write_b128 v230, v[138:141] offset:4096\n\t"
      "s_waitcnt vmcnt(20)\n\t"
      "ds_write_b128 v230, v[142:145] offset:6144\n\t"
      "s_waitcnt vmcnt(19)\n\t"
      "ds_write_b128 v231, v[146:149] offset:0\n\t"
      "s_waitcnt vmcnt(18)\n\t"
      "ds_write_b128 v231, v[150:153] offset:2048\n\t"
      "s_waitcnt vmcnt(17)\n\t"
      "ds_write_b128 v231, v[154:157] offset:4096\n\t"
      "s_waitcnt vmcnt(16)\n\t"
      "ds_write_b128 v231, v[158:161] offset:6144\n\t"
      "ds_read_b64_tr_b16 v[18:19], v232 offset:0\n\t"
      "ds_read_b64_tr_b16 v[20:21], v232 offset:512\n\t"
      "ds_read_b64_tr_b16 v[22:23], v232 offset:4096\n\t"
      "ds_read_b64_tr_b16 v[24:25], v232 offset:4608\n\t"
      "ds_read_b64_tr_b16 a[48:49], v233 offset:0\n\t"
      "ds_read_b64_tr_b16 a[50:51], v233 offset:512\n\t"
      "ds_read_b64_tr_b16 a[52:53], v233 offset:4096\n\t"
      "ds_read_b64_tr_b16 a[54:55], v233 offset:4608\n\t"
      "ds_read_b64_tr_b16 v[146:147], v234 offset:0\n\t"
      "ds_read_b64_tr_b16 v[148:149], v234 offset:512\n\t"
      "ds_read_b64_tr_b16 v[150:151], v234 offset:4096\n\t"
      "ds_read_b64_tr_b16 v[152:153], v234 offset:4608\n\t"
      "ds_read_b64_tr_b16 a[160:161], v235 offset:0\n\t"
      "ds_read_b64_tr_b16 a[162:163], v235 offset:512\n\t"
      "ds_read_b64_tr_b16 a[164:165], v235 offset:4096\n\t"
      "ds_read_b64_tr_b16 a[166:167], v235 offset:4608\n\t"
      "s_waitcnt lgkmcnt(0)\n\t"
      "ds_write_b128 v236, v[146:149] offset:12288\n\t"
      "ds_write_b128 v236, v[150:153] offset:13312\n\t"
      "s_waitcnt lgkmcnt(0)\n\t"
      "buffer_load_dwordx4 v[130:133], v226, s[44:47], s54 offen\n\t"
      "buffer_load_dwordx4 v[134:137], v227, s[44:47], s54 offen\n\t"
      "buffer_load_dwordx4 v[138:141], v228, s[44:47], s54 offen\n\t"
      "buffer_load_dwordx4 v[142:145], v229, s[44:47], s54 offen\n\t"
      "buffer_load_dwordx4 v[146:149], v226, s[44:47], s55 offen\n\t"
      "buffer_load_dwordx4 v[150:153], v227, s[44:47], s55 offen\n\t"
      "buffer_load_dwordx4 v[154:157], v228, s[44:47], s55 offen\n\t"
      "buffer_load_dwordx4 v[158:161], v229, s[44:47], s55 offen\n\t"
      "s_waitcnt vmcnt(23)\n\t"
      "ds_write_b128 v230, v[194:197] offset:0\n\t"
      "s_waitcnt vmcnt(22)\n\t"
      "ds_write_b128 v230, v[198:201] offset:2048\n\t"
      "s_waitcnt vmcnt(21)\n\t"
      "ds_write_b128 v230, v[202:205] offset:4096\n\t"
      "s_waitcnt vmcnt(20)\n\t"
      "ds_write_b128 v230, v[206:209] offset:6144\n\t"
      "s_waitcnt vmcnt(19)\n\t"
      "ds_write_b128 v231, v[210:213] offset:0\n\t"
      "s_waitcnt vmcnt(18)\n\t"
      "ds_write_b128 v231, v[214:217] offset:2048\n\t"
      "s_waitcnt vmcnt(17)\n\t"
      "ds_write_b128 v231, v[218:221] offset:4096\n\t"
      "s_waitcnt vmcnt(16)\n\t"
      "ds_write_b128 v231, v[222:225] offset:6144\n\t"
      "ds_read_b64_tr_b16 a[56:57], v232 offset:0\n\t"
      "ds_read_b64_tr_b16 a[58:59], v232 offset:512\n\t"
      "ds_read_b64_tr_b16 a[60:61], v232 offset:4096\n\t"
      "ds_read_b64_tr_b16 a[62:63], v232 offset:4608\n\t"
      "ds_read_b64_tr_b16 v[202:203], v233 offset:0\n\t"
      "ds_read_b64_tr_b16 v[204:205], v233 offset:512\n\t"
      "ds_read_b64_tr_b16 v[206:207], v233 offset:4096\n\t"
      "ds_read_b64_tr_b16 v[208:209], v233 offset:4608\n\t"
      "ds_read_b64_tr_b16 a[208:209], v234 offset:0\n\t"
      "ds_read_b64_tr_b16 a[210:211], v234 offset:512\n\t"
      "ds_read_b64_tr_b16 a[212:213], v234 offset:4096\n\t"
      "ds_read_b64_tr_b16 a[214:215], v234 offset:4608\n\t"
      "ds_read_b64_tr_b16 a[216:217], v235 offset:0\n\t"
      "ds_read_b64_tr_b16 a[218:219], v235 offset:512\n\t"
      "ds_read_b64_tr_b16 a[220:221], v235 offset:4096\n\t"
      "ds_read_b64_tr_b16 a[222:223], v235 offset:4608\n\t"
      "s_waitcnt lgkmcnt(0)\n\t"
      "ds_write_b128 v236, v[202:205] offset:0\n\t"
      "ds_write_b128 v236, v[206:209] offset:1024\n\t"
      "s_waitcnt lgkmcnt(0)\n\t"
      "buffer_load_dwordx4 v[194:197], v226, s[40:43], s56 offen\n\t"
      "buffer_load_dwordx4 v[198:201], v227, s[40:43], s56 offen\n\t"
      "buffer_load_dwordx4 v[202:205], v228, s[40:43], s56 offen\n\t"
      "buffer_load_dwordx4 v[206:209], v229, s[40:43], s56 offen\n\t"
      "buffer_load_dwordx4 v[210:213], v226, s[40:43], s57 offen\n\t"
      "buffer_load_dwordx4 v[214:217], v227, s[40:43], s57 offen\n\t"
      "buffer_load_dwordx4 v[218:221], v228, s[40:43], s57 offen\n\t"
      "buffer_load_dwordx4 v[222:225], v229, s[40:43], s57 offen\n\t"
      "s_waitcnt vmcnt(23)\n\t"
      "ds_write_b128 v230, v[162:165] offset:0\n\t"
      "s_waitcnt vmcnt(22)\n\t"
      "ds_write_b128 v230, v[166:169] offset:2048\n\t"
      "s_waitcnt vmcnt(21)\n\t"
      "ds_write_b128 v230, v[170:173] offset:4096\n\t"
      "s_waitcnt vmcnt(20)\n\t"
      "ds_write_b128 v230, v[174:177] offset:6144\n\t"
      "s_waitcnt vmcnt(19)\n\t"
      "ds_write_b128 v231, v[178:181] offset:0\n\t"
      "s_waitcnt vmcnt(18)\n\t"
      "ds_write_b128 v231, v[182:185] offset:2048\n\t"
      "s_waitcnt vmcnt(17)\n\t"
      "ds_write_b128 v231, v[186:189] offset:4096\n\t"
      "s_waitcnt vmcnt(16)\n\t"
      "ds_write_b128 v231, v[190:193] offset:6144\n\t"
      "ds_read_b64_tr_b16 v[26:27], v232 offset:0\n\t"
      "ds_read_b64_tr_b16 v[28:29], v232 offset:512\n\t"
      "ds_read_b64_tr_b16 v[30:31], v232 offset:4096\n\t"
      "ds_read_b64_tr_b16 v[32:33], v232 offset:4608\n\t"
      "ds_read_b64_tr_b16 a[64:65], v233 offset:0\n\t"
      "ds_read_b64_tr_b16 a[66:67], v233 offset:512\n\t"
      "ds_read_b64_tr_b16 a[68:69], v233 offset:4096\n\t"
      "ds_read_b64_tr_b16 a[70:71], v233 offset:4608\n\t"
      "ds_read_b64_tr_b16 a[184:185], v234 offset:0\n\t"
      "ds_read_b64_tr_b16 a[186:187], v234 offset:512\n\t"
      "ds_read_b64_tr_b16 a[188:189], v234 offset:4096\n\t"
      "ds_read_b64_tr_b16 a[190:191], v234 offset:4608\n\t"
      "ds_read_b64_tr_b16 v[90:91], v235 offset:0\n\t"
      "ds_read_b64_tr_b16 v[92:93], v235 offset:512\n\t"
      "ds_read_b64_tr_b16 v[94:95], v235 offset:4096\n\t"
      "ds_read_b64_tr_b16 v[96:97], v235 offset:4608\n\t"
      "s_waitcnt lgkmcnt(0)\n\t"
      "buffer_load_dwordx4 v[162:165], v226, s[44:47], s56 offen\n\t"
      "buffer_load_dwordx4 v[166:169], v227, s[44:47], s56 offen\n\t"
      "buffer_load_dwordx4 v[170:173], v228, s[44:47], s56 offen\n\t"
      "buffer_load_dwordx4 v[174:177], v229, s[44:47], s56 offen\n\t"
      "buffer_load_dwordx4 v[178:181], v226, s[44:47], s57 offen\n\t"
      "buffer_load_dwordx4 v[182:185], v227, s[44:47], s57 offen\n\t"
      "buffer_load_dwordx4 v[186:189], v228, s[44:47], s57 offen\n\t"
      "buffer_load_dwordx4 v[190:193], v229, s[44:47], s57 offen\n\t"
      "s_waitcnt vmcnt(23)\n\t"
      "ds_write_b128 v230, v[130:133] offset:0\n\t"
      "s_waitcnt vmcnt(22)\n\t"
      "ds_write_b128 v230, v[134:137] offset:2048\n\t"
      "s_waitcnt vmcnt(21)\n\t"
      "ds_write_b128 v230, v[138:141] offset:4096\n\t"
      "s_waitcnt vmcnt(20)\n\t"
      "ds_write_b128 v230, v[142:145] offset:6144\n\t"
      "s_waitcnt vmcnt(19)\n\t"
      "ds_write_b128 v231, v[146:149] offset:0\n\t"
      "s_waitcnt vmcnt(18)\n\t"
      "ds_write_b128 v231, v[150:153] offset:2048\n\t"
      "s_waitcnt vmcnt(17)\n\t"
      "ds_write_b128 v231, v[154:157] offset:4096\n\t"
      "s_waitcnt vmcnt(16)\n\t"
      "ds_write_b128 v231, v[158:161] offset:6144\n\t"
      "ds_read_b64_tr_b16 a[72:73], v232 offset:0\n\t"
      "ds_read_b64_tr_b16 a[74:75], v232 offset:512\n\t"
      "ds_read_b64_tr_b16 a[76:77], v232 offset:4096\n\t"
      "ds_read_b64_tr_b16 a[78:79], v232 offset:4608\n\t"
      "ds_read_b64_tr_b16 v[138:139], v233 offset:0\n\t"
      "ds_read_b64_tr_b16 v[140:141], v233 offset:512\n\t"
      "ds_read_b64_tr_b16 v[142:143], v233 offset:4096\n\t"
      "ds_read_b64_tr_b16 v[144:145], v233 offset:4608\n\t"
      "ds_read_b64_tr_b16 v[114:115], v234 offset:0\n\t"
      "ds_read_b64_tr_b16 v[116:117], v234 offset:512\n\t"
      "ds_read_b64_tr_b16 v[118:119], v234 offset:4096\n\t"
      "ds_read_b64_tr_b16 v[120:121], v234 offset:4608\n\t"
      "ds_read_b64_tr_b16 a[240:241], v235 offset:0\n\t"
      "ds_read_b64_tr_b16 a[242:243], v235 offset:512\n\t"
      "ds_read_b64_tr_b16 a[244:245], v235 offset:4096\n\t"
      "ds_read_b64_tr_b16 a[246:247], v235 offset:4608\n\t"
      "s_waitcnt lgkmcnt(0)\n\t"
      "ds_write_b128 v236, v[138:141] offset:2048\n\t"
      "ds_write_b128 v236, v[142:145] offset:3072\n\t"
      "s_waitcnt lgkmcnt(0)\n\t"
      "buffer_load_dwordx4 v[130:133], v226, s[40:43], s58 offen\n\t"
      "buffer_load_dwordx4 v[134:137], v227, s[40:43], s58 offen\n\t"
      "buffer_load_dwordx4 v[138:141], v228, s[40:43], s58 offen\n\t"
      "buffer_load_dwordx4 v[142:145], v229, s[40:43], s58 offen\n\t"
      "buffer_load_dwordx4 v[146:149], v226, s[40:43], s59 offen\n\t"
      "buffer_load_dwordx4 v[150:153], v227, s[40:43], s59 offen\n\t"
      "buffer_load_dwordx4 v[154:157], v228, s[40:43], s59 offen\n\t"
      "buffer_load_dwordx4 v[158:161], v229, s[40:43], s59 offen\n\t"
      "s_waitcnt vmcnt(23)\n\t"
      "ds_write_b128 v230, v[194:197] offset:0\n\t"
      "s_waitcnt vmcnt(22)\n\t"
      "ds_write_b128 v230, v[198:201] offset:2048\n\t"
      "s_waitcnt vmcnt(21)\n\t"
      "ds_write_b128 v230, v[202:205] offset:4096\n\t"
      "s_waitcnt vmcnt(20)\n\t"
      "ds_write_b128 v230, v[206:209] offset:6144\n\t"
      "s_waitcnt vmcnt(19)\n\t"
      "ds_write_b128 v231, v[210:213] offset:0\n\t"
      "s_waitcnt vmcnt(18)\n\t"
      "ds_write_b128 v231, v[214:217] offset:2048\n\t"
      "s_waitcnt vmcnt(17)\n\t"
      "ds_write_b128 v231, v[218:221] offset:4096\n\t"
      "s_waitcnt vmcnt(16)\n\t"
      "ds_write_b128 v231, v[222:225] offset:6144\n\t"
      "ds_read_b64_tr_b16 v[42:43], v232 offset:0\n\t"
      "ds_read_b64_tr_b16 v[44:45], v232 offset:512\n\t"
      "ds_read_b64_tr_b16 v[46:47], v232 offset:4096\n\t"
      "ds_read_b64_tr_b16 v[48:49], v232 offset:4608\n\t"
      "ds_read_b64_tr_b16 a[96:97], v233 offset:0\n\t"
      "ds_read_b64_tr_b16 a[98:99], v233 offset:512\n\t"
      "ds_read_b64_tr_b16 a[100:101], v233 offset:4096\n\t"
      "ds_read_b64_tr_b16 a[102:103], v233 offset:4608\n\t"
      "ds_read_b64_tr_b16 v[210:211], v234 offset:0\n\t"
      "ds_read_b64_tr_b16 v[212:213], v234 offset:512\n\t"
      "ds_read_b64_tr_b16 v[214:215], v234 offset:4096\n\t"
      "ds_read_b64_tr_b16 v[216:217], v234 offset:4608\n\t"
      "ds_read_b64_tr_b16 a[112:113], v235 offset:0\n\t"
      "ds_read_b64_tr_b16 a[114:115], v235 offset:512\n\t"
      "ds_read_b64_tr_b16 a[116:117], v235 offset:4096\n\t"
      "ds_read_b64_tr_b16 a[118:119], v235 offset:4608\n\t"
      "s_waitcnt lgkmcnt(0)\n\t"
      "ds_write_b128 v236, v[210:213] offset:6144\n\t"
      "ds_write_b128 v236, v[214:217] offset:7168\n\t"
      "s_waitcnt lgkmcnt(0)\n\t"
      "buffer_load_dwordx4 v[194:197], v226, s[44:47], s58 offen\n\t"
      "buffer_load_dwordx4 v[198:201], v227, s[44:47], s58 offen\n\t"
      "buffer_load_dwordx4 v[202:205], v228, s[44:47], s58 offen\n\t"
      "buffer_load_dwordx4 v[206:209], v229, s[44:47], s58 offen\n\t"
      "buffer_load_dwordx4 v[210:213], v226, s[44:47], s59 offen\n\t"
      "buffer_load_dwordx4 v[214:217], v227, s[44:47], s59 offen\n\t"
      "buffer_load_dwordx4 v[218:221], v228, s[44:47], s59 offen\n\t"
      "buffer_load_dwordx4 v[222:225], v229, s[44:47], s59 offen\n\t"
      "s_waitcnt vmcnt(23)\n\t"
      "ds_write_b128 v230, v[162:165] offset:0\n\t"
      "s_waitcnt vmcnt(22)\n\t"
      "ds_write_b128 v230, v[166:169] offset:2048\n\t"
      "s_waitcnt vmcnt(21)\n\t"
      "ds_write_b128 v230, v[170:173] offset:4096\n\t"
      "s_waitcnt vmcnt(20)\n\t"
      "ds_write_b128 v230, v[174:177] offset:6144\n\t"
      "s_waitcnt vmcnt(19)\n\t"
      "ds_write_b128 v231, v[178:181] offset:0\n\t"
      "s_waitcnt vmcnt(18)\n\t"
      "ds_write_b128 v231, v[182:185] offset:2048\n\t"
      "s_waitcnt vmcnt(17)\n\t"
      "ds_write_b128 v231, v[186:189] offset:4096\n\t"
      "s_waitcnt vmcnt(16)\n\t"
      "ds_write_b128 v231, v[190:193] offset:6144\n\t"
      "ds_read_b64_tr_b16 a[104:105], v232 offset:0\n\t"
      "ds_read_b64_tr_b16 a[106:107], v232 offset:512\n\t"
      "ds_read_b64_tr_b16 a[108:109], v232 offset:4096\n\t"
      "ds_read_b64_tr_b16 a[110:111], v232 offset:4608\n\t"
      "ds_read_b64_tr_b16 v[50:51], v233 offset:0\n\t"
      "ds_read_b64_tr_b16 v[52:53], v233 offset:512\n\t"
      "ds_read_b64_tr_b16 v[54:55], v233 offset:4096\n\t"
      "ds_read_b64_tr_b16 v[56:57], v233 offset:4608\n\t"
      "ds_read_b64_tr_b16 a[120:121], v234 offset:0\n\t"
      "ds_read_b64_tr_b16 a[122:123], v234 offset:512\n\t"
      "ds_read_b64_tr_b16 a[124:125], v234 offset:4096\n\t"
      "ds_read_b64_tr_b16 a[126:127], v234 offset:4608\n\t"
      "ds_read_b64_tr_b16 v[58:59], v235 offset:0\n\t"
      "ds_read_b64_tr_b16 v[60:61], v235 offset:512\n\t"
      "ds_read_b64_tr_b16 v[62:63], v235 offset:4096\n\t"
      "ds_read_b64_tr_b16 v[64:65], v235 offset:4608\n\t"
      "s_waitcnt lgkmcnt(0)\n\t"
      "buffer_load_dwordx4 v[162:165], v226, s[40:43], s60 offen\n\t"
      "buffer_load_dwordx4 v[166:169], v227, s[40:43], s60 offen\n\t"
      "buffer_load_dwordx4 v[170:173], v228, s[40:43], s60 offen\n\t"
      "buffer_load_dwordx4 v[174:177], v229, s[40:43], s60 offen\n\t"
      "buffer_load_dwordx4 v[178:181], v226, s[40:43], s61 offen\n\t"
      "buffer_load_dwordx4 v[182:185], v227, s[40:43], s61 offen\n\t"
      "buffer_load_dwordx4 v[186:189], v228, s[40:43], s61 offen\n\t"
      "buffer_load_dwordx4 v[190:193], v229, s[40:43], s61 offen\n\t"
      "s_waitcnt vmcnt(23)\n\t"
      "ds_write_b128 v230, v[130:133] offset:0\n\t"
      "s_waitcnt vmcnt(22)\n\t"
      "ds_write_b128 v230, v[134:137] offset:2048\n\t"
      "s_waitcnt vmcnt(21)\n\t"
      "ds_write_b128 v230, v[138:141] offset:4096\n\t"
      "s_waitcnt vmcnt(20)\n\t"
      "ds_write_b128 v230, v[142:145] offset:6144\n\t"
      "s_waitcnt vmcnt(19)\n\t"
      "ds_write_b128 v231, v[146:149] offset:0\n\t"
      "s_waitcnt vmcnt(18)\n\t"
      "ds_write_b128 v231, v[150:153] offset:2048\n\t"
      "s_waitcnt vmcnt(17)\n\t"
      "ds_write_b128 v231, v[154:157] offset:4096\n\t"
      "s_waitcnt vmcnt(16)\n\t"
      "ds_write_b128 v231, v[158:161] offset:6144\n\t"
      "ds_read_b64_tr_b16 v[130:131], v232 offset:0\n\t"
      "ds_read_b64_tr_b16 v[132:133], v232 offset:512\n\t"
      "ds_read_b64_tr_b16 v[134:135], v232 offset:4096\n\t"
      "ds_read_b64_tr_b16 v[136:137], v232 offset:4608\n\t"
      "ds_read_b64_tr_b16 a[144:145], v233 offset:0\n\t"
      "ds_read_b64_tr_b16 a[146:147], v233 offset:512\n\t"
      "ds_read_b64_tr_b16 a[148:149], v233 offset:4096\n\t"
      "ds_read_b64_tr_b16 a[150:151], v233 offset:4608\n\t"
      "ds_read_b64_tr_b16 a[152:153], v234 offset:0\n\t"
      "ds_read_b64_tr_b16 a[154:155], v234 offset:512\n\t"
      "ds_read_b64_tr_b16 a[156:157], v234 offset:4096\n\t"
      "ds_read_b64_tr_b16 a[158:159], v234 offset:4608\n\t"
      "ds_read_b64_tr_b16 v[74:75], v235 offset:0\n\t"
      "ds_read_b64_tr_b16 v[76:77], v235 offset:512\n\t"
      "ds_read_b64_tr_b16 v[78:79], v235 offset:4096\n\t"
      "ds_read_b64_tr_b16 v[80:81], v235 offset:4608\n\t"
      "s_waitcnt lgkmcnt(0)\n\t"
      "ds_write_b128 v236, v[130:133] offset:10240\n\t"
      "ds_write_b128 v236, v[134:137] offset:11264\n\t"
      "s_waitcnt lgkmcnt(0)\n\t"
      "buffer_load_dwordx4 v[130:133], v226, s[44:47], s60 offen\n\t"
      "buffer_load_dwordx4 v[134:137], v227, s[44:47], s60 offen\n\t"
      "buffer_load_dwordx4 v[138:141], v228, s[44:47], s60 offen\n\t"
      "buffer_load_dwordx4 v[142:145], v229, s[44:47], s60 offen\n\t"
      "buffer_load_dwordx4 v[146:149], v226, s[44:47], s61 offen\n\t"
      "buffer_load_dwordx4 v[150:153], v227, s[44:47], s61 offen\n\t"
      "buffer_load_dwordx4 v[154:157], v228, s[44:47], s61 offen\n\t"
      "buffer_load_dwordx4 v[158:161], v229, s[44:47], s61 offen\n\t"
      "s_waitcnt vmcnt(23)\n\t"
      "ds_write_b128 v230, v[194:197] offset:0\n\t"
      "s_waitcnt vmcnt(22)\n\t"
      "ds_write_b128 v230, v[198:201] offset:2048\n\t"
      "s_waitcnt vmcnt(21)\n\t"
      "ds_write_b128 v230, v[202:205] offset:4096\n\t"
      "s_waitcnt vmcnt(20)\n\t"
      "ds_write_b128 v230, v[206:209] offset:6144\n\t"
      "s_waitcnt vmcnt(19)\n\t"
      "ds_write_b128 v231, v[210:213] offset:0\n\t"
      "s_waitcnt vmcnt(18)\n\t"
      "ds_write_b128 v231, v[214:217] offset:2048\n\t"
      "s_waitcnt vmcnt(17)\n\t"
      "ds_write_b128 v231, v[218:221] offset:4096\n\t"
      "s_waitcnt vmcnt(16)\n\t"
      "ds_write_b128 v231, v[222:225] offset:6144\n\t"
      "ds_read_b64_tr_b16 a[192:193], v232 offset:0\n\t"
      "ds_read_b64_tr_b16 a[194:195], v232 offset:512\n\t"
      "ds_read_b64_tr_b16 a[196:197], v232 offset:4096\n\t"
      "ds_read_b64_tr_b16 a[198:199], v232 offset:4608\n\t"
      "ds_read_b64_tr_b16 v[202:203], v233 offset:0\n\t"
      "ds_read_b64_tr_b16 v[204:205], v233 offset:512\n\t"
      "ds_read_b64_tr_b16 v[206:207], v233 offset:4096\n\t"
      "ds_read_b64_tr_b16 v[208:209], v233 offset:4608\n\t"
      "ds_read_b64_tr_b16 a[200:201], v234 offset:0\n\t"
      "ds_read_b64_tr_b16 a[202:203], v234 offset:512\n\t"
      "ds_read_b64_tr_b16 a[204:205], v234 offset:4096\n\t"
      "ds_read_b64_tr_b16 a[206:207], v234 offset:4608\n\t"
      "ds_read_b64_tr_b16 v[98:99], v235 offset:0\n\t"
      "ds_read_b64_tr_b16 v[100:101], v235 offset:512\n\t"
      "ds_read_b64_tr_b16 v[102:103], v235 offset:4096\n\t"
      "ds_read_b64_tr_b16 v[104:105], v235 offset:4608\n\t"
      "s_waitcnt lgkmcnt(0)\n\t"
      "ds_write_b128 v236, v[202:205] offset:18432\n\t"
      "ds_write_b128 v236, v[206:209] offset:19456\n\t"
      "s_waitcnt lgkmcnt(0)\n\t"
      "buffer_load_dwordx4 v[194:197], v226, s[40:43], s62 offen\n\t"
      "buffer_load_dwordx4 v[198:201], v227, s[40:43], s62 offen\n\t"
      "buffer_load_dwordx4 v[202:205], v228, s[40:43], s62 offen\n\t"
      "buffer_load_dwordx4 v[206:209], v229, s[40:43], s62 offen\n\t"
      "buffer_load_dwordx4 v[210:213], v226, s[40:43], s63 offen\n\t"
      "buffer_load_dwordx4 v[214:217], v227, s[40:43], s63 offen\n\t"
      "buffer_load_dwordx4 v[218:221], v228, s[40:43], s63 offen\n\t"
      "buffer_load_dwordx4 v[222:225], v229, s[40:43], s63 offen\n\t"
      "s_waitcnt vmcnt(23)\n\t"
      "ds_write_b128 v230, v[162:165] offset:0\n\t"
      "s_waitcnt vmcnt(22)\n\t"
      "ds_write_b128 v230, v[166:169] offset:2048\n\t"
      "s_waitcnt vmcnt(21)\n\t"
      "ds_write_b128 v230, v[170:173] offset:4096\n\t"
      "s_waitcnt vmcnt(20)\n\t"
      "ds_write_b128 v230, v[174:177] offset:6144\n\t"
      "s_waitcnt vmcnt(19)\n\t"
      "ds_write_b128 v231, v[178:181] offset:0\n\t"
      "s_waitcnt vmcnt(18)\n\t"
      "ds_write_b128 v231, v[182:185] offset:2048\n\t"
      "s_waitcnt vmcnt(17)\n\t"
      "ds_write_b128 v231, v[186:189] offset:4096\n\t"
      "s_waitcnt vmcnt(16)\n\t"
      "ds_write_b128 v231, v[190:193] offset:6144\n\t"
      "ds_read_b64_tr_b16 a[168:169], v232 offset:0\n\t"
      "ds_read_b64_tr_b16 a[170:171], v232 offset:512\n\t"
      "ds_read_b64_tr_b16 a[172:173], v232 offset:4096\n\t"
      "ds_read_b64_tr_b16 a[174:175], v232 offset:4608\n\t"
      "ds_read_b64_tr_b16 v[82:83], v233 offset:0\n\t"
      "ds_read_b64_tr_b16 v[84:85], v233 offset:512\n\t"
      "ds_read_b64_tr_b16 v[86:87], v233 offset:4096\n\t"
      "ds_read_b64_tr_b16 v[88:89], v233 offset:4608\n\t"
      "ds_read_b64_tr_b16 v[178:179], v234 offset:0\n\t"
      "ds_read_b64_tr_b16 v[180:181], v234 offset:512\n\t"
      "ds_read_b64_tr_b16 v[182:183], v234 offset:4096\n\t"
      "ds_read_b64_tr_b16 v[184:185], v234 offset:4608\n\t"
      "ds_read_b64_tr_b16 a[176:177], v235 offset:0\n\t"
      "ds_read_b64_tr_b16 a[178:179], v235 offset:512\n\t"
      "ds_read_b64_tr_b16 a[180:181], v235 offset:4096\n\t"
      "ds_read_b64_tr_b16 a[182:183], v235 offset:4608\n\t"
      "s_waitcnt lgkmcnt(0)\n\t"
      "ds_write_b128 v236, v[178:181] offset:14336\n\t"
      "ds_write_b128 v236, v[182:185] offset:15360\n\t"
      "s_waitcnt lgkmcnt(0)\n\t"
      "buffer_load_dwordx4 v[162:165], v226, s[44:47], s62 offen\n\t"
      "buffer_load_dwordx4 v[166:169], v227, s[44:47], s62 offen\n\t"
      "buffer_load_dwordx4 v[170:173], v228, s[44:47], s62 offen\n\t"
      "buffer_load_dwordx4 v[174:177], v229, s[44:47], s62 offen\n\t"
      "buffer_load_dwordx4 v[178:181], v226, s[44:47], s63 offen\n\t"
      "buffer_load_dwordx4 v[182:185], v227, s[44:47], s63 offen\n\t"
      "buffer_load_dwordx4 v[186:189], v228, s[44:47], s63 offen\n\t"
      "buffer_load_dwordx4 v[190:193], v229, s[44:47], s63 offen\n\t"
      "s_waitcnt vmcnt(23)\n\t"
      "ds_write_b128 v230, v[130:133] offset:0\n\t"
      "s_waitcnt vmcnt(22)\n\t"
      "ds_write_b128 v230, v[134:137] offset:2048\n\t"
      "s_waitcnt vmcnt(21)\n\t"
      "ds_write_b128 v230, v[138:141] offset:4096\n\t"
      "s_waitcnt vmcnt(20)\n\t"
      "ds_write_b128 v230, v[142:145] offset:6144\n\t"
      "s_waitcnt vmcnt(19)\n\t"
      "ds_write_b128 v231, v[146:149] offset:0\n\t"
      "s_waitcnt vmcnt(18)\n\t"
      "ds_write_b128 v231, v[150:153] offset:2048\n\t"
      "s_waitcnt vmcnt(17)\n\t"
      "ds_write_b128 v231, v[154:157] offset:4096\n\t"
      "s_waitcnt vmcnt(16)\n\t"
      "ds_write_b128 v231, v[158:161] offset:6144\n\t"
      "ds_read_b64_tr_b16 v[130:131], v232 offset:0\n\t"
      "ds_read_b64_tr_b16 v[132:133], v232 offset:512\n\t"
      "ds_read_b64_tr_b16 v[134:135], v232 offset:4096\n\t"
      "ds_read_b64_tr_b16 v[136:137], v232 offset:4608\n\t"
      "ds_read_b64_tr_b16 v[106:107], v233 offset:0\n\t"
      "ds_read_b64_tr_b16 v[108:109], v233 offset:512\n\t"
      "ds_read_b64_tr_b16 v[110:111], v233 offset:4096\n\t"
      "ds_read_b64_tr_b16 v[112:113], v233 offset:4608\n\t"
      "ds_read_b64_tr_b16 a[224:225], v234 offset:0\n\t"
      "ds_read_b64_tr_b16 a[226:227], v234 offset:512\n\t"
      "ds_read_b64_tr_b16 a[228:229], v234 offset:4096\n\t"
      "ds_read_b64_tr_b16 a[230:231], v234 offset:4608\n\t"
      "ds_read_b64_tr_b16 a[232:233], v235 offset:0\n\t"
      "ds_read_b64_tr_b16 a[234:235], v235 offset:512\n\t"
      "ds_read_b64_tr_b16 a[236:237], v235 offset:4096\n\t"
      "ds_read_b64_tr_b16 a[238:239], v235 offset:4608\n\t"
      "s_waitcnt lgkmcnt(0)\n\t"
      "ds_write_b128 v236, v[130:133] offset:20480\n\t"
      "ds_write_b128 v236, v[134:137] offset:21504\n\t"
      "s_waitcnt lgkmcnt(0)\n\t"
      "buffer_load_dwordx2 v[242:243], off, s[36:39], s64 sc1\n\t"
      "buffer_load_dwordx2 v[244:245], off, s[36:39], s64 offset:128 sc1\n\t"
      "buffer_load_dwordx2 v[246:247], v237, s[36:39], s64 offen offset:768 sc1\n\t"
      "buffer_load_dwordx2 v[248:249], v237, s[36:39], s64 offen offset:1280 sc1\n\t"
      "buffer_load_dwordx2 v[250:251], v238, s[36:39], s64 offen offset:256 sc1\n\t"
      "s_waitcnt vmcnt(20)\n\t"
      "ds_write_b128 v230, v[194:197] offset:0\n\t"
      "s_waitcnt vmcnt(19)\n\t"
      "ds_write_b128 v230, v[198:201] offset:2048\n\t"
      "s_waitcnt vmcnt(18)\n\t"
      "ds_write_b128 v230, v[202:205] offset:4096\n\t"
      "s_waitcnt vmcnt(17)\n\t"
      "ds_write_b128 v230, v[206:209] offset:6144\n\t"
      "s_waitcnt vmcnt(16)\n\t"
      "ds_write_b128 v231, v[210:213] offset:0\n\t"
      "s_waitcnt vmcnt(15)\n\t"
      "ds_write_b128 v231, v[214:217] offset:2048\n\t"
      "s_waitcnt vmcnt(14)\n\t"
      "ds_write_b128 v231, v[218:221] offset:4096\n\t"
      "s_waitcnt vmcnt(13)\n\t"
      "ds_write_b128 v231, v[222:225] offset:6144\n\t"
      "ds_read_b64_tr_b16 v[130:131], v232 offset:0\n\t"
      "ds_read_b64_tr_b16 v[132:133], v232 offset:512\n\t"
      "ds_read_b64_tr_b16 v[134:135], v232 offset:4096\n\t"
      "ds_read_b64_tr_b16 v[136:137], v232 offset:4608\n\t"
      "ds_read_b64_tr_b16 v[138:139], v233 offset:0\n\t"
      "ds_read_b64_tr_b16 v[140:141], v233 offset:512\n\t"
      "ds_read_b64_tr_b16 v[142:143], v233 offset:4096\n\t"
      "ds_read_b64_tr_b16 v[144:145], v233 offset:4608\n\t"
      "ds_read_b64_tr_b16 v[210:211], v234 offset:0\n\t"
      "ds_read_b64_tr_b16 v[212:213], v234 offset:512\n\t"
      "ds_read_b64_tr_b16 v[214:215], v234 offset:4096\n\t"
      "ds_read_b64_tr_b16 v[216:217], v234 offset:4608\n\t"
      "ds_read_b64_tr_b16 v[146:147], v235 offset:0\n\t"
      "ds_read_b64_tr_b16 v[148:149], v235 offset:512\n\t"
      "ds_read_b64_tr_b16 v[150:151], v235 offset:4096\n\t"
      "ds_read_b64_tr_b16 v[152:153], v235 offset:4608\n\t"
      "s_waitcnt lgkmcnt(0)\n\t"
      "ds_write_b128 v236, v[210:213] offset:16384\n\t"
      "ds_write_b128 v236, v[214:217] offset:17408\n\t"
      "s_waitcnt lgkmcnt(0)\n\t"
      "s_waitcnt vmcnt(12)\n\t"
      "ds_write_b128 v230, v[162:165] offset:0\n\t"
      "s_waitcnt vmcnt(11)\n\t"
      "ds_write_b128 v230, v[166:169] offset:2048\n\t"
      "s_waitcnt vmcnt(10)\n\t"
      "ds_write_b128 v230, v[170:173] offset:4096\n\t"
      "s_waitcnt vmcnt(9)\n\t"
      "ds_write_b128 v230, v[174:177] offset:6144\n\t"
      "s_waitcnt vmcnt(8)\n\t"
      "ds_write_b128 v231, v[178:181] offset:0\n\t"
      "s_waitcnt vmcnt(7)\n\t"
      "ds_write_b128 v231, v[182:185] offset:2048\n\t"
      "s_waitcnt vmcnt(6)\n\t"
      "ds_write_b128 v231, v[186:189] offset:4096\n\t"
      "s_waitcnt vmcnt(5)\n\t"
      "ds_write_b128 v231, v[190:193] offset:6144\n\t"
      "ds_read_b64_tr_b16 v[162:163], v232 offset:0\n\t"
      "ds_read_b64_tr_b16 v[164:165], v232 offset:512\n\t"
      "ds_read_b64_tr_b16 v[166:167], v232 offset:4096\n\t"
      "ds_read_b64_tr_b16 v[168:169], v232 offset:4608\n\t"
      "ds_read_b64_tr_b16 a[248:249], v233 offset:0\n\t"
      "ds_read_b64_tr_b16 a[250:251], v233 offset:512\n\t"
      "ds_read_b64_tr_b16 a[252:253], v233 offset:4096\n\t"
      "ds_read_b64_tr_b16 a[254:255], v233 offset:4608\n\t"
      "ds_read_b64_tr_b16 v[154:155], v234 offset:0\n\t"
      "ds_read_b64_tr_b16 v[156:157], v234 offset:512\n\t"
      "ds_read_b64_tr_b16 v[158:159], v234 offset:4096\n\t"
      "ds_read_b64_tr_b16 v[160:161], v234 offset:4608\n\t"
      "ds_read_b64_tr_b16 v[122:123], v235 offset:0\n\t"
      "ds_read_b64_tr_b16 v[124:125], v235 offset:512\n\t"
      "ds_read_b64_tr_b16 v[126:127], v235 offset:4096\n\t"
      "ds_read_b64_tr_b16 v[128:129], v235 offset:4608\n\t"
      "s_waitcnt lgkmcnt(0)\n\t"
      "ds_write_b128 v236, v[162:165] offset:22528\n\t"
      "ds_write_b128 v236, v[166:169] offset:23552\n\t"
      "s_waitcnt lgkmcnt(0)\n\t"
      "s_waitcnt vmcnt(0) lgkmcnt(0)\n\t"
      "s_barrier\n\t"
      : "={a[0:31]}"(A0), "={a[32:63]}"(A1), "={a[64:95]}"(A2), "={a[96:127]}"(A3), "={a[128:159]}"(A4), "={a[160:191]}"(A5), "={a[192:223]}"(A6), "={a[224:255]}"(A7),
        "={v[2:33]}"(V0), "={v[34:65]}"(V1), "={v[66:97]}"(V2), "={v[98:129]}"(V3), "={v[130:161]}"(V4),
        "={v[242:243]}"(ep), "={v[244:245]}"(tk1), "={v[246:247]}"(hv), "={v[248:249]}"(hf), "={v[250:251]}"(h0)
      : "{v[226:241]}"(C), "{s[36:39]}"(rs_o), "{s[40:43]}"(__builtin_amdgcn_make_buffer_rsrc((void*)P.Wq, 0, DM * HDA * 2, 0x00020000)), "{s[44:47]}"(__builtin_amdgcn_make_buffer_rsrc((void*)P.Wk, 0, DM * HDA * 2, 0x00020000)),
        "{s[48:63]}"(R), "{s64}"(HBASE)
      : V162_225, "memory");
    }
    {
    const int lane = opaque_lane(), tid = lane + 64 * w, l15 = lane & 15, lq = lane >> 4;
    tok = uni64(ep) + 1;
    const bool setup = __all(tk1 == tok && (hv | 3) == 4 * tok + 3 && hf == tok), early = setup && __all(h0 == tok);
    if (!setup)
      for (unsigned spins = 0; spins < (1u << 16); ++spins) {
        tk1 = ld_agent(&P.hdr->token[0]); hv = ld_agent(&P.hdr->vote[lane]); hf = ld_agent(&P.hdr->fill[lane]);
        if (__all(tk1 == tok && (hv | 3) == 4 * tok + 3 && hf == tok)) break;
        __builtin_amdgcn_s_sleep(4);
      }
    plc = count_votes(hv);
    asm volatile("" ::: "memory");
    const __amdgpu_buffer_rsrc_t rs_x = __builtin_amdgcn_make_buffer_rsrc(wsp(P, QA0 + ((3 * h + 1) << 20)), 0, 2 << 20, 0x00020000);
    const int kx0o = faro(plc, QA0 + ((3 * h + 1) << 20)) & 8192, vx0o = (1 << 20) + (faro(plc, QA0 + ((3 * h + 2) << 20)) & 8192);
    v16u C, S; v4u C2; v8u S2;
    C[0] = lane * 16; C[1] = lds0 + w * 4096 + lane * 16;
#pragma unroll
    for (int q = 0; q < 3; ++q) C[2 + q] = lds0 + (w ^ (q + 1)) * 4096 + lane * 16;
    C[5] = lds0 + SW0 + w * (NLW * 2048) + lane * 16;
    C[6] = lds0 + KB0 + (w >> 1) * 4096 + (lq >> 1) * 1024 + (((l15 & 2) + (lq & 1)) * 16 + ((8 * (w & 1) + 2 * (l15 >> 2) + (l15 & 1)) ^ (4 * (l15 & 2)))) * 16;
    C[7] = lds0 + KB0 + 8192 + (lq >> 1) * 4096 + w * 1024 + (l15 & 2) * 128 + (8 * (lq & 1) + (l15 >> 2)) * 16 + (l15 & 1) * 8;
    C[8] = w * 2048 + (lane ^ ((lane >> 5) << 3)) * 16; C[9] = w * 2048 + lane * 16;
    C[10] = lds0 + T0 + w * IMGW + (lq >> 1) * 128 + l15 * 8 + (lq & 1) * 4; C[11] = lds0 + T0 + KIMG + w * IMGW + lq * 128 + (lq >> 1) * 32 + l15 * 8;
    C[12] = lds0 + T0 + (lq >> 1) * IMGW + (l15 & 3) * 128 + (l15 & 2) * 16 + (8 * (lq & 1) + (l15 >> 2)) * 8;
    C[13] = lds0 + T0 + PIMG + (w * 8 + (l15 & 7)) * 16 + lq * 4 + (l15 >> 3) * 512; C[14] = lds0 + T0 + PIMG + (lq * 8 + (l15 & 7)) * 16;
    C[15] = quad(h * 2048) + w * 512 + lane * 4;
    C2[0] = (lane & 8) ? 0x7ffffff0 : quad(h * 2048) + (w * 2 + (l15 >> 2)) * 256 + (lq >> 1) * 128 + (l15 & 2) * 32 + (lq & 1) * 32 + (l15 & 1) * 16;
    C2[1] = (lane & 2) ? 0xEEEE : 0x4444; C2[2] = __float_as_uint(24.f)  ; C2[3] = 0;
    { const unsigned long long pl2 = __ballot((sideo(plc, (lane >> 1) << 21, xq != 1) >> (13 + (lane & 1))) & 1);
      S[0] = (unsigned)pl2; S[1] = (unsigned)(pl2 >> 32); }
#pragma unroll
    for (int k = 0; k < 4; ++k) S[2 + k] = mso[k];
    S[6] = kx0o; S[7] = vx0o - kx0o; S[8] = quad(h * 2048) + (w & 1) * 1024; S[9] = 32 * w + 8192; S[10] = (slot == 0 ? 1 : 0) | (early ? 2 : 0) | xq * 4 | (h == 0 && w == 3 ? 16 : 0);
    S[11] = P.sh1; S[12] = w == 0 ? P.sh2 : P.gain | P.inc << 8 | P.dec << 16; S[13] = w == 0 ? P.pad2 : P.dq2 * 16 | P.dq << 20; S[14] = nearo(plc, RC0); S[15] = FBASE + h * 128;
    S2[0] = (unsigned)tok; S2[1] = (unsigned)(tok >> 32); S2[2] = HBASE + 256 + h * 8; S2[3] = lds0 + KB0 + KVB + w * 2048; S2[4] = CBASE + (int)offsetof(Ctl, tmo);
    S2[5] = (int)((char*)prof - (char*)obuf) + (5 << 17) + (b * 4 + w) * 16; S2[6] = w < 2 ? ~0u : 0u; S2[7] = ~S2[6];
#pragma unroll
    for (int k = 0; k < 16; ++k) { S[k] = __builtin_amdgcn_readfirstlane(S[k]); if (k < 8) S2[k] = __builtin_amdgcn_readfirstlane(S2[k]); }
    v4u S3 = w == 0 ? (v4u){23u * 256u, 0u, 0u, 1000u} : (v4u){P.dmv * 16u, P.dov * 16u, P.dq2 * 16u, P.dq * 16u};
    if (early)
#pragma unroll
      for (int k = 0; k < 4; ++k)
        __builtin_amdgcn_raw_ptr_buffer_load_lds(rs_x, (__attribute__((address_space(3))) void*)(Lraw + KB0 + w * 2048 + (k >> 1) * 8192 + (k & 1) * 1024), 16, C[8 + (k >> 1)], (k < 2 ? kx0o : vx0o) + (k & 1) * 1024, 0, 16);
    asm volatile(
      "s_mov_b32 s68, 0\n\t"
      "s_mov_b32 s69, 0\n\t"
      "s_mov_b32 s70, 1\n\t"
      "s_mov_b32 s17, 0\n\t"
      "s_mov_b32 s32, s58\n\t"
      "s_mov_b32 s77, 16512\n\t"
      "s_mov_b32 s78, 2\n\t"
      "s_mov_b32 s79, 0x3e0293ee\n\t"
      "s_mov_b32 s80, 0\n\t"
      "v_mov_b32 v209, 0\n\t"
      "s_mov_b32 s18, 0x3f803f80\n\t"
      "s_mov_b32 s19, 0x3f803f80\n\t"
      "v_mov_b64 v[222:223], -1\n\t"
      "v_mov_b64 v[224:225], -1\n\t"
      "ds_write_b128 v235, v[222:225] offset:0\n\t"
      "ds_write_b128 v235, v[222:225] offset:1024\n\t"
      "ds_write_b128 v235, v[222:225] offset:2048\n\t"
      "ds_write_b128 v235, v[222:225] offset:3072\n\t"
      "Lqk_top:\n\t"
      "v_mov_b32 v182, 23040\n\t"
      "s_memrealtime s[94:95]\n\t"
      "ds_read_b128 v[178:181], v182\n\t"
      "ds_read_b128 v[210:213], v239 offset:0\n\t"
      "ds_read_b128 v[214:217], v239 offset:1024\n\t"
      "ds_read_b128 v[218:221], v239 offset:2048\n\t"
      "ds_read_b128 v[222:225], v239 offset:3072\n\t"
      "ds_read_b128 v[226:229], v239 offset:4096\n\t"
      "ds_read_b128 v[230:233], v239 offset:5120\n\t"
      "s_lshl_b32 s84, s68, 16\n\t"
      "s_lshr_b32 s93, s84, 20\n\t"
      "s_and_b32 s93, s93, 62\n\t"
      "s_lshr_b64 s[98:99], s[44:45], s93\n\t"
      "s_bfe_u32 s93, s84, 0x10013\n\t"
      "s_lshl1_add_u32 s98, s93, s98\n\t"
      "s_and_b32 s98, s98, 3\n\t"
      "s_lshl_b32 s98, s98, 13\n\t"
      "s_or_b32 s84, s84, s98\n\t"
      "s_add_u32 s81, s84, s46\n\t"
      "s_add_u32 s82, s84, s47\n\t"
      "s_add_u32 s83, s84, s48\n\t"
      "s_add_u32 s84, s84, s49\n\t"
      "s_lshl_b32 s85, s68, 8\n\t"
      "s_and_b32 s93, s85, 0x2000\n\t"
      "s_add_u32 s85, s85, s93\n\t"
      "s_add_u32 s85, s85, s58\n\t"
      "v_mov_b64 v[162:163], 0\n\t"
      "v_mov_b64 v[164:165], 0\n\t"
      "v_mov_b64 v[166:167], 0\n\t"
      "v_mov_b64 v[168:169], 0\n\t"
      "v_mov_b64 v[170:171], 0\n\t"
      "v_mov_b64 v[172:173], 0\n\t"
      "v_mov_b64 v[174:175], 0\n\t"
      "v_mov_b64 v[176:177], 0\n\t"
      "s_waitcnt lgkmcnt(0)\n\t"
      "s_cmp_lg_u32 s53, 8192\n\t"
      "s_cbranch_scc1 Lqk_keep\n\t"
      "s_bitcmp1_b32 s78, 2\n\t"
      "s_cbranch_scc1 Lqk_slow\n\t"
      "s_bitcmp0_b32 s78, 0\n\t"
      "s_cbranch_scc1 Lqk_wait\n\t"
      "v_sub_u32 v179, v179, v178\n\t"
      "v_sub_u32 v180, v180, v178\n\t"
      "v_sub_u32 v181, v181, v178\n\t"
      "v_max3_i32 v179, v179, v180, v181\n\t"
      "s_add_u32 s98, s96, s76\n\t"
      "v_readfirstlane_b32 s93, v179\n\t"
      "s_sub_u32 s98, s98, s56\n\t"
      "s_max_i32 s93, s93, 0\n\t"
      "s_add_u32 s98, s98, s93\n\t"
      "s_bitcmp0_b32 s54, 0\n\t"
      "s_cbranch_scc1 Lqk_late\n\t"
      "s_add_u32 s93, s93, s16\n\t"
      "v_mov_b32 v178, s93\n\t"
      "s_mov_b64 exec, 1\n\t"
      "buffer_store_dword v178, off, s[36:39], s32 offset:128 sc1\n\t"
      "s_mov_b64 exec, -1\n\t"
      "Lqk_late:\n\t"
      "s_sub_u32 s93, s98, s69\n\t"
      "s_cmp_gt_i32 s93, 0\n\t"
      "s_cbranch_scc1 Lqk_shift\n\t"
      "Lqk_wait:\n\t"
      "s_cmp_eq_u32 s68, 0\n\t"
      "s_cbranch_scc1 Lqk_bar\n\t"
      "s_sub_u32 s94, s69, s94\n\t"
      "s_cmp_gt_i32 s94, 20000\n\t"
      "s_cbranch_scc1 Lqk_bar\n\t"
      "s_sub_u32 s93, s73, s75\n\t"
      "s_add_u32 s98, s93, 199\n\t"
      "s_ashr_i32 s93, s93, 1\n\t"
      "s_cmp_lt_u32 s98, 399\n\t"
      "s_cselect_b32 s93, s93, 0\n\t"
      "s_add_u32 s72, s72, s93\n\t"
      "s_sub_u32 s93, s69, s74\n\t"
      "s_mul_i32 s93, s93, s72\n\t"
      "s_lshr_b32 s93, s93, 8\n\t"
      "s_add_u32 s75, s93, s73\n\t"
      "s_add_u32 s73, s75, 10\n\t"
      "s_mov_b32 s74, s69\n\t"
      "s_cmp_lt_i32 s94, 16\n\t"
      "s_cselect_b32 s93, 1000, 0\n\t"
      "s_add_u32 s75, s75, s93\n\t"
      "s_lshr_b32 s93, s72, 6\n\t"
      "Lqk_slp:\n\t"
      "s_cmp_lt_i32 s94, 57\n\t"
      "s_cbranch_scc1 Lqk_fine\n\t"
      "s_sleep 2\n\t"
      "s_memrealtime s[94:95]\n\t"
      "s_waitcnt lgkmcnt(0)\n\t"
      "s_sub_u32 s94, s69, s94\n\t"
      "s_branch Lqk_slp\n\t"
      ".p2align 6\n\t"
      ".rept 2\n\t"
      "s_nop 0\n\t"
      ".endr\n\t"
      "Lqk_fine:\n\t"
      "s_memrealtime vcc\n\t"
      "s_memtime s[98:99]\n\t"
      "s_waitcnt lgkmcnt(0)\n\t"
      "s_sub_i32 vcc_lo, s69, vcc_lo\n\t"
      "s_mul_i32 vcc_lo, vcc_lo, s72\n\t"
      "s_ashr_i32 vcc_lo, vcc_lo, 8\n\t"
      "s_add_i32 vcc_lo, vcc_lo, s98\n\t"
      "s_sub_i32 vcc_hi, vcc_lo, s73\n\t"
      "s_cmp_gt_u32 vcc_hi, s93\n\t"
      "s_cselect_b32 s73, vcc_lo, s73\n\t"
      "s_sub_i32 s98, s73, s98\n\t"
      "s_cmpk_gt_i32 s98, 230\n\t"
      "s_cbranch_scc1 Lqk_fine\n\t"
      "s_sub_i32 s98, s98, s57\n\t"
      "s_max_i32 s98, s98, 0\n\t"
      "s_min_i32 s98, s98, 192\n\t"
      "s_and_b32 s98, s98, -4\n\t"
      "s_sub_i32 s98, 204, s98\n\t"
      "s_getpc_b64 vcc\n\t"
      "s_add_u32 vcc_lo, vcc_lo, s98\n\t"
      "s_addc_u32 vcc_hi, vcc_hi, 0\n\t"
      "s_setpc_b64 vcc\n\t"
      ".rept 48\n\t"
      "s_nop 0\n\t"
      ".endr\n\t"
      "Lqk_bar:\n\t"
      "s_barrier\n\t"
      "buffer_load_dwordx4 v[178:181], v234, s[36:39], s81 offen sc1\n\t"
      "s_lshl_b32 s86, s68, 18\n\t"
      "s_add_u32 s86, s86, 0x1400000\n\t"
      "s_lshr_b32 s93, s86, 20\n\t"
      "s_and_b32 s93, s93, 62\n\t"
      "s_lshr_b64 s[98:99], s[44:45], s93\n\t"
      "s_bfe_u32 s93, s86, 0x10013\n\t"
      "s_lshl1_add_u32 s98, s93, s98\n\t"
      "s_and_b32 s98, s98, 3\n\t"
      "buffer_load_dwordx4 v[182:185], v234, s[36:39], s82 offen sc1\n\t"
      "s_lshl_b32 s98, s98, 13\n\t"
      "s_or_b32 s86, s86, s98\n\t"
      "s_xor_b32 s87, s86, 0x4000\n\t"
      "s_add_u32 s87, s87, s52\n\t"
      "s_lshl_b32 s88, s68, 18\n\t"
      "s_add_u32 s88, s88, 0x400000\n\t"
      "s_lshr_b32 s93, s88, 20\n\t"
      "s_and_b32 s93, s93, 62\n\t"
      "buffer_load_dwordx4 v[186:189], v234, s[36:39], s83 offen sc1\n\t"
      "s_lshr_b64 s[98:99], s[44:45], s93\n\t"
      "s_bfe_u32 s93, s88, 0x10013\n\t"
      "s_lshl1_add_u32 s98, s93, s98\n\t"
      "s_and_b32 s98, s98, 3\n\t"
      "s_lshl_b32 s98, s98, 13\n\t"
      "s_or_b32 s88, s88, s98\n\t"
      "s_xor_b32 s89, s88, 0x4000\n\t"
      "s_and_b32 s78, s78, 14\n\t"
      "buffer_load_dwordx4 v[190:193], v234, s[36:39], s84 offen sc1\n\t"
      "s_mov_b32 s34, s69\n\t"
      "s_waitcnt vmcnt(4)\n\t"
      "v_readlane_b32 s93, v209, 0\n\t"
      "v_readlane_b32 s92, v209, 4\n\t"
      "s_min_u32 s93, s93, s92\n\t"
      "s_max_u32 s70, s70, s93\n\t"
      "s_add_u32 s90, s68, 1\n\t"
      "s_cmp_lt_u32 s90, s70\n\t"
      "s_cselect_b32 s90, s90, s68\n\t"
      "s_cselect_b32 s93, 16, 0\n\t"
      "s_or_b32 s78, s78, s93\n\t"
      "s_lshl_b32 s90, s90, 14\n\t"
      "s_add_u32 s90, s90, s50\n\t"
      "s_add_u32 s91, s90, s51\n\t"
      "s_mov_b32 m0, s63\n\t"
      "s_waitcnt vmcnt(3)\n\t"
      "v_smfmac_f32_16x16x64_bf16 v[162:165], v[178:181], a[0:7], v251\n\t"
      "ds_write_b64 v235, v[178:179] offset:0\n\t"
      "v_smfmac_f32_16x16x64_bf16 v[166:169], v[178:181], a[8:15], v251\n\t"
      "ds_write_b64 v235, v[180:181] offset:8\n\t"
      "v_smfmac_f32_16x16x64_bf16 v[170:173], v[178:181], v[2:9], v251\n\t"
      "v_smfmac_f32_16x16x64_bf16 v[174:177], v[178:181], a[16:23], v251\n\t"
      "s_waitcnt vmcnt(2)\n\t"
      "v_smfmac_f32_16x16x64_bf16 v[162:165], v[182:185], a[24:31], v251\n\t"
      "ds_write_b64 v235, v[182:183] offset:1024\n\t"
      "v_smfmac_f32_16x16x64_bf16 v[166:169], v[182:185], v[10:17], v251\n\t"
      "ds_write_b64 v235, v[184:185] offset:1032\n\t"
      "v_smfmac_f32_16x16x64_bf16 v[170:173], v[182:185], a[32:39], v251\n\t"
      "v_smfmac_f32_16x16x64_bf16 v[174:177], v[182:185], a[40:47], v251\n\t"
      "s_waitcnt vmcnt(1)\n\t"
      "v_smfmac_f32_16x16x64_bf16 v[162:165], v[186:189], v[18:25], v251\n\t"
      "ds_write_b64 v235, v[186:187] offset:2048\n\t"
      "v_smfmac_f32_16x16x64_bf16 v[166:169], v[186:189], a[48:55], v251\n\t"
      "ds_write_b64 v235, v[188:189] offset:2056\n\t"
      "v_smfmac_f32_16x16x64_bf16 v[170:173], v[186:189], a[56:63], v251\n\t"
      "v_smfmac_f32_16x16x64_bf16 v[174:177], v[186:189], v[210:217], v251\n\t"
      "ds_read_b128 v[194:197], v236 offset:0\n\t"
      "s_waitcnt vmcnt(0)\n\t"
      "v_smfmac_f32_16x16x64_bf16 v[162:165], v[190:193], v[26:33], v251\n\t"
      "ds_write_b64 v235, v[190:191] offset:3072\n\t"
      "v_smfmac_f32_16x16x64_bf16 v[166:169], v[190:193], a[64:71], v251\n\t"
      "ds_write_b64 v235, v[192:193] offset:3080\n\t"
      "v_smfmac_f32_16x16x64_bf16 v[170:173], v[190:193], a[72:79], v251\n\t"
      "ds_read_b128 v[198:201], v237 offset:0\n\t"
      "v_smfmac_f32_16x16x64_bf16 v[174:177], v[190:193], v[218:225], v251\n\t"
      "ds_read_b128 v[210:213], v239 offset:6144\n\t"
      "s_waitcnt lgkmcnt(4)\n\t"
      "v_smfmac_f32_16x16x64_bf16 v[162:165], v[194:197], v[34:41], v251\n\t"
      "ds_read_b128 v[214:217], v239 offset:7168\n\t"
      "v_smfmac_f32_16x16x64_bf16 v[166:169], v[194:197], a[80:87], v251\n\t"
      "ds_read_b128 v[202:205], v238 offset:0\n\t"
      "v_smfmac_f32_16x16x64_bf16 v[170:173], v[194:197], a[88:95], v251\n\t"
      "v_smfmac_f32_16x16x64_bf16 v[174:177], v[194:197], v[226:233], v251\n\t"
      "ds_read_b128 v[218:221], v239 offset:8192\n\t"
      "s_waitcnt lgkmcnt(4)\n\t"
      "v_smfmac_f32_16x16x64_bf16 v[162:165], v[198:201], v[42:49], v251\n\t"
      "ds_read_b128 v[222:225], v239 offset:9216\n\t"
      "v_smfmac_f32_16x16x64_bf16 v[166:169], v[198:201], a[96:103], v251\n\t"
      "ds_read_b128 v[206:209], v236 offset:1024\n\t"
      "v_smfmac_f32_16x16x64_bf16 v[170:173], v[198:201], a[104:111], v251\n\t"
      "ds_read_b128 v[226:229], v239 offset:10240\n\t"
      "v_smfmac_f32_16x16x64_bf16 v[174:177], v[198:201], v[50:57], v251\n\t"
      "ds_read_b128 v[230:233], v239 offset:11264\n\t"
      "s_waitcnt lgkmcnt(5)\n\t"
      "v_smfmac_f32_16x16x64_bf16 v[162:165], v[202:205], v[210:217], v251\n\t"
      "ds_read_b128 v[178:181], v237 offset:1024\n\t"
      "v_smfmac_f32_16x16x64_bf16 v[166:169], v[202:205], a[112:119], v251\n\t"
      "ds_read_b128 v[210:213], v239 offset:12288\n\t"
      "v_smfmac_f32_16x16x64_bf16 v[170:173], v[202:205], a[120:127], v251\n\t"
      "ds_read_b128 v[182:185], v238 offset:1024\n\t"
      "v_smfmac_f32_16x16x64_bf16 v[174:177], v[202:205], v[58:65], v251\n\t"
      "ds_read_b128 v[214:217], v239 offset:13312\n\t"
      "s_waitcnt lgkmcnt(6)\n\t"
      "v_smfmac_f32_16x16x64_bf16 v[162:165], v[206:209], v[218:225], v251\n\t"
      "ds_read_b128 v[186:189], v236 offset:2048\n\t"
      "v_smfmac_f32_16x16x64_bf16 v[166:169], v[206:209], a[128:135], v251\n\t"
      "ds_read_b128 v[218:221], v239 offset:14336\n\t"
      "v_smfmac_f32_16x16x64_bf16 v[170:173], v[206:209], a[136:143], v251\n\t"
      "ds_read_b128 v[190:193], v237 offset:2048\n\t"
      "v_smfmac_f32_16x16x64_bf16 v[174:177], v[206:209], v[66:73], v251\n\t"
      "ds_read_b128 v[222:225], v239 offset:15360\n\t"
      "s_waitcnt lgkmcnt(7)\n\t"
      "v_smfmac_f32_16x16x64_bf16 v[162:165], v[178:181], v[226:233], v251\n\t"
      "ds_read_b128 v[194:197], v238 offset:2048\n\t"
      "v_smfmac_f32_16x16x64_bf16 v[166:169], v[178:181], a[144:151], v251\n\t"
      "ds_read_b128 v[226:229], v239 offset:16384\n\t"
      "s_waitcnt lgkmcnt(7)\n\t"
      "v_smfmac_f32_16x16x64_bf16 v[162:165], v[182:185], a[152:159], v251\n\t"
      "ds_read_b128 v[198:201], v236 offset:3072\n\t"
      "v_smfmac_f32_16x16x64_bf16 v[166:169], v[182:185], v[74:81], v251\n\t"
      "ds_read_b128 v[202:205], v237 offset:3072\n\t"
      "s_waitcnt lgkmcnt(7)\n\t"
      "v_smfmac_f32_16x16x64_bf16 v[162:165], v[186:189], v[210:217], v251\n\t"
      "ds_read_b128 v[206:209], v238 offset:3072\n\t"
      "v_smfmac_f32_16x16x64_bf16 v[166:169], v[186:189], a[160:167], v251\n\t"
      "ds_read_b128 v[230:233], v239 offset:17408\n\t"
      "s_waitcnt lgkmcnt(7)\n\t"
      "v_smfmac_f32_16x16x64_bf16 v[162:165], v[190:193], a[168:175], v251\n\t"
      "v_smfmac_f32_16x16x64_bf16 v[166:169], v[190:193], v[82:89], v251\n\t"
      "s_waitcnt lgkmcnt(5)\n\t"
      "v_smfmac_f32_16x16x64_bf16 v[162:165], v[194:197], v[218:225], v251\n\t"
      "v_smfmac_f32_16x16x64_bf16 v[166:169], v[194:197], a[176:183], v251\n\t"
      "ds_read_b128 v[210:213], v239 offset:18432\n\t"
      "s_waitcnt lgkmcnt(4)\n\t"
      "v_smfmac_f32_16x16x64_bf16 v[162:165], v[198:201], a[184:191], v251\n\t"
      "ds_read_b128 v[214:217], v239 offset:19456\n\t"
      "s_waitcnt lgkmcnt(4)\n\t"
      "v_smfmac_f32_16x16x64_bf16 v[162:165], v[202:205], v[130:137], v251\n\t"
      "ds_read_b128 v[218:221], v239 offset:20480\n\t"
      "s_waitcnt lgkmcnt(3)\n\t"
      "v_smfmac_f32_16x16x64_bf16 v[162:165], v[206:209], v[226:233], v251\n\t"
      "ds_read_b128 v[222:225], v239 offset:21504\n\t"
      "v_smfmac_f32_16x16x64_bf16 v[166:169], v[198:201], v[90:97], v251\n\t"
      "ds_read_b128 v[226:229], v239 offset:22528\n\t"
      "v_smfmac_f32_16x16x64_bf16 v[166:169], v[202:205], v[138:145], v251\n\t"
      "ds_read_b128 v[230:233], v239 offset:23552\n\t"
      "v_add_f32 v162, v162, v164\n\t"
      "v_smfmac_f32_16x16x64_bf16 v[166:169], v[206:209], v[146:153], v251\n\t"
      "v_add_f32 v163, v163, v165\n\t"
      "v_smfmac_f32_16x16x64_bf16 v[170:173], v[178:181], a[192:199], v251\n\t"
      "v_cvt_pk_bf16_f32 v164, v162, v163\n\t"
      "v_cmp_u_f32 vcc, v162, v163\n\t"
      "s_waitcnt lgkmcnt(4)\n\t"
      "v_smfmac_f32_16x16x64_bf16 v[174:177], v[178:181], v[210:217], v251\n\t"
      "v_add_f32 v166, v166, v168\n\t"
      "v_add_f32 v167, v167, v169\n\t"
      "v_smfmac_f32_16x16x64_bf16 v[170:173], v[182:185], a[200:207], v251\n\t"
      "v_cvt_pk_bf16_f32 v165, v166, v167\n\t"
      "ds_write2_b32 v244, v164, v165 offset1:72\n\t"
      "v_smfmac_f32_16x16x64_bf16 v[174:177], v[182:185], v[98:105], v251\n\t"
      "s_cbranch_vccnz Lqk_nan\n\t"
      "Lqk_res:\n\t"
      "ds_read_b128 v[178:181], v240 offset:0\n\t"
      "v_smfmac_f32_16x16x64_bf16 v[170:173], v[186:189], a[208:215], v251\n\t"
      "ds_read_b128 v[182:185], v240 offset:2048\n\t"
      "v_smfmac_f32_16x16x64_bf16 v[174:177], v[186:189], a[216:223], v251\n\t"
      "s_waitcnt lgkmcnt(2)\n\t"
      "s_barrier\n\t"
      "ds_read_b64_tr_b16 v[162:163], v246 offset:0\n\t"
      "ds_read_b64_tr_b16 v[164:165], v246 offset:32\n\t"
      "v_smfmac_f32_16x16x64_bf16 v[170:173], v[190:193], v[218:225], v251\n\t"
      "ds_read_b64_tr_b16 v[166:167], v246 offset:1152\n\t"
      "ds_read_b64_tr_b16 v[168:169], v246 offset:1184\n\t"
      "v_smfmac_f32_16x16x64_bf16 v[174:177], v[190:193], v[106:113], v251\n\t"
      "v_smfmac_f32_16x16x64_bf16 v[170:173], v[194:197], a[224:231], v251\n\t"
      "v_smfmac_f32_16x16x64_bf16 v[174:177], v[194:197], a[232:239], v251\n\t"
      "s_waitcnt lgkmcnt(2)\n\t"
      "v_mfma_f32_16x16x32_bf16 v[186:189], v[178:181], v[162:165], 0\n\t"
      "v_smfmac_f32_16x16x64_bf16 v[170:173], v[198:201], v[114:121], v251\n\t"
      "buffer_load_dword v190, v249, s[36:39], s86 offen sc1\n\t"
      "s_waitcnt lgkmcnt(0)\n\t"
      "v_mfma_f32_16x16x32_bf16 v[186:189], v[182:185], v[166:169], v[186:189]\n\t"
      "v_smfmac_f32_16x16x64_bf16 v[170:173], v[202:205], v[226:233], v251\n\t"
      "buffer_load_dword v192, v249, s[36:39], s86 offen offset:256 sc1\n\t"
      "v_smfmac_f32_16x16x64_bf16 v[170:173], v[206:209], v[154:161], v251\n\t"
      "v_add_f32_dpp v253, v188, v186 row_ror:8 row_mask:0xf bank_mask:0x3\n\t"
      "v_add_f32_dpp v253, v187, v189 row_ror:8 row_mask:0xf bank_mask:0xc\n\t"
      "v_smfmac_f32_16x16x64_bf16 v[174:177], v[198:201], a[240:247], v251\n\t"
      "v_fma_f32 v253, v253, s79, -v252\n\t"
      "v_exp_f32 v186, v253\n\t"
      "v_smfmac_f32_16x16x64_bf16 v[174:177], v[202:205], a[248:255], v251\n\t"
      "buffer_load_dwordx2 v[196:197], off, s[36:39], s85 offset:8 sc1\n\t"
      "v_add_f32 v170, v170, v172\n\t"
      "v_smfmac_f32_16x16x64_bf16 v[174:177], v[206:209], v[122:129], v251\n\t"
      "v_mov_b32_dpp v187, v186 row_ror:8 row_mask:0xf bank_mask:0xf\n\t"
      "v_cvt_pk_bf16_f32 v186, v186, v187\n\t"
      "ds_write_b32 v247, v186\n\t"
      "v_add_f32 v171, v171, v173\n\t"
      "v_cvt_pk_bf16_f32 v172, v170, v171\n\t"
      "v_cmp_u_f32 vcc, v253, v253\n\t"
      "v_mov_b64 v[198:199], s[18:19]\n\t"
      "v_mov_b64 v[200:201], s[18:19]\n\t"
      "v_add_f32 v174, v174, v176\n\t"
      "v_add_f32 v175, v175, v177\n\t"
      "v_cvt_pk_bf16_f32 v173, v174, v175\n\t"
      "ds_write_b64 v245, v[172:173]\n\t"
      "v_mov_b32 v195, 0\n\t"
      "ds_read_b64_tr_b16 v[178:179], v241 offset:0\n\t"
      "ds_read_b64_tr_b16 v[180:181], v241 offset:64\n\t"
      "ds_read_b64_tr_b16 v[182:183], v241 offset:512\n\t"
      "ds_read_b64_tr_b16 v[184:185], v241 offset:576\n\t"
      "s_bitcmp1_b32 s78, 1\n\t"
      "s_cbranch_scc1 Lqk_first\n\t"
      "Lqk_first_r:\n\t"
      "s_cbranch_vccnz Lqk_nanb\n\t"
      "Lqk_resb:\n\t"
      "s_waitcnt lgkmcnt(4)\n\t"
      "s_barrier\n\t"
      "ds_read_b64_tr_b16 v[210:211], v246 offset:2304\n\t"
      "ds_read_b64_tr_b16 v[212:213], v246 offset:2336\n\t"
      "ds_read_b64_tr_b16 v[214:215], v246 offset:3456\n\t"
      "ds_read_b64_tr_b16 v[216:217], v246 offset:3488\n\t"
      "ds_read_b128 v[218:221], v248\n\t"
      "s_cmp_eq_u32 s68, 63\n\t"
      "s_cbranch_scc1 Lqk_last\n\t"
      "s_waitcnt lgkmcnt(3)\n\t"
      "v_mfma_f32_16x16x32_bf16 v[222:225], v[210:213], v[162:165], 0\n\t"
      "s_waitcnt lgkmcnt(1)\n\t"
      "v_mfma_f32_16x16x32_bf16 v[222:225], v[214:217], v[166:169], v[222:225]\n\t"
      "s_waitcnt lgkmcnt(0)\n\t"
      "v_mfma_f32_16x16x32_bf16 v[226:229], v[178:181], v[218:221], 0\n\t"
      "v_mfma_f32_16x16x32_bf16 v[230:233], v[182:185], v[218:221], 0\n\t"
      "v_add_f32_dpp v205, v224, v222 row_ror:8 row_mask:0xf bank_mask:0x3\n\t"
      "v_add_f32_dpp v205, v223, v225 row_ror:8 row_mask:0xf bank_mask:0xc\n\t"
      "v_fma_f32 v205, v205, s79, -v252\n\t"
      "v_exp_f32 v202, v205\n\t"
      "v_mfma_f32_16x16x32_bf16 v[170:173], v[198:201], v[218:221], 0\n\t"
      "s_waitcnt vmcnt(1)\n\t"
      "v_max_u32 v204, v190, v192\n\t"
      "v_mov_b32_dpp v203, v202 row_ror:8 row_mask:0xf bank_mask:0xf\n\t"
      "v_cvt_pk_bf16_f32 v194, v202, v203\n\t"
      "v_cmp_eq_u32 s[94:95], -1, v204\n\t"
      "s_nop 0\n\t"
      "v_mfma_f32_16x16x16_bf16 v[170:173], v[198:199], v[194:195], v[170:173]\n\t"
      "v_mfma_f32_16x16x16_bf16 v[226:229], v[190:191], v[194:195], v[226:229]\n\t"
      "v_mfma_f32_16x16x16_bf16 v[230:233], v[192:193], v[194:195], v[230:233]\n\t"
      "v_rcp_f32 v202, v170\n\t"
      "v_subrev_u32 v203, 0x2b800000, v170\n\t"
      "v_cmp_lt_u32 vcc, 0x14000000, v203\n\t"
      "s_nop 0\n\t"
      "v_mul_f32 v226, v226, v202\n\t"
      "v_mul_f32 v227, v227, v202\n\t"
      "v_mul_f32 v228, v228, v202\n\t"
      "v_mul_f32 v229, v229, v202\n\t"
      "v_cvt_pk_bf16_f32 v174, v226, v227\n\t"
      "v_cvt_pk_bf16_f32 v175, v228, v229\n\t"
      "v_mul_f32 v230, v230, v202\n\t"
      "v_mul_f32 v231, v231, v202\n\t"
      "v_mul_f32 v232, v232, v202\n\t"
      "v_mul_f32 v233, v233, v202\n\t"
      "s_or_b64 s[94:95], s[94:95], vcc\n\t"
      "v_cvt_pk_bf16_f32 v176, v230, v231\n\t"
      "v_cvt_pk_bf16_f32 v177, v232, v233\n\t"
      "s_cbranch_scc1 Lqk_bad\n\t"
      "Lqk_pub:\n\t"
      "buffer_store_dwordx4 v[174:177], v250, s[36:39], s88 offen sc1\n\t"
      "s_memrealtime s[96:97]\n\t"
      "v_log_f32 v202, v170\n\t"
      "v_add_u32 v240, s77, v240\n\t"
      "v_add_f32 v252, v252, v202\n\t"
      "v_floor_f32 v252, v252\n\t"
      "v_add_f32 v252, 0x41800000, v252\n\t"
      "v_min_f32 v252, 0x44000000, v252\n\t"
      "v_max_f32 v252, 0xc4000000, v252\n\t"
      "v_add_u32 v241, s77, v241\n\t"
      "s_nop 0\n\t"
      "buffer_store_dwordx4 v[174:177], v250, s[36:39], s89 offen sc1\n\t"
      "v_mov_b32_dpp v252, v252 row_ror:8 row_mask:0xf bank_mask:0xc\n\t"
      "s_bitcmp0_b32 s54, 4\n\t"
      "s_cbranch_scc1 Lqk_nkr\n\t"
      "s_sub_u32 s93, s32, 1408\n\t"
      "s_mov_b64 exec, 255\n\t"
      "buffer_load_dword v206, v249, s[36:39], s93 offen sc1\n\t"
      "s_mov_b64 exec, -1\n\t"
      "s_waitcnt vmcnt(3)\n\t"
      "s_branch Lqk_kr\n\t"
      "Lqk_nkr:\n\t"
      "s_waitcnt vmcnt(2)\n\t"
      "Lqk_kr:\n\t"
      "v_readfirstlane_b32 s93, v196\n\t"
      "v_readfirstlane_b32 s76, v197\n\t"
      "s_lshr_b32 s98, s53, 3\n\t"
      "s_add_u32 s98, s98, 22016\n\t"
      "v_mov_b32 v203, s98\n\t"
      "s_max_u32 s98, s93, s76\n\t"
      "s_cmp_eq_u32 s98, -1\n\t"
      "s_cbranch_scc1 Lqk_rec\n\t"
      "Lqk_rec_r:\n\t"
      "s_add_u32 s69, s69, s93\n\t"
      "s_add_u32 s69, s69, s76\n\t"
      "s_and_b32 s32, s54, 12\n\t"
      "s_add_u32 s32, s32, s85\n\t"
      "s_waitcnt lgkmcnt(0)\n\t"
      "v_mov_b32 v202, s96\n\t"
      "ds_write_b32 v203, v202\n\t"
      "s_sub_u32 s16, s96, s34\n\t"
      "s_min_u32 s16, s16, 0x4000\n\t"
      "s_cmp_lg_u32 s80, 0\n\t"
      "s_cselect_b32 s16, 0x4000, s16\n\t"
      "s_mov_b64 exec, s[66:67]\n\t"
      "s_bitcmp1_b32 s53, 5\n\t"
      "s_cbranch_scc1 Lqk_km1\n\t"
      "buffer_store_dwordx4 v[210:213], v234, s[36:39], s87 offen sc1\n\t"
      "s_branch Lqk_km2\n\t"
      "Lqk_km1:\n\t"
      "buffer_store_dwordx4 v[214:217], v234, s[36:39], s87 offen sc1\n\t"
      "Lqk_km2:\n\t"
      "s_mov_b64 exec, -1\n\t"
      "s_mov_b64 exec, 17\n\t"
      "buffer_load_dword v209, v234, s[36:39], s59 offen sc1\n\t"
      "s_mov_b64 exec, -1\n\t"
      "v_mov_b64 v[222:223], -1\n\t"
      "v_mov_b64 v[224:225], -1\n\t"
      "ds_write_b128 v235, v[222:225] offset:0\n\t"
      "ds_write_b128 v235, v[222:225] offset:1024\n\t"
      "ds_write_b128 v235, v[222:225] offset:2048\n\t"
      "ds_write_b128 v235, v[222:225] offset:3072\n\t"
      "s_bitcmp0_b32 s78, 4\n\t"
      "s_cbranch_scc1 Lqk_kvl\n\t"
      "Lqk_kvl_r:\n\t"
      "s_sleep 3\n\t"
      "s_mov_b32 m0, s63\n\t"
      "s_nop 0\n\t"
      "buffer_load_dwordx4 v242, s[40:43], s90 offen offset:0 sc1 lds\n\t"
      "buffer_load_dwordx4 v242, s[40:43], s90 offen offset:1024 sc1 lds\n\t"
      "s_add_u32 m0, s63, 8192\n\t"
      "s_nop 0\n\t"
      "buffer_load_dwordx4 v243, s[40:43], s91 offen offset:0 sc1 lds\n\t"
      "buffer_load_dwordx4 v243, s[40:43], s91 offen offset:1024 sc1 lds\n\t"
      "s_add_u32 s93, s96, 70\n\t"
      "s_cmp_eq_u32 s68, 0\n\t"
      "s_cselect_b32 s69, s93, s69\n\t"
      "s_cselect_b32 s78, 5, 1\n\t"
      "s_sub_u32 s63, s63, s77\n\t"
      "s_sub_u32 s77, 0, s77\n\t"
      "s_add_u32 s68, s68, 1\n\t"
      "s_mov_b32 s80, 0\n\t"
      "s_waitcnt lgkmcnt(0)\n\t"
      "s_barrier\n\t"
      "s_branch Lqk_top\n\t"
      "Lqk_shift:\n\t"
      "s_add_u32 s69, s69, s55\n\t"
      "s_branch Lqk_late\n\t"
      "Lqk_keep:\n\t"
      "s_bitcmp0_b32 s78, 0\n\t"
      "s_cbranch_scc1 Lqk_bar\n\t"
      "s_bitcmp1_b32 s54, 4\n\t"
      "s_cbranch_scc0 Lqk_bar\n\t"
      "s_cmp_lt_u32 s68, 3\n\t"
      "s_cbranch_scc1 Lqk_keep2\n\t"
      "s_mov_b32 s86, -1\n\t"
      "s_mov_b32 s87, -1\n\t"
      "s_mov_b32 s88, 0\n\t"
      "s_waitcnt vmcnt(6)\n\t"
      "s_lshr_b32 s89, s72, 4\n\t"
      "s_add_u32 s89, s89, 8\n\t"
      "v_readlane_b32 s93, v206, 0\n\t"
      "s_cmp_gt_u32 s93, s89\n\t"
      "s_addc_u32 s88, s88, 0\n\t"
      "s_cmp_gt_u32 s93, s89\n\t"
      "s_cselect_b32 s93, -1, s93\n\t"
      "s_max_i32 s86, s86, s93\n\t"
      "v_readlane_b32 s93, v206, 1\n\t"
      "s_cmp_gt_u32 s93, s89\n\t"
      "s_addc_u32 s88, s88, 0\n\t"
      "s_cmp_gt_u32 s93, s89\n\t"
      "s_cselect_b32 s93, -1, s93\n\t"
      "s_max_i32 s86, s86, s93\n\t"
      "v_readlane_b32 s93, v206, 2\n\t"
      "s_cmp_gt_u32 s93, s89\n\t"
      "s_addc_u32 s88, s88, 0\n\t"
      "s_cmp_gt_u32 s93, s89\n\t"
      "s_cselect_b32 s93, -1, s93\n\t"
      "s_max_i32 s86, s86, s93\n\t"
      "s_mov_b32 s90, s88\n\t"
      "s_lshr_b32 s89, s73, 4\n\t"
      "s_add_u32 s89, s89, 8\n\t"
      "v_readlane_b32 s93, v206, 4\n\t"
      "s_cmp_gt_u32 s93, s89\n\t"
      "s_addc_u32 s88, s88, 0\n\t"
      "s_cmp_gt_u32 s93, s89\n\t"
      "s_cselect_b32 s93, -1, s93\n\t"
      "s_max_i32 s87, s87, s93\n\t"
      "v_readlane_b32 s93, v206, 5\n\t"
      "s_cmp_gt_u32 s93, s89\n\t"
      "s_addc_u32 s88, s88, 0\n\t"
      "s_cmp_gt_u32 s93, s89\n\t"
      "s_cselect_b32 s93, -1, s93\n\t"
      "s_max_i32 s87, s87, s93\n\t"
      "v_readlane_b32 s93, v206, 6\n\t"
      "s_cmp_gt_u32 s93, s89\n\t"
      "s_addc_u32 s88, s88, 0\n\t"
      "s_cmp_gt_u32 s93, s89\n\t"
      "s_cselect_b32 s93, -1, s93\n\t"
      "s_max_i32 s87, s87, s93\n\t"
      "s_cmp_eq_u32 s68, 3\n\t"
      "s_cselect_b32 s88, s90, s88\n\t"
      "s_cselect_b32 s87, -1, s87\n\t"
      "s_bfe_u32 s91, s56, 0x80008\n\t"
      "s_bfe_u32 s92, s56, 0x80010\n\t"
      "s_and_b32 s90, s56, 255\n\t"
      "s_cmp_lt_u32 s68, 8\n\t"
      "s_cselect_b32 s90, 10, s90\n\t"
      "s_sub_u32 s92, 0, s92\n\t"
      "s_cmp_lg_u32 s88, 0\n\t"
      "s_cselect_b32 s91, s91, s92\n\t"
      "s_cselect_b32 s88, 32, 0\n\t"
      "s_add_u32 s74, s74, s91\n\t"
      "s_bfe_u32 s93, s57, 0x100000\n\t"
      "s_max_i32 s74, s74, s93\n\t"
      "s_min_i32 s74, s74, 960\n\t"
      "s_lshl_b32 s93, s86, 4\n\t"
      "s_sub_u32 s93, s72, s93\n\t"
      "s_sub_u32 s93, s93, s74\n\t"
      "s_mul_i32 s89, s93, s90\n\t"
      "s_ashr_i32 s89, s89, 4\n\t"
      "s_min_i32 s93, s93, s89\n\t"
      "s_max_i32 s93, s93, -128\n\t"
      "s_cmp_lt_i32 s86, 0\n\t"
      "s_cselect_b32 s93, 0, s93\n\t"
      "s_sub_u32 s72, s72, s93\n\t"
      "s_and_b32 s93, s88, s17\n\t"
      "s_add_u32 s72, s72, s93\n\t"
      "s_add_u32 s75, s75, s91\n\t"
      "s_bfe_u32 s93, s57, 0x100010\n\t"
      "s_max_i32 s75, s75, s93\n\t"
      "s_min_i32 s75, s75, 960\n\t"
      "s_lshl_b32 s93, s87, 4\n\t"
      "s_sub_u32 s93, s73, s93\n\t"
      "s_sub_u32 s93, s93, s75\n\t"
      "s_mul_i32 s89, s93, s90\n\t"
      "s_ashr_i32 s89, s89, 4\n\t"
      "s_min_i32 s93, s93, s89\n\t"
      "s_max_i32 s93, s93, -128\n\t"
      "s_cmp_lt_i32 s87, 0\n\t"
      "s_cselect_b32 s93, 0, s93\n\t"
      "s_sub_u32 s73, s73, s93\n\t"
      "s_and_b32 s93, s88, s17\n\t"
      "s_add_u32 s73, s73, s93\n\t"
      "s_mov_b32 s17, s88\n\t"
      "Lqk_keep2:\n\t"
      "s_lshr_b32 s93, s72, 4\n\t"
      "v_mov_b32 v178, s93\n\t"
      "s_lshr_b32 s93, s73, 4\n\t"
      "v_mov_b32 v179, s93\n\t"
      "s_add_u32 s93, s68, 1\n\t"
      "s_lshl_b32 s93, s93, 8\n\t"
      "s_and_b32 s93, s93, 0x3fff\n\t"
      "s_and_b32 s89, s93, 0x2000\n\t"
      "s_add_u32 s93, s93, s89\n\t"
      "s_add_u32 s93, s93, s58\n\t"
      "s_mov_b64 exec, 1\n\t"
      "buffer_store_dwordx2 v[178:179], off, s[36:39], s93 offset:8 sc1\n\t"
      "s_mov_b64 exec, -1\n\t"
      "s_nop 1\n\t"
      "s_branch Lqk_bar\n\t"
      "Lqk_slow:\n\t"
      "s_memrealtime s[94:95]\n\t"
      "s_waitcnt lgkmcnt(0)\n\t"
      "s_sub_u32 s94, s69, s94\n\t"
      "s_sub_u32 s94, s94, 1\n\t"
      "s_cmp_lt_u32 s94, 20000\n\t"
      "s_cbranch_scc1 Lqk_slow\n\t"
      "s_mov_b32 s71, 0\n\t"
      "Lqk_slow2:\n\t"
      "buffer_load_dword v178, off, s[36:39], s85 sc1\n\t"
      "s_waitcnt vmcnt(0)\n\t"
      "v_readfirstlane_b32 s69, v178\n\t"
      "s_memrealtime s[94:95]\n\t"
      "s_waitcnt lgkmcnt(0)\n\t"
      "s_cmp_lg_u32 s69, -1\n\t"
      "s_cbranch_scc1 Lqk_wait\n\t"
      "s_add_u32 s71, s71, 1\n\t"
      "s_and_b32 s93, s71, 31\n\t"
      "s_cmp_lg_u32 s93, 31\n\t"
      "s_cbranch_scc1 Lqk_sp1\n\t"
      "s_cmp_lt_u32 s71, 0x8000\n\t"
      "s_cbranch_scc1 Lqk_sp1b\n\t"
      "v_mov_b32 v178, 1\n\t"
      "buffer_store_dword v178, off, s[36:39], s64 sc1\n\t"
      "s_branch Lqk_bar\n\t"
      "Lqk_sp1b:\n\t"
      "buffer_load_dword v178, off, s[36:39], s64 sc1\n\t"
      "s_waitcnt vmcnt(0)\n\t"
      "v_readfirstlane_b32 s93, v178\n\t"
      "s_cmp_lg_u32 s93, 0\n\t"
      "s_cbranch_scc1 Lqk_bar\n\t"
      "Lqk_sp1:\n\t"
      "s_sleep 2\n\t"
      "s_branch Lqk_slow2\n\t"
      "Lqk_nanb:\n\t"
      "s_bitcmp1_b32 s78, 3\n\t"
      "s_cbranch_scc1 Lqk_resb\n\t"
      "s_branch Lqk_nan1\n\t"
      "Lqk_nan:\n\t"
      "s_and_b32 s93, s78, 10\n\t"
      "s_cbranch_scc1 Lqk_res\n\t"
      "s_waitcnt lgkmcnt(0)\n\t"
      "s_barrier\n\t"
      "Lqk_nan1:\n\t"
      "s_nop 7\n\t"
      "s_waitcnt vmcnt(0) lgkmcnt(0)\n\t"
      "s_mov_b32 s71, s80\n\t"
      "s_add_u32 s71, s71, 1\n\t"
      "s_and_b32 s93, s71, 31\n\t"
      "s_cmp_lg_u32 s93, 31\n\t"
      "s_cbranch_scc1 Lqk_sp2\n\t"
      "s_cmp_lt_u32 s71, 0x8000\n\t"
      "s_cbranch_scc1 Lqk_sp2b\n\t"
      "v_mov_b32 v178, 1\n\t"
      "buffer_store_dword v178, off, s[36:39], s64 sc1\n\t"
      "s_branch Lqk_end\n\t"
      "Lqk_sp2b:\n\t"
      "buffer_load_dword v178, off, s[36:39], s64 sc1\n\t"
      "s_waitcnt vmcnt(0)\n\t"
      "v_readfirstlane_b32 s93, v178\n\t"
      "s_cmp_lg_u32 s93, 0\n\t"
      "s_cbranch_scc1 Lqk_end\n\t"
      "Lqk_sp2:\n\t"
      "s_mov_b32 s80, s71\n\t"
      "s_and_b32 s78, s78, 2\n\t"
      "s_add_u32 s69, s69, s55\n\t"
      "s_cmp_lt_u32 s80, 2\n\t"
      "s_cbranch_scc1 Lqk_nan2\n\t"
      "v_mov_b32 v253, 0\n\t"
      "ds_read_b128 v[178:181], v234 offset:0\n\t"
      "ds_read_b128 v[182:185], v234 offset:1024\n\t"
      "ds_read_b128 v[186:189], v234 offset:2048\n\t"
      "ds_read_b128 v[190:193], v234 offset:3072\n\t"
      "ds_read_b128 v[194:197], v234 offset:4096\n\t"
      "ds_read_b128 v[198:201], v234 offset:5120\n\t"
      "ds_read_b128 v[202:205], v234 offset:6144\n\t"
      "ds_read_b128 v[206:209], v234 offset:7168\n\t"
      "s_waitcnt lgkmcnt(0)\n\t"
      "v_max3_u32 v253, v253, v178, v179\n\t"
      "v_max3_u32 v253, v253, v180, v181\n\t"
      "v_max3_u32 v253, v253, v182, v183\n\t"
      "v_max3_u32 v253, v253, v184, v185\n\t"
      "v_max3_u32 v253, v253, v186, v187\n\t"
      "v_max3_u32 v253, v253, v188, v189\n\t"
      "v_max3_u32 v253, v253, v190, v191\n\t"
      "v_max3_u32 v253, v253, v192, v193\n\t"
      "v_max3_u32 v253, v253, v194, v195\n\t"
      "v_max3_u32 v253, v253, v196, v197\n\t"
      "v_max3_u32 v253, v253, v198, v199\n\t"
      "v_max3_u32 v253, v253, v200, v201\n\t"
      "v_max3_u32 v253, v253, v202, v203\n\t"
      "v_max3_u32 v253, v253, v204, v205\n\t"
      "v_max3_u32 v253, v253, v206, v207\n\t"
      "v_max3_u32 v253, v253, v208, v209\n\t"
      "ds_read_b128 v[178:181], v234 offset:8192\n\t"
      "ds_read_b128 v[182:185], v234 offset:9216\n\t"
      "ds_read_b128 v[186:189], v234 offset:10240\n\t"
      "ds_read_b128 v[190:193], v234 offset:11264\n\t"
      "ds_read_b128 v[194:197], v234 offset:12288\n\t"
      "ds_read_b128 v[198:201], v234 offset:13312\n\t"
      "ds_read_b128 v[202:205], v234 offset:14336\n\t"
      "ds_read_b128 v[206:209], v234 offset:15360\n\t"
      "s_waitcnt lgkmcnt(0)\n\t"
      "v_max3_u32 v253, v253, v178, v179\n\t"
      "v_max3_u32 v253, v253, v180, v181\n\t"
      "v_max3_u32 v253, v253, v182, v183\n\t"
      "v_max3_u32 v253, v253, v184, v185\n\t"
      "v_max3_u32 v253, v253, v186, v187\n\t"
      "v_max3_u32 v253, v253, v188, v189\n\t"
      "v_max3_u32 v253, v253, v190, v191\n\t"
      "v_max3_u32 v253, v253, v192, v193\n\t"
      "v_max3_u32 v253, v253, v194, v195\n\t"
      "v_max3_u32 v253, v253, v196, v197\n\t"
      "v_max3_u32 v253, v253, v198, v199\n\t"
      "v_max3_u32 v253, v253, v200, v201\n\t"
      "v_max3_u32 v253, v253, v202, v203\n\t"
      "v_max3_u32 v253, v253, v204, v205\n\t"
      "v_max3_u32 v253, v253, v206, v207\n\t"
      "v_max3_u32 v253, v253, v208, v209\n\t"
      "v_cmp_eq_u32 vcc, -1, v253\n\t"
      "s_cbranch_vccnz Lqk_nan2\n\t"
      "s_bitset1_b32 s78, 3\n\t"
      "Lqk_nan2:\n\t"
      "v_mov_b32 v209, 0\n\t"
      "s_barrier\n\t"
      "s_branch Lqk_top\n\t"
      "Lqk_first:\n\t"
      "s_bitset0_b32 s78, 1\n\t"
      "s_bitcmp1_b32 s54, 1\n\t"
      "s_cbranch_scc1 Lqk_first3\n\t"
      "s_mov_b32 s71, 0\n\t"
      "Lqk_first1:\n\t"
      "buffer_load_dwordx2 v[210:211], off, s[36:39], s62 sc1\n\t"
      "buffer_load_dwordx2 v[212:213], off, s[36:39], s62 offset:256 sc1\n\t"
      "s_waitcnt vmcnt(0)\n\t"
      "v_cmp_eq_u64 vcc, s[60:61], v[210:211]\n\t"
      "v_cmp_eq_u64 s[94:95], s[60:61], v[212:213]\n\t"
      "s_nop 1\n\t"
      "s_and_b64 vcc, vcc, s[94:95]\n\t"
      "s_cbranch_scc1 Lqk_first2\n\t"
      "s_add_u32 s71, s71, 1\n\t"
      "s_and_b32 s93, s71, 31\n\t"
      "s_cmp_lg_u32 s93, 31\n\t"
      "s_cbranch_scc1 Lqk_sp3\n\t"
      "s_cmp_lt_u32 s71, 0x8000\n\t"
      "s_cbranch_scc1 Lqk_sp3b\n\t"
      "v_mov_b32 v210, 3\n\t"
      "buffer_store_dword v210, off, s[36:39], s64 sc1\n\t"
      "s_branch Lqk_first2\n\t"
      "Lqk_sp3b:\n\t"
      "buffer_load_dword v210, off, s[36:39], s64 sc1\n\t"
      "s_waitcnt vmcnt(0)\n\t"
      "v_readfirstlane_b32 s93, v210\n\t"
      "s_cmp_lg_u32 s93, 0\n\t"
      "s_cbranch_scc1 Lqk_first2\n\t"
      "Lqk_sp3:\n\t"
      "s_sleep 4\n\t"
      "s_branch Lqk_first1\n\t"
      "Lqk_first2:\n\t"
      "s_sub_u32 s63, s63, s77\n\t"
      "s_mov_b32 m0, s63\n\t"
      "s_nop 0\n\t"
      "buffer_load_dwordx4 v242, s[40:43], s90 offen offset:0 sc1 lds\n\t"
      "buffer_load_dwordx4 v242, s[40:43], s90 offen offset:1024 sc1 lds\n\t"
      "s_add_u32 m0, s63, 8192\n\t"
      "s_nop 0\n\t"
      "buffer_load_dwordx4 v243, s[40:43], s91 offen offset:0 sc1 lds\n\t"
      "buffer_load_dwordx4 v243, s[40:43], s91 offen offset:1024 sc1 lds\n\t"
      "s_add_u32 s63, s63, s77\n\t"
      "s_add_u32 m0, s63, 8192\n\t"
      "Lqk_first3:\n\t"
      "s_waitcnt vmcnt(0) lgkmcnt(0)\n\t"
      "s_barrier\n\t"
      "ds_read_b128 v[178:181], v240 offset:0\n\t"
      "ds_read_b128 v[182:185], v240 offset:2048\n\t"
      "s_waitcnt lgkmcnt(0)\n\t"
      "v_mfma_f32_16x16x32_bf16 v[186:189], v[178:181], v[162:165], 0\n\t"
      "v_mfma_f32_16x16x32_bf16 v[186:189], v[182:185], v[166:169], v[186:189]\n\t"
      "s_nop 7\n\t"
      "ds_read_b64_tr_b16 v[178:179], v241 offset:0\n\t"
      "ds_read_b64_tr_b16 v[180:181], v241 offset:64\n\t"
      "ds_read_b64_tr_b16 v[182:183], v241 offset:512\n\t"
      "ds_read_b64_tr_b16 v[184:185], v241 offset:576\n\t"
      "v_add_f32_dpp v253, v188, v186 row_ror:8 row_mask:0xf bank_mask:0x3\n\t"
      "v_add_f32_dpp v253, v187, v189 row_ror:8 row_mask:0xf bank_mask:0xc\n\t"
      "v_fma_f32 v253, v253, s79, -v252\n\t"
      "v_exp_f32 v186, v253\n\t"
      "v_cmp_u_f32 vcc, v253, v253\n\t"
      "s_nop 0\n\t"
      "v_mov_b32_dpp v187, v186 row_ror:8 row_mask:0xf bank_mask:0xf\n\t"
      "v_cvt_pk_bf16_f32 v186, v186, v187\n\t"
      "ds_write_b32 v247, v186\n\t"
      "s_waitcnt lgkmcnt(0)\n\t"
      "s_branch Lqk_first_r\n\t"
      "Lqk_last:\n\t"
      "s_waitcnt lgkmcnt(0)\n\t"
      "s_mov_b64 exec, s[66:67]\n\t"
      "s_bitcmp1_b32 s53, 5\n\t"
      "s_cbranch_scc1 Lqk_kl1\n\t"
      "buffer_store_dwordx4 v[210:213], v234, s[36:39], s87 offen sc1\n\t"
      "s_branch Lqk_kl2\n\t"
      "Lqk_kl1:\n\t"
      "buffer_store_dwordx4 v[214:217], v234, s[36:39], s87 offen sc1\n\t"
      "Lqk_kl2:\n\t"
      "s_mov_b64 exec, -1\n\t"
      "s_branch Lqk_end\n\t"
      "Lqk_bad:\n\t"
      "s_bitset1_b32 s78, 5\n\t"
      "s_mov_b64 s[96:97], vcc\n\t"
      "s_mov_b32 s71, 0\n\t"
      "Lqk_bad1:\n\t"
      "s_waitcnt vmcnt(0)\n\t"
      "v_max_u32 v204, v190, v192\n\t"
      "v_cmp_eq_u32 vcc, -1, v204\n\t"
      "s_cbranch_vccz Lqk_bad2\n\t"
      "s_add_u32 s71, s71, 1\n\t"
      "s_and_b32 s93, s71, 31\n\t"
      "s_cmp_lg_u32 s93, 31\n\t"
      "s_cbranch_scc1 Lqk_sp4\n\t"
      "s_cmp_lt_u32 s71, 0x8000\n\t"
      "s_cbranch_scc1 Lqk_sp4b\n\t"
      "v_mov_b32 v204, 1\n\t"
      "buffer_store_dword v204, off, s[36:39], s64 sc1\n\t"
      "s_branch Lqk_bad2\n\t"
      "Lqk_sp4b:\n\t"
      "buffer_load_dword v204, off, s[36:39], s64 sc1\n\t"
      "s_waitcnt vmcnt(0)\n\t"
      "v_readfirstlane_b32 s93, v204\n\t"
      "s_cmp_lg_u32 s93, 0\n\t"
      "s_cbranch_scc1 Lqk_bad2\n\t"
      "Lqk_sp4:\n\t"
      "s_sleep 2\n\t"
      "buffer_load_dword v190, v249, s[36:39], s86 offen sc1\n\t"
      "buffer_load_dword v192, v249, s[36:39], s86 offen offset:256 sc1\n\t"
      "s_branch Lqk_bad1\n\t"
      "Lqk_bad2:\n\t"
      "s_cmp_eq_u64 s[96:97], 0\n\t"
      "s_cbranch_scc1 Lqk_bad3\n\t"
      "v_max_f32 v202, v253, v205\n\t"
      "s_nop 1\n\t"
      "v_mov_b32_dpp v203, v202 row_ror:8 row_mask:0xf bank_mask:0xf\n\t"
      "v_max_f32 v202, v202, v203\n\t"
      "v_mov_b32 v203, v202\n\t"
      "s_nop 1\n\t"
      "v_permlane16_swap_b32 v202, v203\n\t"
      "v_max_f32 v202, v202, v203\n\t"
      "v_mov_b32 v203, v202\n\t"
      "s_nop 1\n\t"
      "v_permlane32_swap_b32 v202, v203\n\t"
      "v_max_f32 v202, v202, v203\n\t"
      "s_lshr_b32 s93, s53, 3\n\t"
      "v_add_u32 v203, s93, v234\n\t"
      "ds_write_b32 v203, v202 offset:20992\n\t"
      "s_waitcnt lgkmcnt(0)\n\t"
      "s_barrier\n\t"
      "ds_read_b128 v[186:189], v234 offset:22016\n\t"
      "s_waitcnt lgkmcnt(0)\n\t"
      "v_max_f32 v202, v186, v187\n\t"
      "v_max3_f32 v202, v202, v188, v189\n\t"
      "v_sub_f32 v253, v253, v202\n\t"
      "v_sub_f32 v205, v205, v202\n\t"
      "v_add_f32 v252, v252, v202\n\t"
      "v_exp_f32 v186, v253\n\t"
      "s_nop 1\n\t"
      "v_mov_b32_dpp v187, v186 row_ror:8 row_mask:0xf bank_mask:0xf\n\t"
      "v_cvt_pk_bf16_f32 v186, v186, v187\n\t"
      "ds_write_b32 v247, v186\n\t"
      "s_waitcnt lgkmcnt(0)\n\t"
      "s_barrier\n\t"
      "ds_read_b128 v[218:221], v248\n\t"
      "Lqk_bad3:\n\t"
      "s_waitcnt vmcnt(0) lgkmcnt(0)\n\t"
      "v_mfma_f32_16x16x32_bf16 v[226:229], v[178:181], v[218:221], 0\n\t"
      "v_mfma_f32_16x16x32_bf16 v[230:233], v[182:185], v[218:221], 0\n\t"
      "s_nop 1\n\t"
      "v_exp_f32 v202, v205\n\t"
      "v_mfma_f32_16x16x32_bf16 v[170:173], v[198:201], v[218:221], 0\n\t"
      "s_nop 0\n\t"
      "v_max_u32 v204, v190, v192\n\t"
      "v_mov_b32_dpp v203, v202 row_ror:8 row_mask:0xf bank_mask:0xf\n\t"
      "v_cvt_pk_bf16_f32 v194, v202, v203\n\t"
      "v_cmp_eq_u32 s[94:95], -1, v204\n\t"
      "s_nop 0\n\t"
      "v_mfma_f32_16x16x16_bf16 v[170:173], v[198:199], v[194:195], v[170:173]\n\t"
      "v_mfma_f32_16x16x16_bf16 v[226:229], v[190:191], v[194:195], v[226:229]\n\t"
      "v_mfma_f32_16x16x16_bf16 v[230:233], v[192:193], v[194:195], v[230:233]\n\t"
      "v_rcp_f32 v202, v170\n\t"
      "v_subrev_u32 v203, 0x2b800000, v170\n\t"
      "v_cmp_lt_u32 vcc, 0x14000000, v203\n\t"
      "s_nop 0\n\t"
      "v_mul_f32 v226, v226, v202\n\t"
      "v_mul_f32 v227, v227, v202\n\t"
      "v_mul_f32 v228, v228, v202\n\t"
      "v_mul_f32 v229, v229, v202\n\t"
      "v_cvt_pk_bf16_f32 v174, v226, v227\n\t"
      "v_cvt_pk_bf16_f32 v175, v228, v229\n\t"
      "v_mul_f32 v230, v230, v202\n\t"
      "v_mul_f32 v231, v231, v202\n\t"
      "v_mul_f32 v232, v232, v202\n\t"
      "v_mul_f32 v233, v233, v202\n\t"
      "s_or_b64 s[94:95], s[94:95], vcc\n\t"
      "v_cvt_pk_bf16_f32 v176, v230, v231\n\t"
      "v_cvt_pk_bf16_f32 v177, v232, v233\n\t"
      "s_branch Lqk_pub\n\t"
      "Lqk_rec:\n\t"
      "s_mov_b32 s71, 0\n\t"
      "Lqk_rec1:\n\t"
      "buffer_load_dwordx2 v[196:197], off, s[36:39], s85 offset:8 sc1\n\t"
      "s_waitcnt vmcnt(0)\n\t"
      "v_readfirstlane_b32 s99, v196\n\t"
      "v_readfirstlane_b32 s76, v197\n\t"
      "s_max_u32 s98, s99, s76\n\t"
      "s_cmp_lg_u32 s98, -1\n\t"
      "s_cbranch_scc1 Lqk_rec2\n\t"
      "s_add_u32 s71, s71, 1\n\t"
      "s_and_b32 s93, s71, 31\n\t"
      "s_cmp_lg_u32 s93, 31\n\t"
      "s_cbranch_scc1 Lqk_sp5\n\t"
      "s_cmp_lt_u32 s71, 0x8000\n\t"
      "s_cbranch_scc1 Lqk_sp5b\n\t"
      "v_mov_b32 v202, 1\n\t"
      "buffer_store_dword v202, off, s[36:39], s64 sc1\n\t"
      "s_branch Lqk_rec2\n\t"
      "Lqk_sp5b:\n\t"
      "buffer_load_dword v202, off, s[36:39], s64 sc1\n\t"
      "s_waitcnt vmcnt(0)\n\t"
      "v_readfirstlane_b32 s93, v202\n\t"
      "s_cmp_lg_u32 s93, 0\n\t"
      "s_cbranch_scc1 Lqk_rec2\n\t"
      "Lqk_sp5:\n\t"
      "s_sleep 2\n\t"
      "s_branch Lqk_rec1\n\t"
      "Lqk_rec2:\n\t"
      "s_mov_b32 s93, s99\n\t"
      "s_branch Lqk_rec_r\n\t"
      "Lqk_kvl:\n\t"
      "s_mov_b32 s71, 0\n\t"
      "Lqk_kvl1:\n\t"
      "s_mov_b64 exec, 17\n\t"
      "buffer_load_dword v209, v234, s[36:39], s59 offen sc1\n\t"
      "s_mov_b64 exec, -1\n\t"
      "s_waitcnt vmcnt(0)\n\t"
      "v_readlane_b32 s93, v209, 0\n\t"
      "v_readlane_b32 s92, v209, 4\n\t"
      "s_min_u32 s93, s93, s92\n\t"
      "s_max_u32 s70, s70, s93\n\t"
      "s_add_u32 s93, s68, 1\n\t"
      "s_cmp_lt_u32 s93, s70\n\t"
      "s_cbranch_scc1 Lqk_kvl2\n\t"
      "s_add_u32 s71, s71, 1\n\t"
      "s_and_b32 s93, s71, 31\n\t"
      "s_cmp_lg_u32 s93, 31\n\t"
      "s_cbranch_scc1 Lqk_sp6\n\t"
      "s_cmp_lt_u32 s71, 0x8000\n\t"
      "s_cbranch_scc1 Lqk_sp6b\n\t"
      "v_mov_b32 v204, 3\n\t"
      "buffer_store_dword v204, off, s[36:39], s64 sc1\n\t"
      "s_branch Lqk_kvl2\n\t"
      "Lqk_sp6b:\n\t"
      "buffer_load_dword v204, off, s[36:39], s64 sc1\n\t"
      "s_waitcnt vmcnt(0)\n\t"
      "v_readfirstlane_b32 s93, v204\n\t"
      "s_cmp_lg_u32 s93, 0\n\t"
      "s_cbranch_scc1 Lqk_kvl2\n\t"
      "Lqk_sp6:\n\t"
      "s_sleep 8\n\t"
      "s_branch Lqk_kvl1\n\t"
      "Lqk_kvl2:\n\t"
      "s_add_u32 s90, s90, 0x4000\n\t"
      "s_add_u32 s91, s91, 0x4000\n\t"
      "s_branch Lqk_kvl_r\n\t"
      "Lqk_end:\n\t"
      "s_mov_b64 exec, -1\n\t"
      "s_waitcnt vmcnt(0) lgkmcnt(0)\n\t"
      : "+{v[234:249]}"(C), "+{v[250:253]}"(C2), "+{s[60:67]}"(S2), "+{s[72:75]}"(S3)
      : "{a[0:31]}"(A0), "{a[32:63]}"(A1), "{a[64:95]}"(A2), "{a[96:127]}"(A3), "{a[128:159]}"(A4), "{a[160:191]}"(A5), "{a[192:223]}"(A6), "{a[224:255]}"(A7),
        "{v[2:33]}"(V0), "{v[34:65]}"(V1), "{v[66:97]}"(V2), "{v[98:129]}"(V3), "{v[130:161]}"(V4), "{s[36:39]}"(rs_o), "{s[40:43]}"(rs_x), "{s[44:59]}"(S)
      : V162_225, "v226", "v227", "v228", "v229", "v230", "v231", "v232", "v233", "s16", "s17", "s18", "s19", "s32", "s34", "s68", "s69", "s70", "s71", "s76", "s77", "s78", "s79", R8(s, 8), R8(s, 9), "vcc", "scc", "m0", "memory"
      );
    }
  } else {
    const int c = b - NQK, cg = 2 * tr4(c >> 2) + ((c >> 1) & 1), rh = c & 1, vrot = VROT;
    unsigned long long tk1, ep, hv, hf;
    v8u wv[16], wo[32];
#define OHEAD(i) (4 * ((xq == 0 ? 2 - (i) : 3 * xq + (i)) & 7) + w)
    {
      char* const wb_ = Lraw + w * 40960;
      const unsigned wbl = (unsigned)(size_t)Lraw + w * 40960;
      auto issue = [&](int j) {
        char* dst = wb_ + (j & 1) * 16384;
        if (j < 2) { const int r = lane >> 3, h2a = (r >> 1) & 1;
#pragma unroll
          for (int q = 0; q < 16; ++q) {
            const int h2 = h2a | ((q & 1) << 1), ch = (((((lane & 7) >> 1) ^ h2) << 1) | (lane & 1));
            const int row = 64 * mchunk(xq, 4 * ((2 * j + (q >> 3) + vrot) & 3) + w) + 8 * (q & 7) + r;
            __builtin_amdgcn_global_load_lds((const unsigned*)(P.Wv + (size_t)row * HDA + 64 * c + ch * 8), (unsigned*)(dst + q * 1024), 16, 0, NT & 2);
          } }
        else {
#pragma unroll
          for (int q = 0; q < 16; ++q) {
            const int hd = OHEAD(2 * (j - 2) + (q >> 3));
            __builtin_amdgcn_global_load_lds((const unsigned*)(P.Wo + (size_t)(128 * hd + 16 * (q & 7) + (lane >> 2)) * DM + 32 * cg + (lane & 3) * 8), (unsigned*)(dst + q * 1024), 16, 0, (NT >> 1) & 2);
          } } };
      auto qtouch = [&](int s0, int s1) {
        const int nv = 32 - nq, v = slot - nq;
#pragma unroll
        for (int k = 0; k < 5; ++k) {
          const int id = v + k * nv, q = id % nq, i = s0 + id / nq, hq = 12 * xq + q, part = w;
          const int row = 64 * mchunk(xq, 4 * ((i >> 1) & 3) + (part ^ 2)) + lane;
          __builtin_amdgcn_raw_ptr_buffer_load_lds(__builtin_amdgcn_make_buffer_rsrc((void*)((i & 1) ? P.Wk : P.Wq), 0, DM * HDA * 2, 0x00020000),
              (__attribute__((address_space(3))) void*)(wb_ + 32768), 4, i < s1 ? row * (HDA * 2) + hq * 256 + (part < 2 ? 0 : 128) : 0x7ffffff0, 0, 0, 0);
        } };
#define TRR(d, a, o) asm volatile("ds_read_b64_tr_b16 %0, %1 offset:%2" : "=&v"(d) : "v"(a), "i"(o) : "memory")
      const int rd_h2 = ((l15 >> 3) & 1) | ((lq & 1) << 1), qq = l15 >> 2;
      const unsigned rv = wbl + (8 * lq + (l15 >> 2)) * 128 + (l15 & 3) * 8;
      const unsigned ro_ = wbl + (16 * lq + 4 * (qq >> 1) + (qq & 1)) * 64 + (l15 & 3) * 8;
      issue(0); issue(1);
#pragma unroll
      for (int j = 0; j < 6; ++j) {
        const int TJ = QTJ;
        if (j == 5) asm volatile("s_waitcnt vmcnt(0)" ::: "memory");
        else if (j == TJ + 1) asm volatile("s_waitcnt vmcnt(21)" ::: "memory");
        else if (j == TJ) asm volatile("s_waitcnt vmcnt(21)" ::: "memory");
        else asm volatile("s_waitcnt vmcnt(16)" ::: "memory");
#pragma unroll
        for (int hf_ = 0; hf_ < 2; ++hf_) {
          v2u e[4][4];
#pragma unroll
          for (int i4 = 0; i4 < 4; ++i4) {
            const int i = 4 * hf_ + i4;
            if (j < 2) {
              const unsigned a = rv + (j & 1) * 16384 + (i >> 2) * 8192 + (((i & 3) ^ 3 ^ w ^ rd_h2) << 5);
              TRR(e[i4][0], a, 0); TRR(e[i4][1], a, 4 * 128); TRR(e[i4][2], a, 32 * 128); TRR(e[i4][3], a, 36 * 128);
            } else {
              const unsigned a = ro_ + (j & 1) * 16384 + (i >> 2) * 8192 + ((i >> 1) & 1) * 512 + (i & 1) * 32;
              TRR(e[i4][0], a, 0); TRR(e[i4][1], a, 2 * 64); TRR(e[i4][2], a, 64 * 64); TRR(e[i4][3], a, 66 * 64);
            }
          }
          asm volatile("s_waitcnt lgkmcnt(0)" : "+v"(e[0][0]), "+v"(e[0][1]), "+v"(e[0][2]), "+v"(e[0][3]), "+v"(e[1][0]), "+v"(e[1][1]), "+v"(e[1][2]), "+v"(e[1][3]),
                       "+v"(e[2][0]), "+v"(e[2][1]), "+v"(e[2][2]), "+v"(e[2][3]), "+v"(e[3][0]), "+v"(e[3][1]), "+v"(e[3][2]), "+v"(e[3][3]) :: "memory");
#pragma unroll
          for (int i4 = 0; i4 < 4; ++i4) {
            const int i = 4 * hf_ + i4;
            const v8u z = {e[i4][0].x, e[i4][0].y, e[i4][1].x, e[i4][1].y, e[i4][2].x, e[i4][2].y, e[i4][3].x, e[i4][3].y};
            if (j < 2) wv[8 * j + i] = z; else { wo[8 * (j - 2) + i] = z; asm volatile("" : "+a"(wo[8 * (j - 2) + i])); }
          }
          SB0;
        }
        SB0;
        if (j + 2 < 6) issue(j + 2);
        if (j == TJ - 1) qtouch(P.stag2 & 255, (P.stag2 >> 8) & 255);
        if (j == 3) { ep = ld_agent(&P.hdr->epoch[0]); tk1 = ld_agent(&P.hdr->token[0]); hv = ld_agent(&P.hdr->vote[lane]); hf = ld_agent(&P.hdr->fill[lane]); }
        SB0;
      }
#undef TRR
      lds_barrier();
    }
    const bool keeper = (c == 0 && w == 3);
    int* failw = (int*)(Lraw + T0);
    if (tid < 4) failw[tid] = 0;
    tok = uni64(ep) + 1;
    plc = count_votes(hv);
    if (!__all(tk1 == tok && (hv | 3) == 4 * tok + 3 && hf == tok)) { wait_token(P, tok); plc = wait_votes(P, tok, lane, true); }
    v32u WA[8], WV[4];
#pragma unroll
    for (int i = 0; i < 256; ++i) { WA[i >> 5][i & 31] = wo[i >> 3][i & 7]; if (i < 128) WV[i >> 5][i & 31] = wv[i >> 3][i & 7]; }
    const unsigned lds0 = (unsigned)(size_t)Lraw, ob = lds0 + T0 + 1024 + 4096;
    v16u C;
    C[0] = lane * 16; C[1] = (lq * 2 + rh) * 256 + l15 * 16; C[2] = idx; C[3] = lds0 + w * 512 + lane * 8; C[4] = lds0 + (w ^ 3) * 512 + lane * 8;
    C[5] = ob + w * 576 + lq * 136 + l15 * 8 - (lane >> 5) * 4096; C[6] = ob + lq * 136 + ((lane & 3) * 4 + ((lane >> 3) & 1) * 2) * 8 + ((lane >> 2) & 1) * 4;
    C[7] = quad(c * 1024) + w * 256 + lane * 4; C[8] = quad((c >> 1) * 512) + (lane >> 5) * 256 + rh * 128 + (lane & 31) * 4; C[9] = (slot == nq && tid == 0) ? 144 + 4 * xq : 0x7ffffff0;
    C[10] = lds0 + T0; C[11] = C[6] + 1152; C[12] = lds0 + 2048 + (w ^ 2) * 512 + lane * 8; C[13] = lds0 + 4096 + (w ^ 1) * 512 + lane * 8; C[14] = C[15] = 0;
    v16u S;
#pragma unroll
    for (int i = 0; i < 8; ++i) { S[i] = quad(OHEAD(i) * 2048); if (i < 4) S[8 + i] = quad(mpos(xq, 4 * ((i + vrot) & 3) + w) * 1024); }
    { const unsigned long long pl2 = __ballot((sideo(plc, (lane >> 1) << 21, xq != 1) >> (13 + (lane & 1))) & 1);
      S[12] = (unsigned)pl2; S[13] = (unsigned)(pl2 >> 32); S[14] = nearo(plc, RC0); S[15] = (hside(c >> 1) ^ (xq != 1)) << 14; }
#pragma unroll
    for (int k = 0; k < 16; ++k) S[k] = __builtin_amdgcn_readfirstlane(S[k]);
    v8u Q = {(unsigned)P.pad2, (unsigned)P.stag, w == 0 ? 2u : 10u, CBASE + (unsigned)offsetof(Ctl, tmo), (unsigned)P.sh1, (unsigned)P.dv,
             (unsigned)((char*)prof - (char*)obuf) + (6u << 17) + (c * 4 + w) * 32, keeper ? (unsigned)((P.dmv + P.pad) | (P.dov + (P.pad >> 2)) << 10 | P.rec << 20) : 0u};
#pragma unroll
    for (int k = 0; k < 8; ++k) Q[k] = __builtin_amdgcn_readfirstlane(Q[k]);
    lds_barrier();
      asm volatile(
      "s_mov_b32 s72, 0\n\t"
      "s_mov_b32 s73, 0\n\t"
      "s_mov_b32 s98, 0\n\t"
      "s_mov_b32 s57, 0\n\t"
      "s_mov_b32 s58, 0\n\t"
      "s_mov_b32 s60, 0\n\t"
      "s_mov_b32 s61, 0\n\t"
      "s_mov_b32 s63, 0\n\t"
      "s_mov_b32 s99, 0\n\t"
      "s_mov_b32 s56, 2\n\t"
      "s_mov_b32 s59, 5888\n\t"
      "s_mov_b32 s62, 1000\n\t"
      "Lvo_top:\n\t"
      "s_lshl_b32 s82, s72, 18\n\t"
      "s_add_u32 s82, s82, 0x3c0000\n\t"
      "s_lshr_b32 s76, s82, 20\n\t"
      "s_and_b32 s76, s76, 62\n\t"
      "s_lshr_b64 s[80:81], s[52:53], s76\n\t"
      "s_bfe_u32 s76, s82, 0x10013\n\t"
      "s_lshl1_add_u32 s80, s76, s80\n\t"
      "s_and_b32 s80, s80, 3\n\t"
      "s_lshl_b32 s80, s80, 13\n\t"
      "s_or_b32 s82, s82, s80\n\t"
      "s_lshl_b32 s87, s72, 16\n\t"
      "s_lshr_b32 s76, s87, 20\n\t"
      "s_and_b32 s76, s76, 62\n\t"
      "s_lshr_b64 s[80:81], s[52:53], s76\n\t"
      "s_bfe_u32 s76, s87, 0x10013\n\t"
      "s_lshl1_add_u32 s80, s76, s80\n\t"
      "s_and_b32 s80, s80, 3\n\t"
      "s_lshl_b32 s80, s80, 13\n\t"
      "s_or_b32 s87, s87, s80\n\t"
      "s_lshl_b32 s86, s72, 18\n\t"
      "s_add_u32 s86, s86, 0x1400000\n\t"
      "s_lshr_b32 s76, s86, 20\n\t"
      "s_and_b32 s76, s76, 62\n\t"
      "s_lshr_b64 s[80:81], s[52:53], s76\n\t"
      "s_bfe_u32 s76, s86, 0x10013\n\t"
      "s_lshl1_add_u32 s80, s76, s80\n\t"
      "s_and_b32 s80, s80, 3\n\t"
      "s_lshl_b32 s80, s80, 13\n\t"
      "s_or_b32 s86, s86, s80\n\t"
      "s_lshl_b32 s84, s72, 8\n\t"
      "s_and_b32 s76, s84, 0x2000\n\t"
      "s_add_u32 s84, s84, s76\n\t"
      "s_add_u32 s84, s84, s54\n\t"
      "s_xor_b32 s88, s87, 0x4000\n\t"
      "s_xor_b32 s86, s86, s55\n\t"
      "s_add_u32 s83, s82, s40\n\t"
      "s_add_u32 s89, s87, s48\n\t"
      "s_add_u32 s90, s87, s49\n\t"
      "s_add_u32 s91, s87, s50\n\t"
      "s_add_u32 s92, s87, s51\n\t"
      "s_memrealtime s[80:81]\n\t"
      "v_mov_b64 v[162:163], 0\n\t"
      "v_mov_b64 v[164:165], 0\n\t"
      "v_mov_b64 v[166:167], 0\n\t"
      "v_mov_b64 v[168:169], 0\n\t"
      "v_mov_b64 v[170:171], 0\n\t"
      "v_mov_b64 v[172:173], 0\n\t"
      "v_mov_b64 v[174:175], 0\n\t"
      "v_mov_b64 v[176:177], 0\n\t"
      "v_mov_b64 v[178:179], 0\n\t"
      "v_mov_b64 v[180:181], 0\n\t"
      "v_mov_b64 v[182:183], 0\n\t"
      "v_mov_b64 v[184:185], 0\n\t"
      "v_mov_b64 v[186:187], 0\n\t"
      "v_mov_b64 v[188:189], 0\n\t"
      "v_mov_b64 v[190:191], 0\n\t"
      "v_mov_b64 v[192:193], 0\n\t"
      "s_waitcnt lgkmcnt(0)\n\t"
      "s_and_b32 s76, s56, 3\n\t"
      "s_cbranch_scc1 Lvo_entry\n\t"
      "s_sub_u32 s77, s57, s80\n\t"
      "s_cmp_gt_i32 s77, 20000\n\t"
      "s_cbranch_scc1 Lvo_o\n\t"
      "s_sub_u32 s76, s60, s62\n\t"
      "s_add_u32 s80, s76, 199\n\t"
      "s_ashr_i32 s76, s76, 1\n\t"
      "s_cmp_lt_u32 s80, 399\n\t"
      "s_cselect_b32 s76, s76, 0\n\t"
      "s_add_u32 s59, s59, s76\n\t"
      "s_sub_u32 s76, s57, s61\n\t"
      "s_mul_i32 s76, s76, s59\n\t"
      "s_lshr_b32 s76, s76, 8\n\t"
      "s_add_u32 s62, s76, s60\n\t"
      "s_add_u32 s60, s62, 10\n\t"
      "s_mov_b32 s61, s57\n\t"
      "s_cmp_lt_i32 s77, 16\n\t"
      "s_cselect_b32 s76, 1000, 0\n\t"
      "s_add_u32 s62, s62, s76\n\t"
      "s_lshr_b32 s76, s59, 6\n\t"
      "Lvo_slp:\n\t"
      "s_cmp_lt_i32 s77, 57\n\t"
      "s_cbranch_scc1 Lvo_fo\n\t"
      "s_sleep 2\n\t"
      "s_memrealtime s[80:81]\n\t"
      "s_waitcnt lgkmcnt(0)\n\t"
      "s_sub_u32 s77, s57, s80\n\t"
      "s_branch Lvo_slp\n\t"
      ".p2align 6\n\t"
      ".rept 2\n\t"
      "s_nop 0\n\t"
      ".endr\n\t"
      "Lvo_fo:\n\t"
      "s_memrealtime vcc\n\t"
      "s_memtime s[80:81]\n\t"
      "s_waitcnt lgkmcnt(0)\n\t"
      "Lvo_fo2:\n\t"
      "s_sub_i32 vcc_lo, s57, vcc_lo\n\t"
      "s_mul_i32 vcc_lo, vcc_lo, s59\n\t"
      "s_ashr_i32 vcc_lo, vcc_lo, 8\n\t"
      "s_add_i32 vcc_lo, vcc_lo, s80\n\t"
      "s_sub_i32 vcc_hi, vcc_lo, s60\n\t"
      "s_cmp_gt_u32 vcc_hi, s76\n\t"
      "s_cselect_b32 s60, vcc_lo, s60\n\t"
      "s_sub_i32 s80, s60, s80\n\t"
      "s_cmpk_gt_i32 s80, 230\n\t"
      "s_cbranch_scc1 Lvo_fo\n\t"
      "s_sub_i32 s80, s80, s64\n\t"
      "s_max_i32 s80, s80, 0\n\t"
      "s_min_i32 s80, s80, 192\n\t"
      "s_and_b32 s80, s80, -4\n\t"
      "s_sub_i32 s80, 204, s80\n\t"
      "s_getpc_b64 vcc\n\t"
      "s_add_u32 vcc_lo, vcc_lo, s80\n\t"
      "s_addc_u32 vcc_hi, vcc_hi, 0\n\t"
      "s_setpc_b64 vcc\n\t"
      ".rept 48\n\t"
      "s_nop 0\n\t"
      ".endr\n\t"
      "Lvo_o:\n\t"
      "buffer_load_dwordx4 v[130:133], v195, s[36:39], s83 offen sc1\n\t"
      "s_add_u32 s83, s82, s41\n\t"
      "s_add_u32 s77, s58, s65\n\t"
      "s_sub_u32 s78, s77, s61\n\t"
      "s_mul_i32 s78, s78, s59\n\t"
      "s_lshr_b32 s78, s78, 8\n\t"
      "s_add_u32 s78, s78, s60\n\t"
      "s_lshr_b32 s76, s59, 6\n\t"
      "buffer_load_dwordx4 v[134:137], v195, s[36:39], s83 offen sc1\n\t"
      "s_add_u32 s83, s82, s42\n\t"
      "s_nop 7\n\t"
      "buffer_load_dwordx4 v[138:141], v195, s[36:39], s83 offen sc1\n\t"
      "s_add_u32 s83, s82, s43\n\t"
      "s_nop 7\n\t"
      "buffer_load_dwordx4 v[142:145], v195, s[36:39], s83 offen sc1\n\t"
      "s_add_u32 s83, s82, s44\n\t"
      "s_nop 7\n\t"
      "buffer_load_dwordx4 v[146:149], v195, s[36:39], s83 offen sc1\n\t"
      "s_add_u32 s83, s82, s45\n\t"
      "s_nop 7\n\t"
      "buffer_load_dwordx4 v[150:153], v195, s[36:39], s83 offen sc1\n\t"
      "s_add_u32 s83, s82, s46\n\t"
      "s_nop 7\n\t"
      "buffer_load_dwordx4 v[154:157], v195, s[36:39], s83 offen sc1\n\t"
      "s_add_u32 s83, s82, s47\n\t"
      "s_nop 7\n\t"
      "buffer_load_dwordx4 v[158:161], v195, s[36:39], s83 offen sc1\n\t"
      "s_waitcnt vmcnt(7)\n\t"
      "v_smfmac_f32_16x16x64_bf16 v[166:169], v[130:133], a[8:15], v196\n\t"
      "v_smfmac_f32_16x16x64_bf16 v[170:173], v[130:133], a[16:23], v196\n\t"
      "v_smfmac_f32_16x16x64_bf16 v[162:165], v[130:133], a[0:7], v196\n\t"
      "v_smfmac_f32_16x16x64_bf16 v[174:177], v[130:133], a[24:31], v196\n\t"
      "s_waitcnt vmcnt(6)\n\t"
      "v_smfmac_f32_16x16x64_bf16 v[166:169], v[134:137], a[40:47], v196\n\t"
      "v_smfmac_f32_16x16x64_bf16 v[170:173], v[134:137], a[48:55], v196\n\t"
      "v_smfmac_f32_16x16x64_bf16 v[162:165], v[134:137], a[32:39], v196\n\t"
      "v_smfmac_f32_16x16x64_bf16 v[174:177], v[134:137], a[56:63], v196\n\t"
      "s_waitcnt vmcnt(5)\n\t"
      "v_smfmac_f32_16x16x64_bf16 v[166:169], v[138:141], a[72:79], v196\n\t"
      "v_smfmac_f32_16x16x64_bf16 v[170:173], v[138:141], a[80:87], v196\n\t"
      "v_smfmac_f32_16x16x64_bf16 v[162:165], v[138:141], a[64:71], v196\n\t"
      "v_smfmac_f32_16x16x64_bf16 v[174:177], v[138:141], a[88:95], v196\n\t"
      "s_waitcnt vmcnt(4)\n\t"
      "v_smfmac_f32_16x16x64_bf16 v[166:169], v[142:145], a[104:111], v196\n\t"
      "v_smfmac_f32_16x16x64_bf16 v[170:173], v[142:145], a[112:119], v196\n\t"
      "v_smfmac_f32_16x16x64_bf16 v[162:165], v[142:145], a[96:103], v196\n\t"
      "v_smfmac_f32_16x16x64_bf16 v[174:177], v[142:145], a[120:127], v196\n\t"
      "s_waitcnt vmcnt(3)\n\t"
      "v_smfmac_f32_16x16x64_bf16 v[166:169], v[146:149], a[136:143], v196\n\t"
      "v_smfmac_f32_16x16x64_bf16 v[170:173], v[146:149], a[144:151], v196\n\t"
      "v_smfmac_f32_16x16x64_bf16 v[162:165], v[146:149], a[128:135], v196\n\t"
      "v_smfmac_f32_16x16x64_bf16 v[174:177], v[146:149], a[152:159], v196\n\t"
      "s_waitcnt vmcnt(2)\n\t"
      "v_smfmac_f32_16x16x64_bf16 v[166:169], v[150:153], a[168:175], v196\n\t"
      "v_smfmac_f32_16x16x64_bf16 v[170:173], v[150:153], a[176:183], v196\n\t"
      "v_smfmac_f32_16x16x64_bf16 v[162:165], v[150:153], a[160:167], v196\n\t"
      "v_smfmac_f32_16x16x64_bf16 v[174:177], v[150:153], a[184:191], v196\n\t"
      "s_waitcnt vmcnt(1)\n\t"
      "v_smfmac_f32_16x16x64_bf16 v[166:169], v[154:157], a[200:207], v196\n\t"
      "v_smfmac_f32_16x16x64_bf16 v[170:173], v[154:157], a[208:215], v196\n\t"
      "v_smfmac_f32_16x16x64_bf16 v[162:165], v[154:157], a[192:199], v196\n\t"
      "v_smfmac_f32_16x16x64_bf16 v[174:177], v[154:157], a[216:223], v196\n\t"
      "s_waitcnt vmcnt(0)\n\t"
      "v_smfmac_f32_16x16x64_bf16 v[166:169], v[158:161], a[232:239], v196\n\t"
      "v_smfmac_f32_16x16x64_bf16 v[170:173], v[158:161], a[240:247], v196\n\t"
      "v_smfmac_f32_16x16x64_bf16 v[162:165], v[158:161], a[224:231], v196\n\t"
      "v_add_f32 v166, v166, v168\n\t"
      "v_add_f32 v167, v167, v169\n\t"
      "v_smfmac_f32_16x16x64_bf16 v[174:177], v[158:161], a[248:255], v196\n\t"
      "v_add_f32 v170, v170, v172\n\t"
      "v_add_f32 v171, v171, v173\n\t"
      "v_cmp_u_f32 vcc, v166, v167\n\t"
      "s_nop 0\n\t"
      "v_permlane32_swap_b32 v170, v166\n\t"
      "v_permlane32_swap_b32 v171, v167\n\t"
      "v_add_f32 v162, v162, v164\n\t"
      "v_add_f32 v163, v163, v165\n\t"
      "v_add_f32 v162, v162, v166\n\t"
      "v_add_f32 v163, v163, v167\n\t"
      "v_add_f32 v174, v174, v176\n\t"
      "v_add_f32 v175, v175, v177\n\t"
      "v_add_f32 v174, v174, v170\n\t"
      "v_add_f32 v175, v175, v171\n\t"
      "ds_write2st64_b64 v199, v[162:163], v[174:175] offset1:8\n\t"
      "s_cbranch_vccnz Lvo_onan\n\t"
      "Lvo_ores:\n\t"
      "s_cmp_lg_u32 s66, 2\n\t"
      "s_waitcnt lgkmcnt(0)\n\t"
      "s_barrier\n\t"
      "s_cbranch_scc1 Lvo_np\n\t"
      "ds_read2_b32 v[210:211], v200 offset0:0 offset1:2\n\t"
      "ds_read2_b32 v[212:213], v200 offset0:144 offset1:146\n\t"
      "ds_read2_b32 v[214:215], v205 offset0:0 offset1:2\n\t"
      "ds_read2_b32 v[216:217], v205 offset0:144 offset1:146\n\t"
      "s_waitcnt lgkmcnt(2)\n\t"
      "v_add_f32 v210, v210, v212\n\t"
      "v_add_f32 v211, v211, v213\n\t"
      "s_waitcnt lgkmcnt(0)\n\t"
      "v_add_f32 v214, v214, v216\n\t"
      "v_add_f32 v215, v215, v217\n\t"
      "v_add_f32 v210, v210, v214\n\t"
      "v_add_f32 v211, v211, v215\n\t"
      "v_cvt_pk_bf16_f32 v210, v210, v211\n\t"
      "buffer_store_dword v210, v202, s[36:39], s87 offen sc1\n\t"
      "buffer_store_dword v210, v202, s[36:39], s88 offen sc1\n\t"
      "Lvo_np:\n\t"
      "s_memrealtime vcc\n\t"
      "s_memtime s[80:81]\n\t"
      "s_waitcnt lgkmcnt(0)\n\t"
      "s_mov_b32 s63, vcc_lo\n\t"
      "s_add_u32 vcc_hi, vcc_lo, s66\n\t"
      "s_sub_u32 vcc_hi, vcc_hi, s58\n\t"
      "s_cmp_gt_i32 vcc_hi, 0\n\t"
      "s_cbranch_scc1 Lvo_late\n\t"
      "Lvo_fv2:\n\t"
      "s_sub_i32 vcc_lo, s77, vcc_lo\n\t"
      "s_mul_i32 vcc_lo, vcc_lo, s59\n\t"
      "s_ashr_i32 vcc_lo, vcc_lo, 8\n\t"
      "s_add_i32 vcc_lo, vcc_lo, s80\n\t"
      "s_sub_i32 vcc_hi, vcc_lo, s78\n\t"
      "s_cmp_gt_u32 vcc_hi, s76\n\t"
      "s_cselect_b32 s78, vcc_lo, s78\n\t"
      "s_sub_i32 s80, s78, s80\n\t"
      "s_cmpk_gt_i32 s80, 230\n\t"
      "s_cbranch_scc1 Lvo_fv\n\t"
      "s_sub_i32 s80, s80, s64\n\t"
      "s_max_i32 s80, s80, 0\n\t"
      "s_min_i32 s80, s80, 192\n\t"
      "s_and_b32 s80, s80, -4\n\t"
      "s_sub_i32 s80, 204, s80\n\t"
      "s_getpc_b64 vcc\n\t"
      "s_add_u32 vcc_lo, vcc_lo, s80\n\t"
      "s_addc_u32 vcc_hi, vcc_hi, 0\n\t"
      "s_setpc_b64 vcc\n\t"
      ".rept 48\n\t"
      "s_nop 0\n\t"
      ".endr\n\t"
      "Lvo_v:\n\t"
      "buffer_load_dwordx4 v[130:133], v194, s[36:39], s89 offen sc1\n\t"
      "s_sub_u32 s76, s63, s57\n\t"
      "s_and_b32 s76, s76, 0xffff\n\t"
      "v_mov_b32 v218, s76\n\t"
      "s_nop 3\n\t"
      "buffer_load_dwordx4 v[134:137], v194, s[36:39], s90 offen sc1\n\t"
      "s_nop 7\n\t"
      "buffer_load_dwordx4 v[138:141], v194, s[36:39], s91 offen sc1\n\t"
      "s_nop 7\n\t"
      "buffer_load_dwordx4 v[142:145], v194, s[36:39], s92 offen sc1\n\t"
      "s_waitcnt vmcnt(3)\n\t"
      "v_smfmac_f32_16x16x64_bf16 v[178:181], v[130:133], v[2:9], v196\n\t"
      "v_smfmac_f32_16x16x64_bf16 v[182:185], v[130:133], v[10:17], v196\n\t"
      "buffer_load_dwordx2 v[220:221], off, s[36:39], s84 offset:8 sc1\n\t"
      "v_smfmac_f32_16x16x64_bf16 v[186:189], v[130:133], v[18:25], v196\n\t"
      "buffer_store_dword v218, v203, s[36:39], s84 offen sc1\n\t"
      "v_smfmac_f32_16x16x64_bf16 v[190:193], v[130:133], v[26:33], v196\n\t"
      "s_waitcnt vmcnt(4)\n\t"
      "v_smfmac_f32_16x16x64_bf16 v[178:181], v[134:137], v[34:41], v196\n\t"
      "v_smfmac_f32_16x16x64_bf16 v[182:185], v[134:137], v[42:49], v196\n\t"
      "v_smfmac_f32_16x16x64_bf16 v[186:189], v[134:137], v[50:57], v196\n\t"
      "v_smfmac_f32_16x16x64_bf16 v[190:193], v[134:137], v[58:65], v196\n\t"
      "s_waitcnt vmcnt(3)\n\t"
      "v_smfmac_f32_16x16x64_bf16 v[178:181], v[138:141], v[66:73], v196\n\t"
      "v_smfmac_f32_16x16x64_bf16 v[182:185], v[138:141], v[74:81], v196\n\t"
      "v_smfmac_f32_16x16x64_bf16 v[186:189], v[138:141], v[82:89], v196\n\t"
      "v_smfmac_f32_16x16x64_bf16 v[190:193], v[138:141], v[90:97], v196\n\t"
      "s_waitcnt vmcnt(2)\n\t"
      "v_smfmac_f32_16x16x64_bf16 v[178:181], v[142:145], v[98:105], v196\n\t"
      "v_smfmac_f32_16x16x64_bf16 v[182:185], v[142:145], v[106:113], v196\n\t"
      "v_smfmac_f32_16x16x64_bf16 v[186:189], v[142:145], v[114:121], v196\n\t"
      "v_add_f32 v178, v178, v180\n\t"
      "v_add_f32 v179, v179, v181\n\t"
      "v_smfmac_f32_16x16x64_bf16 v[190:193], v[142:145], v[122:129], v196\n\t"
      "ds_write_b64 v197, v[178:179] offset:0\n\t"
      "v_add_f32 v182, v182, v184\n\t"
      "v_add_f32 v183, v183, v185\n\t"
      "ds_write_b64 v197, v[182:183] offset:2048\n\t"
      "v_cmp_u_f32 vcc, v178, v179\n\t"
      "v_add_f32 v186, v186, v188\n\t"
      "v_add_f32 v187, v187, v189\n\t"
      "ds_write_b64 v197, v[186:187] offset:4096\n\t"
      "s_nop 0\n\t"
      "v_add_f32 v190, v190, v192\n\t"
      "v_add_f32 v191, v191, v193\n\t"
      "s_cbranch_vccnz Lvo_vnan\n\t"
      "Lvo_vres:\n\t"
      "s_waitcnt lgkmcnt(0)\n\t"
      "s_barrier\n\t"
      "ds_read_b64 v[210:211], v198\n\t"
      "ds_read_b64 v[212:213], v206\n\t"
      "ds_read_b64 v[214:215], v207\n\t"
      "ds_read_b32 v222, v204\n\t"
      "s_waitcnt lgkmcnt(3)\n\t"
      "v_add_f32 v210, v210, v190\n\t"
      "v_add_f32 v211, v211, v191\n\t"
      "s_waitcnt lgkmcnt(1)\n\t"
      "v_add_f32 v212, v212, v214\n\t"
      "v_add_f32 v213, v213, v215\n\t"
      "v_add_f32 v210, v210, v212\n\t"
      "v_add_f32 v211, v211, v213\n\t"
      "v_cvt_pk_bf16_f32 v210, v210, v211\n\t"
      "buffer_store_dword v210, v201, s[36:39], s86 offen sc1\n\t"
      "s_waitcnt lgkmcnt(0)\n\t"
      "s_memrealtime s[74:75]\n\t"
      "v_readfirstlane_b32 s95, v222\n\t"
      "s_waitcnt vmcnt(2)\n\t"
      "v_readfirstlane_b32 s93, v220\n\t"
      "v_readfirstlane_b32 s94, v221\n\t"
      "s_add_u32 s72, s72, 1\n\t"
      "s_max_u32 s76, s93, s94\n\t"
      "s_cmp_eq_u32 s76, -1\n\t"
      "s_cbranch_scc1 Lvo_rec\n\t"
      "Lvo_rec_r:\n\t"
      "s_waitcnt lgkmcnt(0)\n\t"
      "s_lshl_b32 s76, s72, 16\n\t"
      "s_sub_u32 s76, s95, s76\n\t"
      "s_sub_u32 s76, s76, s73\n\t"
      "s_max_i32 s76, s76, 0\n\t"
      "s_mul_i32 s76, s76, s68\n\t"
      "s_add_u32 s57, s58, s76\n\t"
      "s_add_u32 s57, s57, s93\n\t"
      "s_add_u32 s58, s57, s94\n\t"
      "s_mov_b32 s73, 0\n\t"
      "s_mov_b32 s98, 0\n\t"
      "s_mov_b32 s99, 0\n\t"
      "s_cmp_eq_u32 s72, 1\n\t"
      "s_cselect_b32 s56, 1, 0\n\t"
      "s_cselect_b32 s58, s74, s58\n\t"
      "s_cmp_lt_u32 s72, 64\n\t"
      "s_cbranch_scc1 Lvo_top\n\t"
      "s_branch Lvo_end\n\t"
      "Lvo_entry:\n\t"
      "s_cmp_eq_u32 s76, 1\n\t"
      "s_cbranch_scc1 Lvo_o\n\t"
      "s_branch Lvo_v\n\t"
      "Lvo_late:\n\t"
      "s_cmp_eq_u32 s72, 1\n\t"
      "s_cbranch_scc1 Lvo_one\n\t"
      "Lvo_late1:\n\t"
      "s_add_u32 s58, s58, s68\n\t"
      "s_add_u32 s73, s73, 1\n\t"
      "s_sub_u32 vcc_hi, vcc_hi, s68\n\t"
      "s_cmp_gt_i32 vcc_hi, 0\n\t"
      "s_cbranch_scc1 Lvo_late1\n\t"
      "Lvo_shift:\n\t"
      "s_add_u32 s76, s72, 1\n\t"
      "s_lshl_b32 s76, s76, 16\n\t"
      "s_add_u32 s76, s76, s73\n\t"
      "v_mov_b32 v210, s76\n\t"
      "s_mov_b64 exec, 1\n\t"
      "ds_max_i32 v204, v210\n\t"
      "s_mov_b64 exec, -1\n\t"
      "s_add_u32 s79, s58, s65\n\t"
      "s_cmp_lt_u32 s72, 2\n\t"
      "s_cbranch_scc1 Lvo_cwv\n\t"
      "s_add_u32 s77, s58, s65\n\t"
      "s_sub_u32 s78, s77, s61\n\t"
      "s_mul_i32 s78, s78, s59\n\t"
      "s_lshr_b32 s78, s78, 8\n\t"
      "s_add_u32 s78, s78, s60\n\t"
      "s_lshr_b32 s76, s59, 6\n\t"
      "Lvo_fv:\n\t"
      "s_memrealtime vcc\n\t"
      "s_memtime s[80:81]\n\t"
      "s_waitcnt lgkmcnt(0)\n\t"
      "s_sub_u32 vcc_hi, s77, vcc_lo\n\t"
      "s_cmp_gt_i32 vcc_hi, 20000\n\t"
      "s_cbranch_scc1 Lvo_v\n\t"
      "s_branch Lvo_fv2\n\t"
      "Lvo_cwv:\n\t"
      "s_memrealtime s[80:81]\n\t"
      "s_waitcnt lgkmcnt(0)\n\t"
      "s_sub_u32 s80, s79, s80\n\t"
      "s_sub_u32 s80, s80, 1\n\t"
      "s_cmp_lt_u32 s80, 20000\n\t"
      "s_cbranch_scc1 Lvo_cwv\n\t"
      "s_branch Lvo_v\n\t"
      "Lvo_onan:\n\t"
      "v_max_u32 v210, v130, v131\n\t"
      "v_max3_u32 v210, v210, v132, v133\n\t"
      "v_max3_u32 v210, v210, v134, v135\n\t"
      "v_max3_u32 v210, v210, v136, v137\n\t"
      "v_max3_u32 v210, v210, v138, v139\n\t"
      "v_max3_u32 v210, v210, v140, v141\n\t"
      "v_max3_u32 v210, v210, v142, v143\n\t"
      "v_max3_u32 v210, v210, v144, v145\n\t"
      "v_max3_u32 v210, v210, v146, v147\n\t"
      "v_max3_u32 v210, v210, v148, v149\n\t"
      "v_max3_u32 v210, v210, v150, v151\n\t"
      "v_max3_u32 v210, v210, v152, v153\n\t"
      "v_max3_u32 v210, v210, v154, v155\n\t"
      "v_max3_u32 v210, v210, v156, v157\n\t"
      "v_max3_u32 v210, v210, v158, v159\n\t"
      "v_max3_u32 v210, v210, v160, v161\n\t"
      "v_cmp_eq_u32 vcc, -1, v210\n\t"
      "s_cbranch_vccz Lvo_ores\n\t"
      "s_mov_b32 s97, s98\n\t"
      "s_mov_b32 s79, s76\n\t"
      "s_add_u32 s97, s97, 1\n\t"
      "s_and_b32 s76, s97, 31\n\t"
      "s_cmp_lg_u32 s76, 31\n\t"
      "s_cbranch_scc1 Lvo_sp7\n\t"
      "s_cmp_lt_u32 s97, 0x8000\n\t"
      "s_cbranch_scc1 Lvo_sp7b\n\t"
      "v_mov_b32 v210, 1\n\t"
      "buffer_store_dword v210, off, s[36:39], s67 sc1\n\t"
      "s_branch Lvo_odead\n\t"
      "Lvo_sp7b:\n\t"
      "buffer_load_dword v210, off, s[36:39], s67 sc1\n\t"
      "s_waitcnt vmcnt(0)\n\t"
      "v_readfirstlane_b32 s76, v210\n\t"
      "s_cmp_lg_u32 s76, 0\n\t"
      "s_cbranch_scc1 Lvo_odead\n\t"
      "Lvo_sp7:\n\t"
      "s_mov_b32 s98, s97\n\t"
      "v_mov_b64 v[162:163], 0\n\t"
      "v_mov_b64 v[164:165], 0\n\t"
      "v_mov_b64 v[166:167], 0\n\t"
      "v_mov_b64 v[168:169], 0\n\t"
      "v_mov_b64 v[170:171], 0\n\t"
      "v_mov_b64 v[172:173], 0\n\t"
      "v_mov_b64 v[174:175], 0\n\t"
      "v_mov_b64 v[176:177], 0\n\t"
      "s_cmp_eq_u32 s72, 1\n\t"
      "s_cbranch_scc1 Lvo_opoll\n\t"
      "s_add_u32 s83, s82, s40\n\t"
      "s_mul_i32 s79, s98, s68\n\t"
      "s_add_u32 s79, s79, s57\n\t"
      "s_sub_u32 s79, s79, s69\n\t"
      "Lvo_cwo:\n\t"
      "s_memrealtime s[80:81]\n\t"
      "s_waitcnt lgkmcnt(0)\n\t"
      "s_sub_u32 s80, s79, s80\n\t"
      "s_sub_u32 s80, s80, 1\n\t"
      "s_cmp_lt_u32 s80, 20000\n\t"
      "s_cbranch_scc1 Lvo_cwo\n\t"
      "s_branch Lvo_o\n\t"
      "Lvo_opoll:\n\t"
      "s_mov_b32 s97, 0\n\t"
      "Lvo_opoll1:\n\t"
      "s_sleep 4\n\t"
      "buffer_load_dwordx4 v[130:133], v195, s[36:39], s83 offen sc1\n\t"
      "s_waitcnt vmcnt(0)\n\t"
      "v_max_u32 v210, v130, v131\n\t"
      "v_max3_u32 v210, v210, v132, v133\n\t"
      "v_cmp_eq_u32 vcc, -1, v210\n\t"
      "s_cbranch_vccz Lvo_opoll2\n\t"
      "s_add_u32 s97, s97, 1\n\t"
      "s_and_b32 s76, s97, 31\n\t"
      "s_cmp_lg_u32 s76, 31\n\t"
      "s_cbranch_scc1 Lvo_sp8\n\t"
      "s_cmp_lt_u32 s97, 0x8000\n\t"
      "s_cbranch_scc1 Lvo_sp8b\n\t"
      "v_mov_b32 v210, 1\n\t"
      "buffer_store_dword v210, off, s[36:39], s67 sc1\n\t"
      "s_branch Lvo_opoll2\n\t"
      "Lvo_sp8b:\n\t"
      "buffer_load_dword v210, off, s[36:39], s67 sc1\n\t"
      "s_waitcnt vmcnt(0)\n\t"
      "v_readfirstlane_b32 s76, v210\n\t"
      "s_cmp_lg_u32 s76, 0\n\t"
      "s_cbranch_scc1 Lvo_opoll2\n\t"
      "Lvo_sp8:\n\t"
      "s_branch Lvo_opoll1\n\t"
      "Lvo_opoll2:\n\t"
      "s_add_u32 s83, s82, s40\n\t"
      "s_branch Lvo_o\n\t"
      "Lvo_odead:\n\t"
      "s_mov_b32 s76, s79\n\t"
      "s_branch Lvo_ores\n\t"
      "Lvo_vnan:\n\t"
      "v_max_u32 v210, v130, v131\n\t"
      "v_max3_u32 v210, v210, v132, v133\n\t"
      "v_max3_u32 v210, v210, v134, v135\n\t"
      "v_max3_u32 v210, v210, v136, v137\n\t"
      "v_max3_u32 v210, v210, v138, v139\n\t"
      "v_max3_u32 v210, v210, v140, v141\n\t"
      "v_max3_u32 v210, v210, v142, v143\n\t"
      "v_max3_u32 v210, v210, v144, v145\n\t"
      "v_cmp_eq_u32 vcc, -1, v210\n\t"
      "s_cbranch_vccz Lvo_vres\n\t"
      "s_mov_b32 s97, s98\n\t"
      "s_mov_b32 s79, s76\n\t"
      "s_add_u32 s97, s97, 1\n\t"
      "s_and_b32 s76, s97, 31\n\t"
      "s_cmp_lg_u32 s76, 31\n\t"
      "s_cbranch_scc1 Lvo_sp9\n\t"
      "s_cmp_lt_u32 s97, 0x8000\n\t"
      "s_cbranch_scc1 Lvo_sp9b\n\t"
      "v_mov_b32 v210, 1\n\t"
      "buffer_store_dword v210, off, s[36:39], s67 sc1\n\t"
      "s_branch Lvo_vdead\n\t"
      "Lvo_sp9b:\n\t"
      "buffer_load_dword v210, off, s[36:39], s67 sc1\n\t"
      "s_waitcnt vmcnt(0)\n\t"
      "v_readfirstlane_b32 s76, v210\n\t"
      "s_cmp_lg_u32 s76, 0\n\t"
      "s_cbranch_scc1 Lvo_vdead\n\t"
      "Lvo_sp9:\n\t"
      "s_mov_b32 s98, s97\n\t"
      "v_mov_b64 v[178:179], 0\n\t"
      "v_mov_b64 v[180:181], 0\n\t"
      "v_mov_b64 v[182:183], 0\n\t"
      "v_mov_b64 v[184:185], 0\n\t"
      "v_mov_b64 v[186:187], 0\n\t"
      "v_mov_b64 v[188:189], 0\n\t"
      "v_mov_b64 v[190:191], 0\n\t"
      "v_mov_b64 v[192:193], 0\n\t"
      "s_add_u32 s58, s58, s68\n\t"
      "s_add_u32 s73, s73, 1\n\t"
      "s_waitcnt vmcnt(0)\n\t"
      "s_branch Lvo_shift\n\t"
      "Lvo_vdead:\n\t"
      "s_mov_b32 s76, s79\n\t"
      "s_branch Lvo_vres\n\t"
      "Lvo_one:\n\t"
      "s_mov_b32 s97, 0\n\t"
      "s_cmp_eq_u32 s71, 0\n\t"
      "s_cbranch_scc1 Lvo_one1\n\t"
      "s_lshr_b32 s58, s71, 20\n\t"
      "s_add_u32 s58, s58, s63\n\t"
      "s_min_u32 s58, s58, -2\n\t"
      "v_mov_b32 v210, s58\n\t"
      "v_mov_b32 v211, 0\n\t"
      "s_and_b32 s76, s71, 1023\n\t"
      "v_mov_b32 v212, s76\n\t"
      "s_bfe_u32 s76, s71, 0xa000a\n\t"
      "v_mov_b32 v213, s76\n\t"
      "s_mov_b64 exec, 1\n\t"
      "buffer_store_dwordx4 v[210:213], off, s[36:39], s84 sc1\n\t"
      "s_mov_b64 exec, -1\n\t"
      "s_branch Lvo_one2\n\t"
      "Lvo_one1:\n\t"
      "buffer_load_dword v210, off, s[36:39], s84 sc1\n\t"
      "s_waitcnt vmcnt(0)\n\t"
      "v_readfirstlane_b32 s58, v210\n\t"
      "s_cmp_lg_u32 s58, -1\n\t"
      "s_cbranch_scc1 Lvo_one2\n\t"
      "s_add_u32 s97, s97, 1\n\t"
      "s_and_b32 s76, s97, 31\n\t"
      "s_cmp_lg_u32 s76, 31\n\t"
      "s_cbranch_scc1 Lvo_sp10\n\t"
      "s_cmp_lt_u32 s97, 0x8000\n\t"
      "s_cbranch_scc1 Lvo_sp10b\n\t"
      "v_mov_b32 v210, 1\n\t"
      "buffer_store_dword v210, off, s[36:39], s67 sc1\n\t"
      "s_branch Lvo_one2\n\t"
      "Lvo_sp10b:\n\t"
      "buffer_load_dword v210, off, s[36:39], s67 sc1\n\t"
      "s_waitcnt vmcnt(0)\n\t"
      "v_readfirstlane_b32 s76, v210\n\t"
      "s_cmp_lg_u32 s76, 0\n\t"
      "s_cbranch_scc1 Lvo_one2\n\t"
      "Lvo_sp10:\n\t"
      "s_sleep 2\n\t"
      "s_branch Lvo_one1\n\t"
      "Lvo_one2:\n\t"
      "s_add_u32 s79, s58, s65\n\t"
      "s_branch Lvo_cwv\n\t"
      "Lvo_rec:\n\t"
      "s_mov_b32 s97, 0\n\t"
      "Lvo_rec1:\n\t"
      "s_sleep 2\n\t"
      "buffer_load_dwordx2 v[220:221], off, s[36:39], s84 offset:8 sc1\n\t"
      "s_waitcnt vmcnt(0)\n\t"
      "v_readfirstlane_b32 s93, v220\n\t"
      "v_readfirstlane_b32 s94, v221\n\t"
      "s_max_u32 s76, s93, s94\n\t"
      "s_cmp_lg_u32 s76, -1\n\t"
      "s_cbranch_scc1 Lvo_rec_r\n\t"
      "s_add_u32 s97, s97, 1\n\t"
      "s_and_b32 s76, s97, 31\n\t"
      "s_cmp_lg_u32 s76, 31\n\t"
      "s_cbranch_scc1 Lvo_sp11\n\t"
      "s_cmp_lt_u32 s97, 0x8000\n\t"
      "s_cbranch_scc1 Lvo_sp11b\n\t"
      "v_mov_b32 v210, 1\n\t"
      "buffer_store_dword v210, off, s[36:39], s67 sc1\n\t"
      "s_branch Lvo_rec_r\n\t"
      "Lvo_sp11b:\n\t"
      "buffer_load_dword v210, off, s[36:39], s67 sc1\n\t"
      "s_waitcnt vmcnt(0)\n\t"
      "v_readfirstlane_b32 s76, v210\n\t"
      "s_cmp_lg_u32 s76, 0\n\t"
      "s_cbranch_scc1 Lvo_rec_r\n\t"
      "Lvo_sp11:\n\t"
      "s_branch Lvo_rec1\n\t"
      "Lvo_end:\n\t"
      "s_waitcnt vmcnt(0) lgkmcnt(0)\n\t"
      :
      : "{a[0:31]}"(WA[0]), "{a[32:63]}"(WA[1]), "{a[64:95]}"(WA[2]), "{a[96:127]}"(WA[3]), "{a[128:159]}"(WA[4]), "{a[160:191]}"(WA[5]), "{a[192:223]}"(WA[6]), "{a[224:255]}"(WA[7]),
        "{v[2:33]}"(WV[0]), "{v[34:65]}"(WV[1]), "{v[66:97]}"(WV[2]), "{v[98:129]}"(WV[3]), "{v[194:209]}"(C), "{s[36:39]}"(rs_o), "{s[40:55]}"(S), "{s[64:71]}"(Q)
      : R8(v, 13), R8(v, 14), R8(v, 15), R8(v, 16), R8(v, 17), R8(v, 18), "v190", "v191", "v192", "v193", "v210", "v211", "v212", "v213", "v214", "v215", "v216", "v217", "v218", "v219", "v220", "v221", "v222", "v223", "v224", "v225",
        "s56", "s57", "s58", "s59", "s60", "s61", "s62", "s63", "s72", "s73", "s74", "s75", "s76", "s77", "s78", "s79", R8(s, 8), R8(s, 9), "vcc", "scc", "memory"
      );
  }
  leave_epoch(P, wvn);
}
__device__ __attribute__((used, noinline)) void code_end_marker() { asm volatile("Lfused_end:"); }

extern "C" void kernel_launch(void* const* d_in, const int* in_sizes, int n_in,
                              void* d_out, int out_size, void* d_ws, size_t ws_size,
                              hipStream_t stream) {
  const u16* A_x = (const u16*)d_in[0];
  const u16* A_mem0 = (const u16*)d_in[1];
  const u16* A_Wq = (const u16*)d_in[2];
  const u16* A_Wk = (const u16*)d_in[3];
  const u16* A_Wv = (const u16*)d_in[4];
  const u16* A_Wo = (const u16*)d_in[5];
  const u16* B_x = (const u16*)d_in[7];
  const u16* B_wqkv = (const u16*)d_in[8];
  const u16* B_wo = (const u16*)d_in[9];
  u16* out = (u16*)d_out;

  char* ws = (char*)d_ws;
  size_t off = 0;
  auto alloc = [&](size_t bytes) { char* p = ws + off; off += (bytes + 255) & ~(size_t)255; return p; };
  off = (size_t)(-(long long)(size_t)ws) & ((1u << 21) - 1);
  char* obuf = alloc(CBASE);
  Ctl* ctl = (Ctl*)alloc(sizeof(Ctl));
  Hdr* hdr = (Hdr*)alloc(sizeof(Hdr));
  unsigned long long* prof = (unsigned long long*)alloc(96 * 4 * 72 * 8 + 8 * 256 * 16 + 64 * 384 * 8);

  FParams cp;
  cp.Ax = A_x; cp.Bx = B_x; cp.Wq = A_Wq; cp.Wk = A_Wk; cp.Wv = A_Wv; cp.Wo = A_Wo; cp.wqkvB = B_wqkv; cp.woB = B_wo;
  cp.obuf = (u16*)obuf; cp.hdr = hdr; cp.mem0 = A_mem0; cp.out = out;
  cp.ctl = ctl; cp.prof = prof;
  cp.dq = DQ; cp.dq2 = DQ2; cp.dmv = DMV; cp.dov = DOV; cp.gain = GAIN; cp.inc = MINC; cp.dec = MDEC; cp.rec = RECOV; cp.stag = STAG; cp.sh1 = SH1; cp.sh2 = SH2; cp.sh3 = SH3; cp.dv = DVV; cp.pad = RANKM; cp.stag2 = STAG2; cp.pad2 = FLAT;
  k_fused<<<256, 256, 0, stream>>>(cp);
}
```
